```python
import torch

def _softmax(x, axis=-1):
    return torch.softmax(x, dim=axis)
def _gelu(x):
    return torch.nn.functional.gelu(x, approximate="tanh")
def _silu(x):
    return torch.nn.functional.silu(x)
def _sigmoid(x):
    return torch.sigmoid(x)
def _layernorm(x, axis=-1, eps=1e-5):
    m = x.mean(dim=axis, keepdim=True)
    v = x.var(dim=axis, keepdim=True, unbiased=False)
    return (x - m) / torch.sqrt(v + eps)
def _scatter_add(tgt, idx, src, axis=0):
    return tgt.clone().index_add_(axis, idx, src)
def _segment_sum(src, seg, n):
    out = torch.zeros((n,) + src.shape[1:], dtype=src.dtype)
    return out.index_add_(0, seg, src)
def _pad(x, pw):
    flat = tuple(v for pair in reversed(pw) for v in pair)
    return torch.nn.functional.pad(x, flat)

kernel_name = 'standalone__kv_cache_quant_int4_int2_kv_with_per_channel_or_per_token_scales__prompt'

def setup_inputs():

    _w = (lambda a: (lambda t: t.to(torch.bfloat16) if t.is_floating_point() else t)(torch.as_tensor(a)))
    B = 1
    Sq = 2048
    Skv = 2048
    Hq = 32
    Hkv = 32
    Dh = 192
    bits = 3
    G = 32
    if G <= 0 or G >= Skv:
        G = max(1, Skv // 2)
    Skv = (Skv // G) * G
    ngk = Skv // G
    qmax = (1 << bits) - 1
    return {
        "q": _w((lambda *s: torch.randn(*s, dtype=torch.bfloat16) * 1.6)(B, Hq, Sq, Dh)),
        "qk": _w(torch.randint(0, qmax + 1, (B, Hkv, Skv, Dh)).to(torch.int32)),
        "qv": _w(torch.randint(0, qmax + 1, (B, Hkv, Skv, Dh)).to(torch.int32)),
        "k_scales": _w(torch.abs((lambda *s: torch.randn(*s, dtype=torch.bfloat16) * 1.6)(B, Hkv, ngk, Dh)) * 0.05 + 0.02),
        "k_zeros": _w((lambda *s: torch.randn(*s, dtype=torch.bfloat16) * 1.6)(B, Hkv, ngk, Dh) + float(qmax) / 2.0),
        "v_scales": _w(torch.abs((lambda *s: torch.randn(*s, dtype=torch.bfloat16) * 0.05)(B, Hkv, Skv, 1)) * 0.05 + 0.02),
        "v_zeros": _w((lambda *s: torch.randn(*s, dtype=torch.bfloat16) * 0.05)(B, Hkv, Skv, 1) + float(qmax) / 2.0),
    }

def reference(q, qk, qv, k_scales, k_zeros, v_scales, v_zeros):
    B, Hq, Sq, Dh = q.shape
    Hkv, Skv = qk.shape[1], qk.shape[2]
    ngk = k_scales.shape[2]
    G = Skv // ngk
    ks = (lambda x, n, axis: torch.repeat_interleave(x, n, dim=axis))(k_scales, G, axis=2)
    kz = (lambda x, n, axis: torch.repeat_interleave(x, n, dim=axis))(k_zeros, G, axis=2)
    k = ((lambda x, dt: x.to(dt))(qk, q.dtype) - kz) * ks
    v = ((lambda x, dt: x.to(dt))(qv, q.dtype) - v_zeros) * v_scales
    rep = Hq // Hkv
    if rep > 1:
        k = (lambda x, n, axis: torch.repeat_interleave(x, n, dim=axis))(k, rep, axis=1)
        v = (lambda x, n, axis: torch.repeat_interleave(x, n, dim=axis))(v, rep, axis=1)
    scale = float(Dh) ** -0.5
    logits = torch.einsum("bhqd,bhkd->bhqk", q, k) * scale
    p = _softmax(logits, axis=-1)
    o = torch.einsum("bhqk,bhkd->bhqd", p, v)
    return o
```

```cpp
#include <hip/hip_runtime.h>
#include <hip/hip_bf16.h>
#include <stdint.h>
typedef __attribute__((ext_vector_type(8))) short s16x8;
typedef __attribute__((ext_vector_type(8))) __bf16 bf16x8;
typedef __attribute__((ext_vector_type(4))) float f32x4;
typedef __attribute__((ext_vector_type(4))) int i32x4;
typedef __attribute__((ext_vector_type(4))) unsigned u32x4;
typedef __attribute__((ext_vector_type(2))) float f32x2;
typedef __attribute__((ext_vector_type(2))) __bf16 bf16x2;
constexpr int H = 32, S = 2048, D = 192;
__device__ __forceinline__ unsigned cvtpk(float lo, float hi) { f32x2 v = {lo, hi}; bf16x2 b = __builtin_convertvector(v, bf16x2); return __builtin_bit_cast(unsigned, b); }
__device__ __forceinline__ float bflo(unsigned w) { return __uint_as_float(w << 16); }
__device__ __forceinline__ float bfhi(unsigned w) { return __uint_as_float(w & 0xffff0000u); }
__device__ __forceinline__ float rbf(float x) { return bflo(cvtpk(x, 0.f)); }
__device__ __forceinline__ float bf2f(unsigned short x) { return __uint_as_float(((unsigned)x) << 16); }

__global__ void dequant(const int* qk, const int* qv, const unsigned short* ks, const unsigned short* kz, const unsigned short* vs, const unsigned short* vz,
                        unsigned short* Kf, unsigned short* Vf) {
  int wid = (blockIdx.x * blockDim.x + threadIdx.x) >> 6, l = threadIdx.x & 63, i = l & 15, g = l >> 4;
  const int NK = H * 128 * 6;
  if (wid < NK) {
    int h = wid / 768, rem = wid % 768, kb = rem / 6, ds = rem % 6;
    int key = kb * 16 + i, d0 = 32 * ds + 8 * g;
    unsigned w[4];
    for (int j = 0; j < 8; j += 2) {
      float x[2];
      for (int e = 0; e < 2; ++e) {
        int d = d0 + j + e;
        float q = rbf((float)qk[((long)h * S + key) * D + d]);
        float z = bf2f(kz[((long)h * 64 + key / 32) * D + d]), s = bf2f(ks[((long)h * 64 + key / 32) * D + d]);
        x[e] = rbf(q - z) * s;
      }
      w[j / 2] = cvtpk(x[0], x[1]);
    }
    *(u32x4*)(Kf + ((long)wid * 64 + l) * 8) = u32x4{w[0], w[1], w[2], w[3]};
  } else {
    wid -= NK;
    int h = wid / 768, rem = wid % 768, kst = rem / 12, db = rem % 12;
    int sb = db >> 2, c = db & 3, gp = i >> 2, rp = i & 3;
    int d = 64 * sb + 4 * (8 * (rp >> 1) + 2 * gp + (rp & 1)) + c;
    unsigned w[4];
    for (int j = 0; j < 8; j += 2) {
      float x[2];
      for (int e = 0; e < 2; ++e) {
        int jj = j + e;
        int key = 32 * kst + 16 * (jj >> 2) + 4 * g + (jj & 3);
        float q = rbf((float)qv[((long)h * S + key) * D + d]);
        float z = bf2f(vz[(long)h * S + key]), s = bf2f(vs[(long)h * S + key]);
        x[e] = rbf(q - z) * s;
      }
      w[j / 2] = cvtpk(x[0], x[1]);
    }
    *(u32x4*)(Vf + ((long)wid * 64 + l) * 8) = u32x4{w[0], w[1], w[2], w[3]};
  }
}
__global__ __launch_bounds__(256) void attn(const unsigned short* Q, const unsigned short* Kf, const unsigned short* Vf, unsigned short* O) {
  int wid = (blockIdx.x * blockDim.x + threadIdx.x) >> 6, l = threadIdx.x & 63, i = l & 15, g = l >> 4;
  int h = wid / 128, q0 = (wid % 128) * 16;
  bf16x8 q[6];
  for (int ds = 0; ds < 6; ++ds) q[ds] = *(const bf16x8*)(Q + ((long)h * S + q0 + i) * D + 32 * ds + 8 * g);
  f32x4 o[12];
  for (int db = 0; db < 12; ++db) o[db] = f32x4{0, 0, 0, 0};
  float m = -1e30f, lsum = 0.f;
  const float C = 0.07216878364870322f * 1.4426950408889634f;
  for (int ks = 0; ks < 64; ++ks) {
    f32x4 s0 = {0, 0, 0, 0}, s1 = {0, 0, 0, 0};
    for (int ds = 0; ds < 6; ++ds) {
      bf16x8 k0 = *(const bf16x8*)(Kf + (((long)h * 128 + 2 * ks) * 6 + ds) * 512 + l * 8);
      bf16x8 k1 = *(const bf16x8*)(Kf + (((long)h * 128 + 2 * ks + 1) * 6 + ds) * 512 + l * 8);
      s0 = __builtin_amdgcn_mfma_f32_16x16x32_bf16(k0, q[ds], s0, 0, 0, 0);
      s1 = __builtin_amdgcn_mfma_f32_16x16x32_bf16(k1, q[ds], s1, 0, 0, 0);
    }
    float mx = fmaxf(fmaxf(fmaxf(s0[0], s0[1]), fmaxf(s0[2], s0[3])), fmaxf(fmaxf(s1[0], s1[1]), fmaxf(s1[2], s1[3])));
    mx = fmaxf(mx, __shfl_xor(mx, 16)); mx = fmaxf(mx, __shfl_xor(mx, 32));
    float mn = fmaxf(m, mx), al = exp2f((m - mn) * C); m = mn;
    float p[8];
    for (int r = 0; r < 4; ++r) { p[r] = exp2f((s0[r] - mn) * C); p[4 + r] = exp2f((s1[r] - mn) * C); }
    float ps = 0; for (int r = 0; r < 8; ++r) ps += p[r];
    lsum = lsum * al + ps;
    u32x4 pw = {cvtpk(p[0], p[1]), cvtpk(p[2], p[3]), cvtpk(p[4], p[5]), cvtpk(p[6], p[7])};
    bf16x8 pb = __builtin_bit_cast(bf16x8, pw);
    for (int db = 0; db < 12; ++db) {
      bf16x8 v = *(const bf16x8*)(Vf + (((long)h * 64 + ks) * 12 + db) * 512 + l * 8);
      o[db] *= al;
      o[db] = __builtin_amdgcn_mfma_f32_16x16x32_bf16(v, pb, o[db], 0, 0, 0);
    }
  }
  lsum += __shfl_xor(lsum, 16); lsum += __shfl_xor(lsum, 32);
  float r = 1.f / lsum;
  for (int sb = 0; sb < 3; ++sb)
    for (int hf = 0; hf < 2; ++hf) {
      u32x4 w = {cvtpk(o[4 * sb + 0][2 * hf] * r, o[4 * sb + 1][2 * hf] * r), cvtpk(o[4 * sb + 2][2 * hf] * r, o[4 * sb + 3][2 * hf] * r),
                 cvtpk(o[4 * sb + 0][2 * hf + 1] * r, o[4 * sb + 1][2 * hf + 1] * r), cvtpk(o[4 * sb + 2][2 * hf + 1] * r, o[4 * sb + 3][2 * hf + 1] * r)};
      *(u32x4*)(O + ((long)h * S + q0 + i) * D + 64 * sb + 32 * hf + 8 * g) = w;
    }
}
extern "C" void kernel_launch(void* const* d_in, const int* in_sizes, int n_in, void* d_out, int out_size, void* d_ws, size_t ws_size, hipStream_t stream) {
  unsigned short* Kf = (unsigned short*)d_ws; unsigned short* Vf = Kf + (size_t)H * S * D;
  dequant<<<(2 * H * 768) / 4, 256, 0, stream>>>((const int*)d_in[1], (const int*)d_in[2], (const unsigned short*)d_in[3], (const unsigned short*)d_in[4],
                                                 (const unsigned short*)d_in[5], (const unsigned short*)d_in[6], Kf, Vf);
  attn<<<H * 128 / 4, 256, 0, stream>>>((const unsigned short*)d_in[0], Kf, Vf, (unsigned short*)d_out);
}
```

```cpp
#include <hip/hip_runtime.h>
#include <hip/hip_bf16.h>
#include <stdint.h>
typedef __attribute__((ext_vector_type(8))) __bf16 bf16x8;
typedef __attribute__((ext_vector_type(4))) float f32x4;
typedef __attribute__((ext_vector_type(4))) unsigned u32x4;
typedef __attribute__((ext_vector_type(2))) unsigned u32x2;
typedef __attribute__((ext_vector_type(2))) float f32x2;
typedef __attribute__((ext_vector_type(2))) __bf16 bf16x2;
constexpr int H = 32, S = 2048, D = 192;
constexpr size_t KV_BYTES = (size_t)H * S * D * 2;
__device__ __forceinline__ unsigned cvtpk(float lo, float hi) { f32x2 v = {lo, hi}; bf16x2 b = __builtin_convertvector(v, bf16x2); return __builtin_bit_cast(unsigned, b); }

__device__ __forceinline__ void slow_rows(const char* Qw, const char* Kh, const char* Vh, char* Ow) {
  const unsigned l = __builtin_amdgcn_mbcnt_hi(-1, __builtin_amdgcn_mbcnt_lo(-1, 0)), i = l & 15, g = l >> 4;
  const float C = 0.07216878364870322f * 1.4426950408889634f;
  for (int qb = 0; qb < 4; ++qb) {
    bf16x8 q[6];
#pragma unroll
    for (int ds = 0; ds < 6; ++ds) {
      const char* p = Qw + (16 * qb + i) * 384 + 128 * (ds >> 1) + 8 * (4 * (ds & 1) + g);
      const u32x2 lo = *(const u32x2*)p, hi = *(const u32x2*)(p + 64);
      q[ds] = __builtin_bit_cast(bf16x8, u32x4{lo[0], lo[1], hi[0], hi[1]});
    }
    f32x4 o[12];
#pragma unroll
    for (int db = 0; db < 12; ++db) o[db] = f32x4{0, 0, 0, 0};
    float m = -INFINITY, lsum = 0.f;
    for (int ks = 0; ks < 64; ++ks) {
      f32x4 s0 = {0, 0, 0, 0}, s1 = {0, 0, 0, 0};
#pragma unroll
      for (int ds = 0; ds < 6; ++ds) {
        const char* kp = Kh + (size_t)(ks * 6 + (ds >> 1)) * 2048 + 128 * i + 16 * ((4 * (ds & 1) + g) ^ (i & 6));
        s0 = __builtin_amdgcn_mfma_f32_16x16x32_bf16(*(const bf16x8*)kp, q[ds], s0, 0, 0, 0);
        s1 = __builtin_amdgcn_mfma_f32_16x16x32_bf16(*(const bf16x8*)(kp + 6144), q[ds], s1, 0, 0, 0);
      }
      float mx = fmaxf(fmaxf(fmaxf(s0[0], s0[1]), fmaxf(s0[2], s0[3])), fmaxf(fmaxf(s1[0], s1[1]), fmaxf(s1[2], s1[3])));
      mx = fmaxf(mx, __shfl_xor(mx, 16)); mx = fmaxf(mx, __shfl_xor(mx, 32));
      const float mn = fmaxf(m, mx), al = __builtin_amdgcn_exp2f((m - mn) * C); m = mn;
      float p[8], ps = 0.f;
#pragma unroll
      for (int r = 0; r < 4; ++r) { p[r] = __builtin_amdgcn_exp2f((s0[r] - mn) * C); p[4 + r] = __builtin_amdgcn_exp2f((s1[r] - mn) * C); }
#pragma unroll
      for (int r = 0; r < 8; ++r) ps += p[r];
      lsum = lsum * al + ps;
      const u32x4 pw = {cvtpk(p[0], p[1]), cvtpk(p[2], p[3]), cvtpk(p[4], p[5]), cvtpk(p[6], p[7])};
#pragma unroll
      for (int db = 0; db < 12; ++db) {
        o[db] *= al;
        o[db] = __builtin_amdgcn_mfma_f32_16x16x32_bf16(*(const bf16x8*)(Vh + (size_t)(ks * 12 + db) * 1024 + l * 16), __builtin_bit_cast(bf16x8, pw), o[db], 0, 0, 0);
      }
    }
    lsum += __shfl_xor(lsum, 16); lsum += __shfl_xor(lsum, 32);
    const float r = 1.f / lsum;
#pragma unroll
    for (int sb = 0; sb < 6; ++sb) {
      const u32x4 w = {cvtpk(o[2 * sb][0] * r, o[2 * sb + 1][0] * r), cvtpk(o[2 * sb][1] * r, o[2 * sb + 1][1] * r),
                       cvtpk(o[2 * sb][2] * r, o[2 * sb + 1][2] * r), cvtpk(o[2 * sb][3] * r, o[2 * sb + 1][3] * r)};
      *(u32x4*)(Ow + (16 * qb + i) * 384 + 64 * sb + 16 * g) = w;
    }
  }
}

__global__ __launch_bounds__(256, 1) void kvq_attn(const char* __restrict__ Q, const char* __restrict__ QK, const char* __restrict__ QV, const char* __restrict__ KS,
                                                   const char* __restrict__ KZ, const char* __restrict__ VS, const char* __restrict__ VZ, char* WS, char* O) {
  __shared__ __attribute__((aligned(16))) char lds[163840];
  const unsigned b = blockIdx.x, h = (b & 7) * 4 + (b >> 6), j = (b >> 3) & 7;
  const unsigned w = __builtin_amdgcn_readfirstlane(threadIdx.x >> 6);
  const size_t qoff = ((size_t)h * S + j * 256 + w * 64) * (D * 2);
  const char* qp = Q + qoff; char* op = O + qoff;
  char* kw = WS + (size_t)h * (S * D * 2); char* vw = kw + KV_BYTES;
  unsigned bad;
  asm volatile("" :: "v"((unsigned)(size_t)lds));
  asm volatile(
    "v_mbcnt_lo_u32_b32 v150, -1, 0\n"
    "v_mbcnt_hi_u32_b32 v150, -1, v150\n"
    "s_mov_b64 s[64:65], %9\n"
    "s_and_b32 s65, s65, 0xffff\n"
    "s_mov_b32 s66, 4096\n"
    "s_mov_b32 s67, 0x20000\n"
    "buffer_load_dwordx2 v[252:253], off, s[64:67], 0 offset:3072 sc1\n"
    "v_and_b32 v151, 15, v150\n"
    "v_lshrrev_b32 v152, 4, v150\n"
    "v_lshrrev_b32 v153, 3, v150\n"
    "v_and_b32 v154, 6, v153\n"
    "v_and_b32 v155, 7, v150\n"
    "v_xor_b32 v154, v154, v155\n"
    "v_lshlrev_b32 v179, 3, v154\n"
    "v_lshrrev_b32 v155, 5, v150\n"
    "v_and_b32 v153, 3, v153\n"
    "v_lshl_add_u32 v153, v155, 3, v153\n"
    "v_mul_u32_u24 v153, 768, v153\n"
    "v_lshl_add_u32 v178, v154, 4, v153\n"
    "v_lshlrev_b32 v181, 4, v152\n"
    "v_mul_u32_u24 v154, 6144, v152\n"
    "v_lshl_add_u32 v180, v151, 3, v154\n"
    "s_mov_b64 s[40:41], %2\n"
    "s_and_b32 s41, s41, 0xffff\n"
    "s_mov_b32 s42, 1572864\n"
    "s_mov_b32 s43, 0x20000\n"
    "s_mov_b64 s[44:45], %4\n"
    "s_and_b32 s45, s45, 0xffff\n"
    "s_mov_b32 s46, 24576\n"
    "s_mov_b32 s47, 0x20000\n"
    "s_mov_b64 s[48:49], %5\n"
    "s_and_b32 s49, s49, 0xffff\n"
    "s_mov_b32 s50, 24576\n"
    "s_mov_b32 s51, 0x20000\n"
    "s_mov_b64 s[28:29], %3\n"
    "s_and_b32 s29, s29, 0xffff\n"
    "s_mov_b32 s30, 1572864\n"
    "s_mov_b32 s31, 0x20000\n"
    "s_mov_b64 s[36:37], %6\n"
    "s_and_b32 s37, s37, 0xffff\n"
    "s_mov_b32 s38, 4096\n"
    "s_mov_b32 s39, 0x20000\n"
    "s_mov_b64 s[52:53], %7\n"
    "s_and_b32 s53, s53, 0xffff\n"
    "s_mov_b32 s54, 4096\n"
    "s_mov_b32 s55, 0x20000\n"
    "s_lshr_b32 s79, %11, 1\n"
    "s_and_b32 s97, %11, 1\n"
    "s_mov_b32 s75, 0\n"
    "s_add_u32 s89, %12, 0\n"
    "s_mul_i32 s88, s89, 11\n"
    "s_lshr_b32 s88, s88, 5\n"
    "s_mul_i32 s90, s88, 3\n"
    "s_sub_u32 s89, s89, s90\n"
    "s_lshr_b32 s90, 0xd4, s88\n"
    "s_and_b32 s90, s90, 1\n"
    "s_lshl_b32 s90, s90, 0\n"
    "s_or_b32 s75, s75, s90\n"
    "s_lshl_b32 s88, s88, 1\n"
    "s_lshr_b32 s88, 0xed84, s88\n"
    "s_and_b32 s88, s88, 3\n"
    "s_mul_i32 s72, s88, 12\n"
    "s_mul_i32 s90, %11, 3\n"
    "s_add_u32 s72, s72, s90\n"
    "s_add_u32 s72, s72, s89\n"
    "s_lshl_b32 s72, s72, 11\n"
    "s_lshl_b32 s88, s88, 1\n"
    "s_add_u32 s88, s88, s79\n"
    "s_mul_i32 s82, s88, 24576\n"
    "s_bitcmp1_b32 s75, 0\n"
    "s_cbranch_scc1 FVL0%=\n"
    "s_mul_i32 s90, s97, 12\n"
    "s_add_u32 s90, s90, s89\n"
    "s_lshl_b32 s90, s90, 8\n"
    "s_add_u32 s82, s82, s90\n"
    "s_add_u32 s83, s82, 12288\n"
    "s_mul_i32 s84, s88, 3\n"
    "s_add_u32 s84, s84, s89\n"
    "s_lshl_b32 s84, s84, 7\n"
    "buffer_load_dwordx4 v[72:75], v178, s[40:43], s82 offen offset:0 nt\n"
    "buffer_load_dwordx4 v[76:79], v178, s[40:43], s82 offen offset:128 nt\n"
    "buffer_load_dwordx4 v[80:83], v178, s[40:43], s83 offen offset:0 nt\n"
    "buffer_load_dwordx4 v[84:87], v178, s[40:43], s83 offen offset:128 nt\n"
    "buffer_load_dwordx2 v[88:89], v179, s[44:47], s84 offen offset:0 nt\n"
    "buffer_load_dwordx2 v[90:91], v179, s[44:47], s84 offen offset:64 nt\n"
    "buffer_load_dwordx2 v[92:93], v179, s[48:51], s84 offen offset:0 nt\n"
    "buffer_load_dwordx2 v[94:95], v179, s[48:51], s84 offen offset:64 nt\n"
    "buffer_load_dwordx2 v[92:93], v179, s[48:51], s84 offen offset:0 nt\n"
    "buffer_load_dwordx2 v[94:95], v179, s[48:51], s84 offen offset:64 nt\n"
    "s_branch FJL0%=\n"
    "FVL0%=:\n"
    "s_mul_i32 s90, s97, 3\n"
    "s_add_u32 s90, s90, s89\n"
    "s_lshl_b32 s90, s90, 7\n"
    "s_add_u32 s82, s82, s90\n"
    "s_add_u32 s83, s82, 3072\n"
    "s_lshl_b32 s84, s88, 6\n"
    "buffer_load_dwordx2 v[72:73], v180, s[28:31], s82 offen offset:0 nt\n"
    "buffer_load_dwordx2 v[74:75], v180, s[28:31], s82 offen offset:768 nt\n"
    "buffer_load_dwordx2 v[76:77], v180, s[28:31], s82 offen offset:1536 nt\n"
    "buffer_load_dwordx2 v[78:79], v180, s[28:31], s82 offen offset:2304 nt\n"
    "buffer_load_dwordx2 v[80:81], v180, s[28:31], s83 offen offset:0 nt\n"
    "buffer_load_dwordx2 v[82:83], v180, s[28:31], s83 offen offset:768 nt\n"
    "buffer_load_dwordx2 v[84:85], v180, s[28:31], s83 offen offset:1536 nt\n"
    "buffer_load_dwordx2 v[86:87], v180, s[28:31], s83 offen offset:2304 nt\n"
    "buffer_load_dwordx4 v[88:91], v181, s[36:39], s84 offen nt\n"
    "buffer_load_dwordx4 v[92:95], v181, s[52:55], s84 offen nt\n"
    "FJL0%=:\n"
    "s_add_u32 s89, %12, 8\n"
    "s_mul_i32 s88, s89, 11\n"
    "s_lshr_b32 s88, s88, 5\n"
    "s_mul_i32 s90, s88, 3\n"
    "s_sub_u32 s89, s89, s90\n"
    "s_lshr_b32 s90, 0xd4, s88\n"
    "s_and_b32 s90, s90, 1\n"
    "s_lshl_b32 s90, s90, 1\n"
    "s_or_b32 s75, s75, s90\n"
    "s_lshl_b32 s88, s88, 1\n"
    "s_lshr_b32 s88, 0xed84, s88\n"
    "s_and_b32 s88, s88, 3\n"
    "s_mul_i32 s73, s88, 12\n"
    "s_mul_i32 s90, %11, 3\n"
    "s_add_u32 s73, s73, s90\n"
    "s_add_u32 s73, s73, s89\n"
    "s_lshl_b32 s73, s73, 11\n"
    "s_lshl_b32 s88, s88, 1\n"
    "s_add_u32 s88, s88, s79\n"
    "s_mul_i32 s82, s88, 24576\n"
    "s_bitcmp1_b32 s75, 1\n"
    "s_cbranch_scc1 FVL1%=\n"
    "s_mul_i32 s90, s97, 12\n"
    "s_add_u32 s90, s90, s89\n"
    "s_lshl_b32 s90, s90, 8\n"
    "s_add_u32 s82, s82, s90\n"
    "s_add_u32 s83, s82, 12288\n"
    "s_mul_i32 s84, s88, 3\n"
    "s_add_u32 s84, s84, s89\n"
    "s_lshl_b32 s84, s84, 7\n"
    "buffer_load_dwordx4 v[96:99], v178, s[40:43], s82 offen offset:0 nt\n"
    "buffer_load_dwordx4 v[100:103], v178, s[40:43], s82 offen offset:128 nt\n"
    "buffer_load_dwordx4 v[104:107], v178, s[40:43], s83 offen offset:0 nt\n"
    "buffer_load_dwordx4 v[108:111], v178, s[40:43], s83 offen offset:128 nt\n"
    "buffer_load_dwordx2 v[112:113], v179, s[44:47], s84 offen offset:0 nt\n"
    "buffer_load_dwordx2 v[114:115], v179, s[44:47], s84 offen offset:64 nt\n"
    "buffer_load_dwordx2 v[116:117], v179, s[48:51], s84 offen offset:0 nt\n"
    "buffer_load_dwordx2 v[118:119], v179, s[48:51], s84 offen offset:64 nt\n"
    "buffer_load_dwordx2 v[116:117], v179, s[48:51], s84 offen offset:0 nt\n"
    "buffer_load_dwordx2 v[118:119], v179, s[48:51], s84 offen offset:64 nt\n"
    "s_branch FJL1%=\n"
    "FVL1%=:\n"
    "s_mul_i32 s90, s97, 3\n"
    "s_add_u32 s90, s90, s89\n"
    "s_lshl_b32 s90, s90, 7\n"
    "s_add_u32 s82, s82, s90\n"
    "s_add_u32 s83, s82, 3072\n"
    "s_lshl_b32 s84, s88, 6\n"
    "buffer_load_dwordx2 v[96:97], v180, s[28:31], s82 offen offset:0 nt\n"
    "buffer_load_dwordx2 v[98:99], v180, s[28:31], s82 offen offset:768 nt\n"
    "buffer_load_dwordx2 v[100:101], v180, s[28:31], s82 offen offset:1536 nt\n"
    "buffer_load_dwordx2 v[102:103], v180, s[28:31], s82 offen offset:2304 nt\n"
    "buffer_load_dwordx2 v[104:105], v180, s[28:31], s83 offen offset:0 nt\n"
    "buffer_load_dwordx2 v[106:107], v180, s[28:31], s83 offen offset:768 nt\n"
    "buffer_load_dwordx2 v[108:109], v180, s[28:31], s83 offen offset:1536 nt\n"
    "buffer_load_dwordx2 v[110:111], v180, s[28:31], s83 offen offset:2304 nt\n"
    "buffer_load_dwordx4 v[112:115], v181, s[36:39], s84 offen nt\n"
    "buffer_load_dwordx4 v[116:119], v181, s[52:55], s84 offen nt\n"
    "FJL1%=:\n"
    "v_lshlrev_b32 v149, 4, v150\n"
    "s_mov_b64 s[68:69], %1\n"
    "s_and_b32 s69, s69, 0xffff\n"
    "s_mov_b32 s70, 24576\n"
    "s_mov_b32 s71, 0x20000\n"
    "s_mov_b32 s76, 49152\n"
    "s_mov_b32 s77, 147456\n"
    "s_mov_b32 s78, 0xffff0000\n"
    "s_add_u32 s96, %11, 2\n"
    "s_cmp_eq_u32 %11, 0\n"
    "s_cselect_b32 s96, 1, s96\n"
    "s_mul_i32 s96, s96, 24576\n"
    "s_add_u32 s96, s96, 0\n"
    "v_add_u32 v157, 0, v150\n"
    "v_mul_u32_u24 v160, 2731, v157\n"
    "v_lshrrev_b32 v160, 16, v160\n"
    "v_and_b32 v160, 6, v160\n"
    "v_xor_b32 v157, v160, v150\n"
    "v_lshlrev_b32 v157, 4, v157\n"
    "v_add_u32 v158, 64, v150\n"
    "v_mul_u32_u24 v160, 2731, v158\n"
    "v_lshrrev_b32 v160, 16, v160\n"
    "v_and_b32 v160, 6, v160\n"
    "v_xor_b32 v158, v160, v150\n"
    "v_lshlrev_b32 v158, 4, v158\n"
    "v_add_u32 v159, 128, v150\n"
    "v_mul_u32_u24 v160, 2731, v159\n"
    "v_lshrrev_b32 v160, 16, v160\n"
    "v_and_b32 v160, 6, v160\n"
    "v_xor_b32 v159, v160, v150\n"
    "v_lshlrev_b32 v159, 4, v159\n"
    "s_mov_b64 s[56:57], %8\n"
    "s_and_b32 s57, s57, 0xffff\n"
    "s_mov_b32 s58, 786432\n"
    "s_mov_b32 s59, 0x20000\n"
    "s_add_u32 s60, s56, 25165824\n"
    "s_addc_u32 s61, s57, 0\n"
    "s_mov_b32 s62, 786432\n"
    "s_mov_b32 s63, 0x20000\n"
    "s_mov_b32 s95, 0x40000\n"
    "s_mov_b32 s94, 0x4b800000\n"
    "s_mov_b32 s93, 0x3dd53b95\n"
    "s_lshl_b32 s86, %12, 2\n"
    "s_add_u32 s86, s86, %11\n"
    "s_lshl_b32 s86, s86, 3\n"
    "s_mov_b32 s87, 0\n"
    "v_and_b32 v250, 31, v150\n"
    "v_lshlrev_b32 v250, 3, v250\n"
    "s_waitcnt vmcnt(20)\n"
    "v_readfirstlane_b32 s80, v252\n"
    "v_readfirstlane_b32 s81, v253\n"
    "s_nop 2\n"
    "s_mul_i32 s80, s80, 0x9e3779b1\n"
    "s_add_u32 s80, s80, 0x7f4a7c15\n"
    "s_mul_i32 s81, s81, 0x85ebca6d\n"
    "s_add_u32 s81, s81, 0xc2b2ae35\n"
    "v_mov_b32 v252, s80\n"
    "v_mov_b32 v253, s81\n"
    "v_mov_b32 v182, s80\n"
    "v_mov_b32 v183, s81\n"
    "s_waitcnt vmcnt(10)\n"
    "s_bitcmp1_b32 s75, 0\n"
    "s_cbranch_scc1 FVP0%=\n"
    "v_cvt_f32_i32 v72, v72\n"
    "v_cvt_f32_i32 v73, v73\n"
    "v_cvt_f32_i32 v80, v80\n"
    "v_cvt_f32_i32 v81, v81\n"
    "v_lshlrev_b32 v246, 16, v92\n"
    "v_and_b32 v92, s78, v92\n"
    "v_lshlrev_b32 v247, 16, v88\n"
    "v_and_b32 v88, s78, v88\n"
    "v_mul_f32 v247, v247, v246\n"
    "v_mul_f32 v88, v88, v92\n"
    "v_fma_f32 v72, v72, v246, -v247\n"
    "v_fma_f32 v73, v73, v92, -v88\n"
    "v_fma_f32 v80, v80, v246, -v247\n"
    "v_fma_f32 v81, v81, v92, -v88\n"
    "v_cvt_pk_bf16_f32 v238, v72, v73\n"
    "v_cvt_pk_bf16_f32 v242, v80, v81\n"
    "v_cvt_f32_i32 v74, v74\n"
    "v_cvt_f32_i32 v75, v75\n"
    "v_cvt_f32_i32 v82, v82\n"
    "v_cvt_f32_i32 v83, v83\n"
    "v_lshlrev_b32 v246, 16, v93\n"
    "v_and_b32 v93, s78, v93\n"
    "v_lshlrev_b32 v247, 16, v89\n"
    "v_and_b32 v89, s78, v89\n"
    "v_mul_f32 v247, v247, v246\n"
    "v_mul_f32 v89, v89, v93\n"
    "v_fma_f32 v74, v74, v246, -v247\n"
    "v_fma_f32 v75, v75, v93, -v89\n"
    "v_fma_f32 v82, v82, v246, -v247\n"
    "v_fma_f32 v83, v83, v93, -v89\n"
    "v_cvt_pk_bf16_f32 v239, v74, v75\n"
    "v_cvt_pk_bf16_f32 v243, v82, v83\n"
    "v_cvt_f32_i32 v76, v76\n"
    "v_cvt_f32_i32 v77, v77\n"
    "v_cvt_f32_i32 v84, v84\n"
    "v_cvt_f32_i32 v85, v85\n"
    "v_lshlrev_b32 v246, 16, v94\n"
    "v_and_b32 v94, s78, v94\n"
    "v_lshlrev_b32 v247, 16, v90\n"
    "v_and_b32 v90, s78, v90\n"
    "v_mul_f32 v247, v247, v246\n"
    "v_mul_f32 v90, v90, v94\n"
    "v_fma_f32 v76, v76, v246, -v247\n"
    "v_fma_f32 v77, v77, v94, -v90\n"
    "v_fma_f32 v84, v84, v246, -v247\n"
    "v_fma_f32 v85, v85, v94, -v90\n"
    "v_cvt_pk_bf16_f32 v240, v76, v77\n"
    "v_cvt_pk_bf16_f32 v244, v84, v85\n"
    "v_cvt_f32_i32 v78, v78\n"
    "v_cvt_f32_i32 v79, v79\n"
    "v_cvt_f32_i32 v86, v86\n"
    "v_cvt_f32_i32 v87, v87\n"
    "v_lshlrev_b32 v246, 16, v95\n"
    "v_and_b32 v95, s78, v95\n"
    "v_lshlrev_b32 v247, 16, v91\n"
    "v_and_b32 v91, s78, v91\n"
    "v_mul_f32 v247, v247, v246\n"
    "v_mul_f32 v91, v91, v95\n"
    "v_fma_f32 v78, v78, v246, -v247\n"
    "v_fma_f32 v79, v79, v95, -v91\n"
    "v_fma_f32 v86, v86, v246, -v247\n"
    "v_fma_f32 v87, v87, v95, -v91\n"
    "v_cvt_pk_bf16_f32 v241, v78, v79\n"
    "v_cvt_pk_bf16_f32 v245, v86, v87\n"
    "buffer_store_dwordx4 v[238:241], v149, s[56:59], s72 offen offset:0 sc1\n"
    "buffer_store_dwordx4 v[242:245], v149, s[56:59], s72 offen offset:1024 sc1\n"
    "s_branch FJP0%=\n"
    "FVP0%=:\n"
    "v_cvt_f32_i32 v72, v72\n"
    "v_cvt_f32_i32 v74, v74\n"
    "v_cvt_f32_i32 v73, v73\n"
    "v_cvt_f32_i32 v75, v75\n"
    "v_lshlrev_b32 v246, 16, v92\n"
    "v_and_b32 v92, s78, v92\n"
    "v_lshlrev_b32 v247, 16, v88\n"
    "v_and_b32 v88, s78, v88\n"
    "v_mul_f32 v247, v247, v246\n"
    "v_mul_f32 v88, v88, v92\n"
    "v_fma_f32 v72, v72, v246, -v247\n"
    "v_fma_f32 v74, v74, v92, -v88\n"
    "v_fma_f32 v73, v73, v246, -v247\n"
    "v_fma_f32 v75, v75, v92, -v88\n"
    "v_cvt_pk_bf16_f32 v238, v72, v74\n"
    "v_cvt_pk_bf16_f32 v242, v73, v75\n"
    "v_cvt_f32_i32 v76, v76\n"
    "v_cvt_f32_i32 v78, v78\n"
    "v_cvt_f32_i32 v77, v77\n"
    "v_cvt_f32_i32 v79, v79\n"
    "v_lshlrev_b32 v246, 16, v93\n"
    "v_and_b32 v93, s78, v93\n"
    "v_lshlrev_b32 v247, 16, v89\n"
    "v_and_b32 v89, s78, v89\n"
    "v_mul_f32 v247, v247, v246\n"
    "v_mul_f32 v89, v89, v93\n"
    "v_fma_f32 v76, v76, v246, -v247\n"
    "v_fma_f32 v78, v78, v93, -v89\n"
    "v_fma_f32 v77, v77, v246, -v247\n"
    "v_fma_f32 v79, v79, v93, -v89\n"
    "v_cvt_pk_bf16_f32 v239, v76, v78\n"
    "v_cvt_pk_bf16_f32 v243, v77, v79\n"
    "v_cvt_f32_i32 v80, v80\n"
    "v_cvt_f32_i32 v82, v82\n"
    "v_cvt_f32_i32 v81, v81\n"
    "v_cvt_f32_i32 v83, v83\n"
    "v_lshlrev_b32 v246, 16, v94\n"
    "v_and_b32 v94, s78, v94\n"
    "v_lshlrev_b32 v247, 16, v90\n"
    "v_and_b32 v90, s78, v90\n"
    "v_mul_f32 v247, v247, v246\n"
    "v_mul_f32 v90, v90, v94\n"
    "v_fma_f32 v80, v80, v246, -v247\n"
    "v_fma_f32 v82, v82, v94, -v90\n"
    "v_fma_f32 v81, v81, v246, -v247\n"
    "v_fma_f32 v83, v83, v94, -v90\n"
    "v_cvt_pk_bf16_f32 v240, v80, v82\n"
    "v_cvt_pk_bf16_f32 v244, v81, v83\n"
    "v_cvt_f32_i32 v84, v84\n"
    "v_cvt_f32_i32 v86, v86\n"
    "v_cvt_f32_i32 v85, v85\n"
    "v_cvt_f32_i32 v87, v87\n"
    "v_lshlrev_b32 v246, 16, v95\n"
    "v_and_b32 v95, s78, v95\n"
    "v_lshlrev_b32 v247, 16, v91\n"
    "v_and_b32 v91, s78, v91\n"
    "v_mul_f32 v247, v247, v246\n"
    "v_mul_f32 v91, v91, v95\n"
    "v_fma_f32 v84, v84, v246, -v247\n"
    "v_fma_f32 v86, v86, v95, -v91\n"
    "v_fma_f32 v85, v85, v246, -v247\n"
    "v_fma_f32 v87, v87, v95, -v91\n"
    "v_cvt_pk_bf16_f32 v241, v84, v86\n"
    "v_cvt_pk_bf16_f32 v245, v85, v87\n"
    "buffer_store_dwordx4 v[238:241], v149, s[60:63], s72 offen offset:0 sc1\n"
    "buffer_store_dwordx4 v[242:245], v149, s[60:63], s72 offen offset:1024 sc1\n"
    "FJP0%=:\n"
    "s_add_u32 m0, s96, 0\n"
    "s_mov_b32 s92, 0\n"
    "buffer_load_dwordx4 v157, s[68:71], s92 offen offset:0 nt lds\n"
    "buffer_load_dwordx4 v158, s[68:71], s92 offen offset:1024 nt lds\n"
    "buffer_load_dwordx4 v159, s[68:71], s92 offen offset:2048 nt lds\n"
    "s_waitcnt vmcnt(5)\n"
    "s_bitcmp1_b32 s75, 1\n"
    "s_cbranch_scc1 FVP1%=\n"
    "v_cvt_f32_i32 v96, v96\n"
    "v_cvt_f32_i32 v97, v97\n"
    "v_cvt_f32_i32 v104, v104\n"
    "v_cvt_f32_i32 v105, v105\n"
    "v_lshlrev_b32 v246, 16, v116\n"
    "v_and_b32 v116, s78, v116\n"
    "v_lshlrev_b32 v247, 16, v112\n"
    "v_and_b32 v112, s78, v112\n"
    "v_mul_f32 v247, v247, v246\n"
    "v_mul_f32 v112, v112, v116\n"
    "v_fma_f32 v96, v96, v246, -v247\n"
    "v_fma_f32 v97, v97, v116, -v112\n"
    "v_fma_f32 v104, v104, v246, -v247\n"
    "v_fma_f32 v105, v105, v116, -v112\n"
    "v_cvt_pk_bf16_f32 v190, v96, v97\n"
    "v_cvt_pk_bf16_f32 v194, v104, v105\n"
    "v_cvt_f32_i32 v98, v98\n"
    "v_cvt_f32_i32 v99, v99\n"
    "v_cvt_f32_i32 v106, v106\n"
    "v_cvt_f32_i32 v107, v107\n"
    "v_lshlrev_b32 v246, 16, v117\n"
    "v_and_b32 v117, s78, v117\n"
    "v_lshlrev_b32 v247, 16, v113\n"
    "v_and_b32 v113, s78, v113\n"
    "v_mul_f32 v247, v247, v246\n"
    "v_mul_f32 v113, v113, v117\n"
    "v_fma_f32 v98, v98, v246, -v247\n"
    "v_fma_f32 v99, v99, v117, -v113\n"
    "v_fma_f32 v106, v106, v246, -v247\n"
    "v_fma_f32 v107, v107, v117, -v113\n"
    "v_cvt_pk_bf16_f32 v191, v98, v99\n"
    "v_cvt_pk_bf16_f32 v195, v106, v107\n"
    "v_cvt_f32_i32 v100, v100\n"
    "v_cvt_f32_i32 v101, v101\n"
    "v_cvt_f32_i32 v108, v108\n"
    "v_cvt_f32_i32 v109, v109\n"
    "v_lshlrev_b32 v246, 16, v118\n"
    "v_and_b32 v118, s78, v118\n"
    "v_lshlrev_b32 v247, 16, v114\n"
    "v_and_b32 v114, s78, v114\n"
    "v_mul_f32 v247, v247, v246\n"
    "v_mul_f32 v114, v114, v118\n"
    "v_fma_f32 v100, v100, v246, -v247\n"
    "v_fma_f32 v101, v101, v118, -v114\n"
    "v_fma_f32 v108, v108, v246, -v247\n"
    "v_fma_f32 v109, v109, v118, -v114\n"
    "v_cvt_pk_bf16_f32 v192, v100, v101\n"
    "v_cvt_pk_bf16_f32 v196, v108, v109\n"
    "v_cvt_f32_i32 v102, v102\n"
    "v_cvt_f32_i32 v103, v103\n"
    "v_cvt_f32_i32 v110, v110\n"
    "v_cvt_f32_i32 v111, v111\n"
    "v_lshlrev_b32 v246, 16, v119\n"
    "v_and_b32 v119, s78, v119\n"
    "v_lshlrev_b32 v247, 16, v115\n"
    "v_and_b32 v115, s78, v115\n"
    "v_mul_f32 v247, v247, v246\n"
    "v_mul_f32 v115, v115, v119\n"
    "v_fma_f32 v102, v102, v246, -v247\n"
    "v_fma_f32 v103, v103, v119, -v115\n"
    "v_fma_f32 v110, v110, v246, -v247\n"
    "v_fma_f32 v111, v111, v119, -v115\n"
    "v_cvt_pk_bf16_f32 v193, v102, v103\n"
    "v_cvt_pk_bf16_f32 v197, v110, v111\n"
    "buffer_store_dwordx4 v[190:193], v149, s[56:59], s73 offen offset:0 sc1\n"
    "buffer_store_dwordx4 v[194:197], v149, s[56:59], s73 offen offset:1024 sc1\n"
    "s_branch FJP1%=\n"
    "FVP1%=:\n"
    "v_cvt_f32_i32 v96, v96\n"
    "v_cvt_f32_i32 v98, v98\n"
    "v_cvt_f32_i32 v97, v97\n"
    "v_cvt_f32_i32 v99, v99\n"
    "v_lshlrev_b32 v246, 16, v116\n"
    "v_and_b32 v116, s78, v116\n"
    "v_lshlrev_b32 v247, 16, v112\n"
    "v_and_b32 v112, s78, v112\n"
    "v_mul_f32 v247, v247, v246\n"
    "v_mul_f32 v112, v112, v116\n"
    "v_fma_f32 v96, v96, v246, -v247\n"
    "v_fma_f32 v98, v98, v116, -v112\n"
    "v_fma_f32 v97, v97, v246, -v247\n"
    "v_fma_f32 v99, v99, v116, -v112\n"
    "v_cvt_pk_bf16_f32 v190, v96, v98\n"
    "v_cvt_pk_bf16_f32 v194, v97, v99\n"
    "v_cvt_f32_i32 v100, v100\n"
    "v_cvt_f32_i32 v102, v102\n"
    "v_cvt_f32_i32 v101, v101\n"
    "v_cvt_f32_i32 v103, v103\n"
    "v_lshlrev_b32 v246, 16, v117\n"
    "v_and_b32 v117, s78, v117\n"
    "v_lshlrev_b32 v247, 16, v113\n"
    "v_and_b32 v113, s78, v113\n"
    "v_mul_f32 v247, v247, v246\n"
    "v_mul_f32 v113, v113, v117\n"
    "v_fma_f32 v100, v100, v246, -v247\n"
    "v_fma_f32 v102, v102, v117, -v113\n"
    "v_fma_f32 v101, v101, v246, -v247\n"
    "v_fma_f32 v103, v103, v117, -v113\n"
    "v_cvt_pk_bf16_f32 v191, v100, v102\n"
    "v_cvt_pk_bf16_f32 v195, v101, v103\n"
    "v_cvt_f32_i32 v104, v104\n"
    "v_cvt_f32_i32 v106, v106\n"
    "v_cvt_f32_i32 v105, v105\n"
    "v_cvt_f32_i32 v107, v107\n"
    "v_lshlrev_b32 v246, 16, v118\n"
    "v_and_b32 v118, s78, v118\n"
    "v_lshlrev_b32 v247, 16, v114\n"
    "v_and_b32 v114, s78, v114\n"
    "v_mul_f32 v247, v247, v246\n"
    "v_mul_f32 v114, v114, v118\n"
    "v_fma_f32 v104, v104, v246, -v247\n"
    "v_fma_f32 v106, v106, v118, -v114\n"
    "v_fma_f32 v105, v105, v246, -v247\n"
    "v_fma_f32 v107, v107, v118, -v114\n"
    "v_cvt_pk_bf16_f32 v192, v104, v106\n"
    "v_cvt_pk_bf16_f32 v196, v105, v107\n"
    "v_cvt_f32_i32 v108, v108\n"
    "v_cvt_f32_i32 v110, v110\n"
    "v_cvt_f32_i32 v109, v109\n"
    "v_cvt_f32_i32 v111, v111\n"
    "v_lshlrev_b32 v246, 16, v119\n"
    "v_and_b32 v119, s78, v119\n"
    "v_lshlrev_b32 v247, 16, v115\n"
    "v_and_b32 v115, s78, v115\n"
    "v_mul_f32 v247, v247, v246\n"
    "v_mul_f32 v115, v115, v119\n"
    "v_fma_f32 v108, v108, v246, -v247\n"
    "v_fma_f32 v110, v110, v119, -v115\n"
    "v_fma_f32 v109, v109, v246, -v247\n"
    "v_fma_f32 v111, v111, v119, -v115\n"
    "v_cvt_pk_bf16_f32 v193, v108, v110\n"
    "v_cvt_pk_bf16_f32 v197, v109, v111\n"
    "buffer_store_dwordx4 v[190:193], v149, s[60:63], s73 offen offset:0 sc1\n"
    "buffer_store_dwordx4 v[194:197], v149, s[60:63], s73 offen offset:1024 sc1\n"
    "FJP1%=:\n"
    "s_add_u32 m0, s96, 3072\n"
    "s_mov_b32 s92, 3072\n"
    "buffer_load_dwordx4 v157, s[68:71], s92 offen offset:0 nt lds\n"
    "buffer_load_dwordx4 v158, s[68:71], s92 offen offset:1024 nt lds\n"
    "buffer_load_dwordx4 v159, s[68:71], s92 offen offset:2048 nt lds\n"
    "s_waitcnt vmcnt(8)\n"
    "buffer_store_dwordx2 v[182:183], off, s[64:67], s86 offset:0 sc1\n"
    "s_add_u32 m0, s96, 6144\n"
    "s_mov_b32 s92, 6144\n"
    "buffer_load_dwordx4 v157, s[68:71], s92 offen offset:0 nt lds\n"
    "buffer_load_dwordx4 v158, s[68:71], s92 offen offset:1024 nt lds\n"
    "buffer_load_dwordx4 v159, s[68:71], s92 offen offset:2048 nt lds\n"
    "s_add_u32 m0, s96, 9216\n"
    "s_mov_b32 s92, 9216\n"
    "buffer_load_dwordx4 v157, s[68:71], s92 offen offset:0 nt lds\n"
    "buffer_load_dwordx4 v158, s[68:71], s92 offen offset:1024 nt lds\n"
    "buffer_load_dwordx4 v159, s[68:71], s92 offen offset:2048 nt lds\n"
    "v_and_b32 v161, 6, v151\n"
    "v_lshrrev_b32 v166, 1, v152\n"
    "v_xor_b32 v166, v166, v161\n"
    "v_mul_u32_u24 v161, 384, v151\n"
    "v_add_u32 v161, s96, v161\n"
    "v_and_b32 v167, 1, v152\n"
    "v_lshl_add_u32 v161, v167, 3, v161\n"
    "v_xor_b32 v167, 0, v166\n"
    "v_lshl_add_u32 v162, v167, 4, v161\n"
    "v_xor_b32 v167, 4, v166\n"
    "v_lshl_add_u32 v163, v167, 4, v161\n"
    "v_xor_b32 v167, 2, v166\n"
    "v_lshl_add_u32 v164, v167, 4, v161\n"
    "v_xor_b32 v167, 6, v166\n"
    "v_lshl_add_u32 v165, v167, 4, v161\n"
    "v_add_u32 v148, 0, v149\n"
    "v_and_b32 v166, 6, v151\n"
    "v_xor_b32 v166, v166, v152\n"
    "v_lshlrev_b32 v166, 4, v166\n"
    "v_lshl_add_u32 v254, v151, 7, v166\n"
    "v_add_u32 v254, 0, v254\n"
    "v_xor_b32 v251, 64, v254\n"
    "v_accvgpr_write_b32 a0, 0\n"
    "v_accvgpr_write_b32 a1, 0\n"
    "v_accvgpr_write_b32 a2, 0\n"
    "v_accvgpr_write_b32 a3, 0\n"
    "v_accvgpr_write_b32 a4, 0\n"
    "v_accvgpr_write_b32 a5, 0\n"
    "v_accvgpr_write_b32 a6, 0\n"
    "v_accvgpr_write_b32 a7, 0\n"
    "v_accvgpr_write_b32 a8, 0\n"
    "v_accvgpr_write_b32 a9, 0\n"
    "v_accvgpr_write_b32 a10, 0\n"
    "v_accvgpr_write_b32 a11, 0\n"
    "v_accvgpr_write_b32 a12, 0\n"
    "v_accvgpr_write_b32 a13, 0\n"
    "v_accvgpr_write_b32 a14, 0\n"
    "v_accvgpr_write_b32 a15, 0\n"
    "v_accvgpr_write_b32 a16, 0\n"
    "v_accvgpr_write_b32 a17, 0\n"
    "v_accvgpr_write_b32 a18, 0\n"
    "v_accvgpr_write_b32 a19, 0\n"
    "v_accvgpr_write_b32 a20, 0\n"
    "v_accvgpr_write_b32 a21, 0\n"
    "v_accvgpr_write_b32 a22, 0\n"
    "v_accvgpr_write_b32 a23, 0\n"
    "v_accvgpr_write_b32 a24, 0\n"
    "v_accvgpr_write_b32 a25, 0\n"
    "v_accvgpr_write_b32 a26, 0\n"
    "v_accvgpr_write_b32 a27, 0\n"
    "v_accvgpr_write_b32 a28, 0\n"
    "v_accvgpr_write_b32 a29, 0\n"
    "v_accvgpr_write_b32 a30, 0\n"
    "v_accvgpr_write_b32 a31, 0\n"
    "v_accvgpr_write_b32 a32, 0\n"
    "v_accvgpr_write_b32 a33, 0\n"
    "v_accvgpr_write_b32 a34, 0\n"
    "v_accvgpr_write_b32 a35, 0\n"
    "v_accvgpr_write_b32 a36, 0\n"
    "v_accvgpr_write_b32 a37, 0\n"
    "v_accvgpr_write_b32 a38, 0\n"
    "v_accvgpr_write_b32 a39, 0\n"
    "v_accvgpr_write_b32 a40, 0\n"
    "v_accvgpr_write_b32 a41, 0\n"
    "v_accvgpr_write_b32 a42, 0\n"
    "v_accvgpr_write_b32 a43, 0\n"
    "v_accvgpr_write_b32 a44, 0\n"
    "v_accvgpr_write_b32 a45, 0\n"
    "v_accvgpr_write_b32 a46, 0\n"
    "v_accvgpr_write_b32 a47, 0\n"
    "v_accvgpr_write_b32 a48, 0\n"
    "v_accvgpr_write_b32 a49, 0\n"
    "v_accvgpr_write_b32 a50, 0\n"
    "v_accvgpr_write_b32 a51, 0\n"
    "v_accvgpr_write_b32 a52, 0\n"
    "v_accvgpr_write_b32 a53, 0\n"
    "v_accvgpr_write_b32 a54, 0\n"
    "v_accvgpr_write_b32 a55, 0\n"
    "v_accvgpr_write_b32 a56, 0\n"
    "v_accvgpr_write_b32 a57, 0\n"
    "v_accvgpr_write_b32 a58, 0\n"
    "v_accvgpr_write_b32 a59, 0\n"
    "v_accvgpr_write_b32 a60, 0\n"
    "v_accvgpr_write_b32 a61, 0\n"
    "v_accvgpr_write_b32 a62, 0\n"
    "v_accvgpr_write_b32 a63, 0\n"
    "s_waitcnt vmcnt(10)\n"
    "buffer_store_dwordx2 v[182:183], off, s[64:67], s86 offset:256 sc1\n"
    "s_getpc_b64 s[88:89]\n"
    "PC0%=:\n"
    "s_and_b32 s89, s89, 0xffff\n"
    "s_mov_b32 s90, END%=-PC0%=\n"
    "s_mov_b32 s91, 0x20000\n"
    "v_and_b32 v156, 3, v150\n"
    "v_lshlrev_b32 v156, 7, v156\n"
    "s_lshl_b32 s82, %13, 9\n"
    "buffer_load_dword v156, v156, s[88:91], s82 offen\n"
    "s_add_u32 s89, %12, 16\n"
    "s_mul_i32 s88, s89, 11\n"
    "s_lshr_b32 s88, s88, 5\n"
    "s_mul_i32 s90, s88, 3\n"
    "s_sub_u32 s89, s89, s90\n"
    "s_lshr_b32 s90, 0xd4, s88\n"
    "s_and_b32 s90, s90, 1\n"
    "s_lshl_b32 s90, s90, 2\n"
    "s_or_b32 s75, s75, s90\n"
    "s_lshl_b32 s88, s88, 1\n"
    "s_lshr_b32 s88, 0xed84, s88\n"
    "s_and_b32 s88, s88, 3\n"
    "s_mul_i32 s74, s88, 12\n"
    "s_mul_i32 s90, %11, 3\n"
    "s_add_u32 s74, s74, s90\n"
    "s_add_u32 s74, s74, s89\n"
    "s_lshl_b32 s74, s74, 11\n"
    "s_lshl_b32 s88, s88, 1\n"
    "s_add_u32 s88, s88, s79\n"
    "s_mul_i32 s82, s88, 24576\n"
    "s_bitcmp1_b32 s75, 2\n"
    "s_cbranch_scc1 FVL2%=\n"
    "s_mul_i32 s90, s97, 12\n"
    "s_add_u32 s90, s90, s89\n"
    "s_lshl_b32 s90, s90, 8\n"
    "s_add_u32 s82, s82, s90\n"
    "s_add_u32 s83, s82, 12288\n"
    "s_mul_i32 s84, s88, 3\n"
    "s_add_u32 s84, s84, s89\n"
    "s_lshl_b32 s84, s84, 7\n"
    "buffer_load_dwordx4 v[120:123], v178, s[40:43], s82 offen offset:0 nt\n"
    "buffer_load_dwordx4 v[124:127], v178, s[40:43], s82 offen offset:128 nt\n"
    "buffer_load_dwordx4 v[128:131], v178, s[40:43], s83 offen offset:0 nt\n"
    "buffer_load_dwordx4 v[132:135], v178, s[40:43], s83 offen offset:128 nt\n"
    "buffer_load_dwordx2 v[136:137], v179, s[44:47], s84 offen offset:0 nt\n"
    "buffer_load_dwordx2 v[138:139], v179, s[44:47], s84 offen offset:64 nt\n"
    "buffer_load_dwordx2 v[140:141], v179, s[48:51], s84 offen offset:0 nt\n"
    "buffer_load_dwordx2 v[142:143], v179, s[48:51], s84 offen offset:64 nt\n"
    "buffer_load_dwordx2 v[140:141], v179, s[48:51], s84 offen offset:0 nt\n"
    "buffer_load_dwordx2 v[142:143], v179, s[48:51], s84 offen offset:64 nt\n"
    "s_branch FJL2%=\n"
    "FVL2%=:\n"
    "s_mul_i32 s90, s97, 3\n"
    "s_add_u32 s90, s90, s89\n"
    "s_lshl_b32 s90, s90, 7\n"
    "s_add_u32 s82, s82, s90\n"
    "s_add_u32 s83, s82, 3072\n"
    "s_lshl_b32 s84, s88, 6\n"
    "buffer_load_dwordx2 v[120:121], v180, s[28:31], s82 offen offset:0 nt\n"
    "buffer_load_dwordx2 v[122:123], v180, s[28:31], s82 offen offset:768 nt\n"
    "buffer_load_dwordx2 v[124:125], v180, s[28:31], s82 offen offset:1536 nt\n"
    "buffer_load_dwordx2 v[126:127], v180, s[28:31], s82 offen offset:2304 nt\n"
    "buffer_load_dwordx2 v[128:129], v180, s[28:31], s83 offen offset:0 nt\n"
    "buffer_load_dwordx2 v[130:131], v180, s[28:31], s83 offen offset:768 nt\n"
    "buffer_load_dwordx2 v[132:133], v180, s[28:31], s83 offen offset:1536 nt\n"
    "buffer_load_dwordx2 v[134:135], v180, s[28:31], s83 offen offset:2304 nt\n"
    "buffer_load_dwordx4 v[136:139], v181, s[36:39], s84 offen nt\n"
    "buffer_load_dwordx4 v[140:143], v181, s[52:55], s84 offen nt\n"
    "FJL2%=:\n"
    "s_add_u32 m0, s96, 12288\n"
    "s_mov_b32 s92, 12288\n"
    "buffer_load_dwordx4 v157, s[68:71], s92 offen offset:0 nt lds\n"
    "buffer_load_dwordx4 v158, s[68:71], s92 offen offset:1024 nt lds\n"
    "buffer_load_dwordx4 v159, s[68:71], s92 offen offset:2048 nt lds\n"
    "v_accvgpr_write_b32 a64, 0\n"
    "v_accvgpr_write_b32 a65, 0\n"
    "v_accvgpr_write_b32 a66, 0\n"
    "v_accvgpr_write_b32 a67, 0\n"
    "v_accvgpr_write_b32 a68, 0\n"
    "v_accvgpr_write_b32 a69, 0\n"
    "v_accvgpr_write_b32 a70, 0\n"
    "v_accvgpr_write_b32 a71, 0\n"
    "v_accvgpr_write_b32 a72, 0\n"
    "v_accvgpr_write_b32 a73, 0\n"
    "v_accvgpr_write_b32 a74, 0\n"
    "v_accvgpr_write_b32 a75, 0\n"
    "v_accvgpr_write_b32 a76, 0\n"
    "v_accvgpr_write_b32 a77, 0\n"
    "v_accvgpr_write_b32 a78, 0\n"
    "v_accvgpr_write_b32 a79, 0\n"
    "v_accvgpr_write_b32 a80, 0\n"
    "v_accvgpr_write_b32 a81, 0\n"
    "v_accvgpr_write_b32 a82, 0\n"
    "v_accvgpr_write_b32 a83, 0\n"
    "v_accvgpr_write_b32 a84, 0\n"
    "v_accvgpr_write_b32 a85, 0\n"
    "v_accvgpr_write_b32 a86, 0\n"
    "v_accvgpr_write_b32 a87, 0\n"
    "v_accvgpr_write_b32 a88, 0\n"
    "v_accvgpr_write_b32 a89, 0\n"
    "v_accvgpr_write_b32 a90, 0\n"
    "v_accvgpr_write_b32 a91, 0\n"
    "v_accvgpr_write_b32 a92, 0\n"
    "v_accvgpr_write_b32 a93, 0\n"
    "v_accvgpr_write_b32 a94, 0\n"
    "v_accvgpr_write_b32 a95, 0\n"
    "v_accvgpr_write_b32 a96, 0\n"
    "v_accvgpr_write_b32 a97, 0\n"
    "v_accvgpr_write_b32 a98, 0\n"
    "v_accvgpr_write_b32 a99, 0\n"
    "v_accvgpr_write_b32 a100, 0\n"
    "v_accvgpr_write_b32 a101, 0\n"
    "v_accvgpr_write_b32 a102, 0\n"
    "v_accvgpr_write_b32 a103, 0\n"
    "v_accvgpr_write_b32 a104, 0\n"
    "v_accvgpr_write_b32 a105, 0\n"
    "v_accvgpr_write_b32 a106, 0\n"
    "v_accvgpr_write_b32 a107, 0\n"
    "v_accvgpr_write_b32 a108, 0\n"
    "v_accvgpr_write_b32 a109, 0\n"
    "v_accvgpr_write_b32 a110, 0\n"
    "v_accvgpr_write_b32 a111, 0\n"
    "v_accvgpr_write_b32 a112, 0\n"
    "v_accvgpr_write_b32 a113, 0\n"
    "v_accvgpr_write_b32 a114, 0\n"
    "v_accvgpr_write_b32 a115, 0\n"
    "v_accvgpr_write_b32 a116, 0\n"
    "v_accvgpr_write_b32 a117, 0\n"
    "v_accvgpr_write_b32 a118, 0\n"
    "v_accvgpr_write_b32 a119, 0\n"
    "v_accvgpr_write_b32 a120, 0\n"
    "v_accvgpr_write_b32 a121, 0\n"
    "v_accvgpr_write_b32 a122, 0\n"
    "v_accvgpr_write_b32 a123, 0\n"
    "v_accvgpr_write_b32 a124, 0\n"
    "v_accvgpr_write_b32 a125, 0\n"
    "v_accvgpr_write_b32 a126, 0\n"
    "v_accvgpr_write_b32 a127, 0\n"
    "v_accvgpr_write_b32 a128, 0\n"
    "v_accvgpr_write_b32 a129, 0\n"
    "v_accvgpr_write_b32 a130, 0\n"
    "v_accvgpr_write_b32 a131, 0\n"
    "v_accvgpr_write_b32 a132, 0\n"
    "v_accvgpr_write_b32 a133, 0\n"
    "v_accvgpr_write_b32 a134, 0\n"
    "v_accvgpr_write_b32 a135, 0\n"
    "v_accvgpr_write_b32 a136, 0\n"
    "v_accvgpr_write_b32 a137, 0\n"
    "v_accvgpr_write_b32 a138, 0\n"
    "v_accvgpr_write_b32 a139, 0\n"
    "v_accvgpr_write_b32 a140, 0\n"
    "v_accvgpr_write_b32 a141, 0\n"
    "v_accvgpr_write_b32 a142, 0\n"
    "v_accvgpr_write_b32 a143, 0\n"
    "v_accvgpr_write_b32 a144, 0\n"
    "v_accvgpr_write_b32 a145, 0\n"
    "v_accvgpr_write_b32 a146, 0\n"
    "v_accvgpr_write_b32 a147, 0\n"
    "v_accvgpr_write_b32 a148, 0\n"
    "v_accvgpr_write_b32 a149, 0\n"
    "v_accvgpr_write_b32 a150, 0\n"
    "v_accvgpr_write_b32 a151, 0\n"
    "v_accvgpr_write_b32 a152, 0\n"
    "v_accvgpr_write_b32 a153, 0\n"
    "v_accvgpr_write_b32 a154, 0\n"
    "v_accvgpr_write_b32 a155, 0\n"
    "v_accvgpr_write_b32 a156, 0\n"
    "v_accvgpr_write_b32 a157, 0\n"
    "v_accvgpr_write_b32 a158, 0\n"
    "v_accvgpr_write_b32 a159, 0\n"
    "v_accvgpr_write_b32 a160, 0\n"
    "v_accvgpr_write_b32 a161, 0\n"
    "v_accvgpr_write_b32 a162, 0\n"
    "v_accvgpr_write_b32 a163, 0\n"
    "v_accvgpr_write_b32 a164, 0\n"
    "v_accvgpr_write_b32 a165, 0\n"
    "v_accvgpr_write_b32 a166, 0\n"
    "v_accvgpr_write_b32 a167, 0\n"
    "v_accvgpr_write_b32 a168, 0\n"
    "v_accvgpr_write_b32 a169, 0\n"
    "v_accvgpr_write_b32 a170, 0\n"
    "v_accvgpr_write_b32 a171, 0\n"
    "v_accvgpr_write_b32 a172, 0\n"
    "v_accvgpr_write_b32 a173, 0\n"
    "v_accvgpr_write_b32 a174, 0\n"
    "v_accvgpr_write_b32 a175, 0\n"
    "v_accvgpr_write_b32 a176, 0\n"
    "v_accvgpr_write_b32 a177, 0\n"
    "v_accvgpr_write_b32 a178, 0\n"
    "v_accvgpr_write_b32 a179, 0\n"
    "v_accvgpr_write_b32 a180, 0\n"
    "v_accvgpr_write_b32 a181, 0\n"
    "v_accvgpr_write_b32 a182, 0\n"
    "v_accvgpr_write_b32 a183, 0\n"
    "v_accvgpr_write_b32 a184, 0\n"
    "v_accvgpr_write_b32 a185, 0\n"
    "v_accvgpr_write_b32 a186, 0\n"
    "v_accvgpr_write_b32 a187, 0\n"
    "v_accvgpr_write_b32 a188, 0\n"
    "v_accvgpr_write_b32 a189, 0\n"
    "v_accvgpr_write_b32 a190, 0\n"
    "v_accvgpr_write_b32 a191, 0\n"
    "v_mov_b32 v144, 0\n"
    "v_mov_b32 v145, 0\n"
    "v_mov_b32 v146, 0\n"
    "v_mov_b32 v147, 0\n"
    "s_waitcnt vmcnt(22)\n"
    "s_add_u32 m0, s96, 15360\n"
    "s_mov_b32 s92, 15360\n"
    "buffer_load_dwordx4 v157, s[68:71], s92 offen offset:0 nt lds\n"
    "buffer_load_dwordx4 v158, s[68:71], s92 offen offset:1024 nt lds\n"
    "buffer_load_dwordx4 v159, s[68:71], s92 offen offset:2048 nt lds\n"
    "s_waitcnt vmcnt(25)\n"
    "ds_read_b64 v[32:33], v162 offset:0\n"
    "ds_read_b64 v[34:35], v163 offset:0\n"
    "ds_read_b64 v[36:37], v164 offset:0\n"
    "ds_read_b64 v[38:39], v165 offset:0\n"
    "ds_read_b64 v[40:41], v162 offset:128\n"
    "ds_read_b64 v[42:43], v163 offset:128\n"
    "ds_read_b64 v[44:45], v164 offset:128\n"
    "ds_read_b64 v[46:47], v165 offset:128\n"
    "ds_read_b64 v[48:49], v162 offset:256\n"
    "ds_read_b64 v[50:51], v163 offset:256\n"
    "ds_read_b64 v[52:53], v164 offset:256\n"
    "ds_read_b64 v[54:55], v165 offset:256\n"
    "s_waitcnt lgkmcnt(11)\n"
    "v_lshlrev_b32 v170, 16, v32\n"
    "v_and_b32 v171, s78, v32\n"
    "v_lshlrev_b32 v172, 16, v33\n"
    "v_and_b32 v173, s78, v33\n"
    "s_waitcnt lgkmcnt(10)\n"
    "v_lshlrev_b32 v174, 16, v34\n"
    "v_and_b32 v175, s78, v34\n"
    "v_lshlrev_b32 v176, 16, v35\n"
    "v_and_b32 v177, s78, v35\n"
    "v_pk_mul_f32 v[170:171], v[170:171], s[92:93] op_sel:[0,1] op_sel_hi:[1,1]\n"
    "v_pk_mul_f32 v[172:173], v[172:173], s[92:93] op_sel:[0,1] op_sel_hi:[1,1]\n"
    "v_pk_mul_f32 v[174:175], v[174:175], s[92:93] op_sel:[0,1] op_sel_hi:[1,1]\n"
    "v_pk_mul_f32 v[176:177], v[176:177], s[92:93] op_sel:[0,1] op_sel_hi:[1,1]\n"
    "v_cvt_pk_bf16_f32 v32, v170, v171\n"
    "v_cvt_pk_bf16_f32 v33, v172, v173\n"
    "v_cvt_pk_bf16_f32 v34, v174, v175\n"
    "v_cvt_pk_bf16_f32 v35, v176, v177\n"
    "v_accvgpr_write_b32 a192, v32\n"
    "v_accvgpr_write_b32 a193, v33\n"
    "v_accvgpr_write_b32 a194, v34\n"
    "v_accvgpr_write_b32 a195, v35\n"
    "s_waitcnt lgkmcnt(9)\n"
    "v_lshlrev_b32 v170, 16, v36\n"
    "v_and_b32 v171, s78, v36\n"
    "v_lshlrev_b32 v172, 16, v37\n"
    "v_and_b32 v173, s78, v37\n"
    "s_waitcnt lgkmcnt(8)\n"
    "v_lshlrev_b32 v174, 16, v38\n"
    "v_and_b32 v175, s78, v38\n"
    "v_lshlrev_b32 v176, 16, v39\n"
    "v_and_b32 v177, s78, v39\n"
    "v_pk_mul_f32 v[170:171], v[170:171], s[92:93] op_sel:[0,1] op_sel_hi:[1,1]\n"
    "v_pk_mul_f32 v[172:173], v[172:173], s[92:93] op_sel:[0,1] op_sel_hi:[1,1]\n"
    "v_pk_mul_f32 v[174:175], v[174:175], s[92:93] op_sel:[0,1] op_sel_hi:[1,1]\n"
    "v_pk_mul_f32 v[176:177], v[176:177], s[92:93] op_sel:[0,1] op_sel_hi:[1,1]\n"
    "v_cvt_pk_bf16_f32 v36, v170, v171\n"
    "v_cvt_pk_bf16_f32 v37, v172, v173\n"
    "v_cvt_pk_bf16_f32 v38, v174, v175\n"
    "v_cvt_pk_bf16_f32 v39, v176, v177\n"
    "v_accvgpr_write_b32 a196, v36\n"
    "v_accvgpr_write_b32 a197, v37\n"
    "v_accvgpr_write_b32 a198, v38\n"
    "v_accvgpr_write_b32 a199, v39\n"
    "s_waitcnt lgkmcnt(7)\n"
    "v_lshlrev_b32 v170, 16, v40\n"
    "v_and_b32 v171, s78, v40\n"
    "v_lshlrev_b32 v172, 16, v41\n"
    "v_and_b32 v173, s78, v41\n"
    "s_waitcnt lgkmcnt(6)\n"
    "v_lshlrev_b32 v174, 16, v42\n"
    "v_and_b32 v175, s78, v42\n"
    "v_lshlrev_b32 v176, 16, v43\n"
    "v_and_b32 v177, s78, v43\n"
    "v_pk_mul_f32 v[170:171], v[170:171], s[92:93] op_sel:[0,1] op_sel_hi:[1,1]\n"
    "v_pk_mul_f32 v[172:173], v[172:173], s[92:93] op_sel:[0,1] op_sel_hi:[1,1]\n"
    "v_pk_mul_f32 v[174:175], v[174:175], s[92:93] op_sel:[0,1] op_sel_hi:[1,1]\n"
    "v_pk_mul_f32 v[176:177], v[176:177], s[92:93] op_sel:[0,1] op_sel_hi:[1,1]\n"
    "v_cvt_pk_bf16_f32 v40, v170, v171\n"
    "v_cvt_pk_bf16_f32 v41, v172, v173\n"
    "v_cvt_pk_bf16_f32 v42, v174, v175\n"
    "v_cvt_pk_bf16_f32 v43, v176, v177\n"
    "v_accvgpr_write_b32 a200, v40\n"
    "v_accvgpr_write_b32 a201, v41\n"
    "v_accvgpr_write_b32 a202, v42\n"
    "v_accvgpr_write_b32 a203, v43\n"
    "s_waitcnt lgkmcnt(5)\n"
    "v_lshlrev_b32 v170, 16, v44\n"
    "v_and_b32 v171, s78, v44\n"
    "v_lshlrev_b32 v172, 16, v45\n"
    "v_and_b32 v173, s78, v45\n"
    "s_waitcnt lgkmcnt(4)\n"
    "v_lshlrev_b32 v174, 16, v46\n"
    "v_and_b32 v175, s78, v46\n"
    "v_lshlrev_b32 v176, 16, v47\n"
    "v_and_b32 v177, s78, v47\n"
    "v_pk_mul_f32 v[170:171], v[170:171], s[92:93] op_sel:[0,1] op_sel_hi:[1,1]\n"
    "v_pk_mul_f32 v[172:173], v[172:173], s[92:93] op_sel:[0,1] op_sel_hi:[1,1]\n"
    "v_pk_mul_f32 v[174:175], v[174:175], s[92:93] op_sel:[0,1] op_sel_hi:[1,1]\n"
    "v_pk_mul_f32 v[176:177], v[176:177], s[92:93] op_sel:[0,1] op_sel_hi:[1,1]\n"
    "v_cvt_pk_bf16_f32 v44, v170, v171\n"
    "v_cvt_pk_bf16_f32 v45, v172, v173\n"
    "v_cvt_pk_bf16_f32 v46, v174, v175\n"
    "v_cvt_pk_bf16_f32 v47, v176, v177\n"
    "v_accvgpr_write_b32 a204, v44\n"
    "v_accvgpr_write_b32 a205, v45\n"
    "v_accvgpr_write_b32 a206, v46\n"
    "v_accvgpr_write_b32 a207, v47\n"
    "s_waitcnt lgkmcnt(3)\n"
    "v_lshlrev_b32 v170, 16, v48\n"
    "v_and_b32 v171, s78, v48\n"
    "v_lshlrev_b32 v172, 16, v49\n"
    "v_and_b32 v173, s78, v49\n"
    "s_waitcnt lgkmcnt(2)\n"
    "v_lshlrev_b32 v174, 16, v50\n"
    "v_and_b32 v175, s78, v50\n"
    "v_lshlrev_b32 v176, 16, v51\n"
    "v_and_b32 v177, s78, v51\n"
    "v_pk_mul_f32 v[170:171], v[170:171], s[92:93] op_sel:[0,1] op_sel_hi:[1,1]\n"
    "v_pk_mul_f32 v[172:173], v[172:173], s[92:93] op_sel:[0,1] op_sel_hi:[1,1]\n"
    "v_pk_mul_f32 v[174:175], v[174:175], s[92:93] op_sel:[0,1] op_sel_hi:[1,1]\n"
    "v_pk_mul_f32 v[176:177], v[176:177], s[92:93] op_sel:[0,1] op_sel_hi:[1,1]\n"
    "v_cvt_pk_bf16_f32 v48, v170, v171\n"
    "v_cvt_pk_bf16_f32 v49, v172, v173\n"
    "v_cvt_pk_bf16_f32 v50, v174, v175\n"
    "v_cvt_pk_bf16_f32 v51, v176, v177\n"
    "v_accvgpr_write_b32 a208, v48\n"
    "v_accvgpr_write_b32 a209, v49\n"
    "v_accvgpr_write_b32 a210, v50\n"
    "v_accvgpr_write_b32 a211, v51\n"
    "s_waitcnt lgkmcnt(1)\n"
    "v_lshlrev_b32 v170, 16, v52\n"
    "v_and_b32 v171, s78, v52\n"
    "v_lshlrev_b32 v172, 16, v53\n"
    "v_and_b32 v173, s78, v53\n"
    "s_waitcnt lgkmcnt(0)\n"
    "v_lshlrev_b32 v174, 16, v54\n"
    "v_and_b32 v175, s78, v54\n"
    "v_lshlrev_b32 v176, 16, v55\n"
    "v_and_b32 v177, s78, v55\n"
    "v_pk_mul_f32 v[170:171], v[170:171], s[92:93] op_sel:[0,1] op_sel_hi:[1,1]\n"
    "v_pk_mul_f32 v[172:173], v[172:173], s[92:93] op_sel:[0,1] op_sel_hi:[1,1]\n"
    "v_pk_mul_f32 v[174:175], v[174:175], s[92:93] op_sel:[0,1] op_sel_hi:[1,1]\n"
    "v_pk_mul_f32 v[176:177], v[176:177], s[92:93] op_sel:[0,1] op_sel_hi:[1,1]\n"
    "v_cvt_pk_bf16_f32 v52, v170, v171\n"
    "v_cvt_pk_bf16_f32 v53, v172, v173\n"
    "v_cvt_pk_bf16_f32 v54, v174, v175\n"
    "v_cvt_pk_bf16_f32 v55, v176, v177\n"
    "v_accvgpr_write_b32 a212, v52\n"
    "v_accvgpr_write_b32 a213, v53\n"
    "v_accvgpr_write_b32 a214, v54\n"
    "v_accvgpr_write_b32 a215, v55\n"
    "buffer_load_dwordx2 v[252:253], v250, s[64:67], s87 offen sc1\n"
    "s_add_u32 m0, s96, 18432\n"
    "s_mov_b32 s92, 18432\n"
    "buffer_load_dwordx4 v157, s[68:71], s92 offen offset:0 nt lds\n"
    "buffer_load_dwordx4 v158, s[68:71], s92 offen offset:1024 nt lds\n"
    "buffer_load_dwordx4 v159, s[68:71], s92 offen offset:2048 nt lds\n"
    "s_waitcnt vmcnt(22)\n"
    "s_add_u32 m0, s96, 21504\n"
    "s_mov_b32 s92, 21504\n"
    "buffer_load_dwordx4 v157, s[68:71], s92 offen offset:0 nt lds\n"
    "buffer_load_dwordx4 v158, s[68:71], s92 offen offset:1024 nt lds\n"
    "buffer_load_dwordx4 v159, s[68:71], s92 offen offset:2048 nt lds\n"
    "s_waitcnt vmcnt(25)\n"
    "ds_read_b64 v[32:33], v162 offset:6144\n"
    "ds_read_b64 v[34:35], v163 offset:6144\n"
    "ds_read_b64 v[36:37], v164 offset:6144\n"
    "ds_read_b64 v[38:39], v165 offset:6144\n"
    "ds_read_b64 v[40:41], v162 offset:6272\n"
    "ds_read_b64 v[42:43], v163 offset:6272\n"
    "ds_read_b64 v[44:45], v164 offset:6272\n"
    "ds_read_b64 v[46:47], v165 offset:6272\n"
    "ds_read_b64 v[48:49], v162 offset:6400\n"
    "ds_read_b64 v[50:51], v163 offset:6400\n"
    "ds_read_b64 v[52:53], v164 offset:6400\n"
    "ds_read_b64 v[54:55], v165 offset:6400\n"
    "s_waitcnt lgkmcnt(11)\n"
    "v_lshlrev_b32 v170, 16, v32\n"
    "v_and_b32 v171, s78, v32\n"
    "v_lshlrev_b32 v172, 16, v33\n"
    "v_and_b32 v173, s78, v33\n"
    "s_waitcnt lgkmcnt(10)\n"
    "v_lshlrev_b32 v174, 16, v34\n"
    "v_and_b32 v175, s78, v34\n"
    "v_lshlrev_b32 v176, 16, v35\n"
    "v_and_b32 v177, s78, v35\n"
    "v_pk_mul_f32 v[170:171], v[170:171], s[92:93] op_sel:[0,1] op_sel_hi:[1,1]\n"
    "v_pk_mul_f32 v[172:173], v[172:173], s[92:93] op_sel:[0,1] op_sel_hi:[1,1]\n"
    "v_pk_mul_f32 v[174:175], v[174:175], s[92:93] op_sel:[0,1] op_sel_hi:[1,1]\n"
    "v_pk_mul_f32 v[176:177], v[176:177], s[92:93] op_sel:[0,1] op_sel_hi:[1,1]\n"
    "v_cvt_pk_bf16_f32 v32, v170, v171\n"
    "v_cvt_pk_bf16_f32 v33, v172, v173\n"
    "v_cvt_pk_bf16_f32 v34, v174, v175\n"
    "v_cvt_pk_bf16_f32 v35, v176, v177\n"
    "v_accvgpr_write_b32 a216, v32\n"
    "v_accvgpr_write_b32 a217, v33\n"
    "v_accvgpr_write_b32 a218, v34\n"
    "v_accvgpr_write_b32 a219, v35\n"
    "s_waitcnt lgkmcnt(9)\n"
    "v_lshlrev_b32 v170, 16, v36\n"
    "v_and_b32 v171, s78, v36\n"
    "v_lshlrev_b32 v172, 16, v37\n"
    "v_and_b32 v173, s78, v37\n"
    "s_waitcnt lgkmcnt(8)\n"
    "v_lshlrev_b32 v174, 16, v38\n"
    "v_and_b32 v175, s78, v38\n"
    "v_lshlrev_b32 v176, 16, v39\n"
    "v_and_b32 v177, s78, v39\n"
    "v_pk_mul_f32 v[170:171], v[170:171], s[92:93] op_sel:[0,1] op_sel_hi:[1,1]\n"
    "v_pk_mul_f32 v[172:173], v[172:173], s[92:93] op_sel:[0,1] op_sel_hi:[1,1]\n"
    "v_pk_mul_f32 v[174:175], v[174:175], s[92:93] op_sel:[0,1] op_sel_hi:[1,1]\n"
    "v_pk_mul_f32 v[176:177], v[176:177], s[92:93] op_sel:[0,1] op_sel_hi:[1,1]\n"
    "v_cvt_pk_bf16_f32 v36, v170, v171\n"
    "v_cvt_pk_bf16_f32 v37, v172, v173\n"
    "v_cvt_pk_bf16_f32 v38, v174, v175\n"
    "v_cvt_pk_bf16_f32 v39, v176, v177\n"
    "v_accvgpr_write_b32 a220, v36\n"
    "v_accvgpr_write_b32 a221, v37\n"
    "v_accvgpr_write_b32 a222, v38\n"
    "v_accvgpr_write_b32 a223, v39\n"
    "s_waitcnt lgkmcnt(7)\n"
    "v_lshlrev_b32 v170, 16, v40\n"
    "v_and_b32 v171, s78, v40\n"
    "v_lshlrev_b32 v172, 16, v41\n"
    "v_and_b32 v173, s78, v41\n"
    "s_waitcnt lgkmcnt(6)\n"
    "v_lshlrev_b32 v174, 16, v42\n"
    "v_and_b32 v175, s78, v42\n"
    "v_lshlrev_b32 v176, 16, v43\n"
    "v_and_b32 v177, s78, v43\n"
    "v_pk_mul_f32 v[170:171], v[170:171], s[92:93] op_sel:[0,1] op_sel_hi:[1,1]\n"
    "v_pk_mul_f32 v[172:173], v[172:173], s[92:93] op_sel:[0,1] op_sel_hi:[1,1]\n"
    "v_pk_mul_f32 v[174:175], v[174:175], s[92:93] op_sel:[0,1] op_sel_hi:[1,1]\n"
    "v_pk_mul_f32 v[176:177], v[176:177], s[92:93] op_sel:[0,1] op_sel_hi:[1,1]\n"
    "v_cvt_pk_bf16_f32 v40, v170, v171\n"
    "v_cvt_pk_bf16_f32 v41, v172, v173\n"
    "v_cvt_pk_bf16_f32 v42, v174, v175\n"
    "v_cvt_pk_bf16_f32 v43, v176, v177\n"
    "v_accvgpr_write_b32 a224, v40\n"
    "v_accvgpr_write_b32 a225, v41\n"
    "v_accvgpr_write_b32 a226, v42\n"
    "v_accvgpr_write_b32 a227, v43\n"
    "s_waitcnt lgkmcnt(5)\n"
    "v_lshlrev_b32 v170, 16, v44\n"
    "v_and_b32 v171, s78, v44\n"
    "v_lshlrev_b32 v172, 16, v45\n"
    "v_and_b32 v173, s78, v45\n"
    "s_waitcnt lgkmcnt(4)\n"
    "v_lshlrev_b32 v174, 16, v46\n"
    "v_and_b32 v175, s78, v46\n"
    "v_lshlrev_b32 v176, 16, v47\n"
    "v_and_b32 v177, s78, v47\n"
    "v_pk_mul_f32 v[170:171], v[170:171], s[92:93] op_sel:[0,1] op_sel_hi:[1,1]\n"
    "v_pk_mul_f32 v[172:173], v[172:173], s[92:93] op_sel:[0,1] op_sel_hi:[1,1]\n"
    "v_pk_mul_f32 v[174:175], v[174:175], s[92:93] op_sel:[0,1] op_sel_hi:[1,1]\n"
    "v_pk_mul_f32 v[176:177], v[176:177], s[92:93] op_sel:[0,1] op_sel_hi:[1,1]\n"
    "v_cvt_pk_bf16_f32 v44, v170, v171\n"
    "v_cvt_pk_bf16_f32 v45, v172, v173\n"
    "v_cvt_pk_bf16_f32 v46, v174, v175\n"
    "v_cvt_pk_bf16_f32 v47, v176, v177\n"
    "v_accvgpr_write_b32 a228, v44\n"
    "v_accvgpr_write_b32 a229, v45\n"
    "v_accvgpr_write_b32 a230, v46\n"
    "v_accvgpr_write_b32 a231, v47\n"
    "s_waitcnt lgkmcnt(3)\n"
    "v_lshlrev_b32 v170, 16, v48\n"
    "v_and_b32 v171, s78, v48\n"
    "v_lshlrev_b32 v172, 16, v49\n"
    "v_and_b32 v173, s78, v49\n"
    "s_waitcnt lgkmcnt(2)\n"
    "v_lshlrev_b32 v174, 16, v50\n"
    "v_and_b32 v175, s78, v50\n"
    "v_lshlrev_b32 v176, 16, v51\n"
    "v_and_b32 v177, s78, v51\n"
    "v_pk_mul_f32 v[170:171], v[170:171], s[92:93] op_sel:[0,1] op_sel_hi:[1,1]\n"
    "v_pk_mul_f32 v[172:173], v[172:173], s[92:93] op_sel:[0,1] op_sel_hi:[1,1]\n"
    "v_pk_mul_f32 v[174:175], v[174:175], s[92:93] op_sel:[0,1] op_sel_hi:[1,1]\n"
    "v_pk_mul_f32 v[176:177], v[176:177], s[92:93] op_sel:[0,1] op_sel_hi:[1,1]\n"
    "v_cvt_pk_bf16_f32 v48, v170, v171\n"
    "v_cvt_pk_bf16_f32 v49, v172, v173\n"
    "v_cvt_pk_bf16_f32 v50, v174, v175\n"
    "v_cvt_pk_bf16_f32 v51, v176, v177\n"
    "v_accvgpr_write_b32 a232, v48\n"
    "v_accvgpr_write_b32 a233, v49\n"
    "v_accvgpr_write_b32 a234, v50\n"
    "v_accvgpr_write_b32 a235, v51\n"
    "s_waitcnt lgkmcnt(1)\n"
    "v_lshlrev_b32 v170, 16, v52\n"
    "v_and_b32 v171, s78, v52\n"
    "v_lshlrev_b32 v172, 16, v53\n"
    "v_and_b32 v173, s78, v53\n"
    "s_waitcnt lgkmcnt(0)\n"
    "v_lshlrev_b32 v174, 16, v54\n"
    "v_and_b32 v175, s78, v54\n"
    "v_lshlrev_b32 v176, 16, v55\n"
    "v_and_b32 v177, s78, v55\n"
    "v_pk_mul_f32 v[170:171], v[170:171], s[92:93] op_sel:[0,1] op_sel_hi:[1,1]\n"
    "v_pk_mul_f32 v[172:173], v[172:173], s[92:93] op_sel:[0,1] op_sel_hi:[1,1]\n"
    "v_pk_mul_f32 v[174:175], v[174:175], s[92:93] op_sel:[0,1] op_sel_hi:[1,1]\n"
    "v_pk_mul_f32 v[176:177], v[176:177], s[92:93] op_sel:[0,1] op_sel_hi:[1,1]\n"
    "v_cvt_pk_bf16_f32 v52, v170, v171\n"
    "v_cvt_pk_bf16_f32 v53, v172, v173\n"
    "v_cvt_pk_bf16_f32 v54, v174, v175\n"
    "v_cvt_pk_bf16_f32 v55, v176, v177\n"
    "v_accvgpr_write_b32 a236, v52\n"
    "v_accvgpr_write_b32 a237, v53\n"
    "v_accvgpr_write_b32 a238, v54\n"
    "v_accvgpr_write_b32 a239, v55\n"
    "s_waitcnt vmcnt(13)\n"
    "s_bitcmp1_b32 s75, 2\n"
    "s_cbranch_scc1 FVP2%=\n"
    "v_cvt_f32_i32 v120, v120\n"
    "v_cvt_f32_i32 v121, v121\n"
    "v_cvt_f32_i32 v128, v128\n"
    "v_cvt_f32_i32 v129, v129\n"
    "v_lshlrev_b32 v246, 16, v140\n"
    "v_and_b32 v140, s78, v140\n"
    "v_lshlrev_b32 v247, 16, v136\n"
    "v_and_b32 v136, s78, v136\n"
    "v_mul_f32 v247, v247, v246\n"
    "v_mul_f32 v136, v136, v140\n"
    "v_fma_f32 v120, v120, v246, -v247\n"
    "v_fma_f32 v121, v121, v140, -v136\n"
    "v_fma_f32 v128, v128, v246, -v247\n"
    "v_fma_f32 v129, v129, v140, -v136\n"
    "v_cvt_pk_bf16_f32 v198, v120, v121\n"
    "v_cvt_pk_bf16_f32 v202, v128, v129\n"
    "v_cvt_f32_i32 v122, v122\n"
    "v_cvt_f32_i32 v123, v123\n"
    "v_cvt_f32_i32 v130, v130\n"
    "v_cvt_f32_i32 v131, v131\n"
    "v_lshlrev_b32 v246, 16, v141\n"
    "v_and_b32 v141, s78, v141\n"
    "v_lshlrev_b32 v247, 16, v137\n"
    "v_and_b32 v137, s78, v137\n"
    "v_mul_f32 v247, v247, v246\n"
    "v_mul_f32 v137, v137, v141\n"
    "v_fma_f32 v122, v122, v246, -v247\n"
    "v_fma_f32 v123, v123, v141, -v137\n"
    "v_fma_f32 v130, v130, v246, -v247\n"
    "v_fma_f32 v131, v131, v141, -v137\n"
    "v_cvt_pk_bf16_f32 v199, v122, v123\n"
    "v_cvt_pk_bf16_f32 v203, v130, v131\n"
    "v_cvt_f32_i32 v124, v124\n"
    "v_cvt_f32_i32 v125, v125\n"
    "v_cvt_f32_i32 v132, v132\n"
    "v_cvt_f32_i32 v133, v133\n"
    "v_lshlrev_b32 v246, 16, v142\n"
    "v_and_b32 v142, s78, v142\n"
    "v_lshlrev_b32 v247, 16, v138\n"
    "v_and_b32 v138, s78, v138\n"
    "v_mul_f32 v247, v247, v246\n"
    "v_mul_f32 v138, v138, v142\n"
    "v_fma_f32 v124, v124, v246, -v247\n"
    "v_fma_f32 v125, v125, v142, -v138\n"
    "v_fma_f32 v132, v132, v246, -v247\n"
    "v_fma_f32 v133, v133, v142, -v138\n"
    "v_cvt_pk_bf16_f32 v200, v124, v125\n"
    "v_cvt_pk_bf16_f32 v204, v132, v133\n"
    "v_cvt_f32_i32 v126, v126\n"
    "v_cvt_f32_i32 v127, v127\n"
    "v_cvt_f32_i32 v134, v134\n"
    "v_cvt_f32_i32 v135, v135\n"
    "v_lshlrev_b32 v246, 16, v143\n"
    "v_and_b32 v143, s78, v143\n"
    "v_lshlrev_b32 v247, 16, v139\n"
    "v_and_b32 v139, s78, v139\n"
    "v_mul_f32 v247, v247, v246\n"
    "v_mul_f32 v139, v139, v143\n"
    "v_fma_f32 v126, v126, v246, -v247\n"
    "v_fma_f32 v127, v127, v143, -v139\n"
    "v_fma_f32 v134, v134, v246, -v247\n"
    "v_fma_f32 v135, v135, v143, -v139\n"
    "v_cvt_pk_bf16_f32 v201, v126, v127\n"
    "v_cvt_pk_bf16_f32 v205, v134, v135\n"
    "buffer_store_dwordx4 v[198:201], v149, s[56:59], s74 offen offset:0 sc1\n"
    "buffer_store_dwordx4 v[202:205], v149, s[56:59], s74 offen offset:1024 sc1\n"
    "s_branch FJP2%=\n"
    "FVP2%=:\n"
    "v_cvt_f32_i32 v120, v120\n"
    "v_cvt_f32_i32 v122, v122\n"
    "v_cvt_f32_i32 v121, v121\n"
    "v_cvt_f32_i32 v123, v123\n"
    "v_lshlrev_b32 v246, 16, v140\n"
    "v_and_b32 v140, s78, v140\n"
    "v_lshlrev_b32 v247, 16, v136\n"
    "v_and_b32 v136, s78, v136\n"
    "v_mul_f32 v247, v247, v246\n"
    "v_mul_f32 v136, v136, v140\n"
    "v_fma_f32 v120, v120, v246, -v247\n"
    "v_fma_f32 v122, v122, v140, -v136\n"
    "v_fma_f32 v121, v121, v246, -v247\n"
    "v_fma_f32 v123, v123, v140, -v136\n"
    "v_cvt_pk_bf16_f32 v198, v120, v122\n"
    "v_cvt_pk_bf16_f32 v202, v121, v123\n"
    "v_cvt_f32_i32 v124, v124\n"
    "v_cvt_f32_i32 v126, v126\n"
    "v_cvt_f32_i32 v125, v125\n"
    "v_cvt_f32_i32 v127, v127\n"
    "v_lshlrev_b32 v246, 16, v141\n"
    "v_and_b32 v141, s78, v141\n"
    "v_lshlrev_b32 v247, 16, v137\n"
    "v_and_b32 v137, s78, v137\n"
    "v_mul_f32 v247, v247, v246\n"
    "v_mul_f32 v137, v137, v141\n"
    "v_fma_f32 v124, v124, v246, -v247\n"
    "v_fma_f32 v126, v126, v141, -v137\n"
    "v_fma_f32 v125, v125, v246, -v247\n"
    "v_fma_f32 v127, v127, v141, -v137\n"
    "v_cvt_pk_bf16_f32 v199, v124, v126\n"
    "v_cvt_pk_bf16_f32 v203, v125, v127\n"
    "v_cvt_f32_i32 v128, v128\n"
    "v_cvt_f32_i32 v130, v130\n"
    "v_cvt_f32_i32 v129, v129\n"
    "v_cvt_f32_i32 v131, v131\n"
    "v_lshlrev_b32 v246, 16, v142\n"
    "v_and_b32 v142, s78, v142\n"
    "v_lshlrev_b32 v247, 16, v138\n"
    "v_and_b32 v138, s78, v138\n"
    "v_mul_f32 v247, v247, v246\n"
    "v_mul_f32 v138, v138, v142\n"
    "v_fma_f32 v128, v128, v246, -v247\n"
    "v_fma_f32 v130, v130, v142, -v138\n"
    "v_fma_f32 v129, v129, v246, -v247\n"
    "v_fma_f32 v131, v131, v142, -v138\n"
    "v_cvt_pk_bf16_f32 v200, v128, v130\n"
    "v_cvt_pk_bf16_f32 v204, v129, v131\n"
    "v_cvt_f32_i32 v132, v132\n"
    "v_cvt_f32_i32 v134, v134\n"
    "v_cvt_f32_i32 v133, v133\n"
    "v_cvt_f32_i32 v135, v135\n"
    "v_lshlrev_b32 v246, 16, v143\n"
    "v_and_b32 v143, s78, v143\n"
    "v_lshlrev_b32 v247, 16, v139\n"
    "v_and_b32 v139, s78, v139\n"
    "v_mul_f32 v247, v247, v246\n"
    "v_mul_f32 v139, v139, v143\n"
    "v_fma_f32 v132, v132, v246, -v247\n"
    "v_fma_f32 v134, v134, v143, -v139\n"
    "v_fma_f32 v133, v133, v246, -v247\n"
    "v_fma_f32 v135, v135, v143, -v139\n"
    "v_cvt_pk_bf16_f32 v201, v132, v134\n"
    "v_cvt_pk_bf16_f32 v205, v133, v135\n"
    "buffer_store_dwordx4 v[198:201], v149, s[60:63], s74 offen offset:0 sc1\n"
    "buffer_store_dwordx4 v[202:205], v149, s[60:63], s74 offen offset:1024 sc1\n"
    "FJP2%=:\n"
    "s_waitcnt vmcnt(8)\n"
    "v_cmp_ne_u64 vcc, s[80:81], v[252:253]\n"
    "s_nop 3\n"
    "s_cbranch_vccnz SPA%=\n"
    "RTA%=:\n"
    "s_mov_b32 s87, 256\n"
    "s_mul_i32 s89, %11, 6144\n"
    "s_add_u32 s88, s89, 0\n"
    "s_add_u32 m0, s89, 0\n"
    "s_nop 0\n"
    "buffer_load_dwordx4 v149, s[56:59], s88 offen offset:0 sc1 lds\n"
    "buffer_load_dwordx4 v149, s[56:59], s88 offen offset:1024 sc1 lds\n"
    "buffer_load_dwordx4 v149, s[56:59], s88 offen offset:2048 sc1 lds\n"
    "buffer_load_dwordx4 v149, s[56:59], s88 offen offset:3072 sc1 lds\n"
    "s_add_u32 m0, m0, 4096\n"
    "s_add_u32 s88, s88, 4096\n"
    "buffer_load_dwordx4 v149, s[56:59], s88 offen offset:0 sc1 lds\n"
    "buffer_load_dwordx4 v149, s[56:59], s88 offen offset:1024 sc1 lds\n"
    "s_mul_i32 s89, %11, 6144\n"
    "s_add_u32 s88, s89, 24576\n"
    "s_add_u32 m0, s89, 49152\n"
    "s_nop 0\n"
    "buffer_load_dwordx4 v149, s[56:59], s88 offen offset:0 sc1 lds\n"
    "buffer_load_dwordx4 v149, s[56:59], s88 offen offset:1024 sc1 lds\n"
    "buffer_load_dwordx4 v149, s[56:59], s88 offen offset:2048 sc1 lds\n"
    "buffer_load_dwordx4 v149, s[56:59], s88 offen offset:3072 sc1 lds\n"
    "s_add_u32 m0, m0, 4096\n"
    "s_add_u32 s88, s88, 4096\n"
    "buffer_load_dwordx4 v149, s[56:59], s88 offen offset:0 sc1 lds\n"
    "buffer_load_dwordx4 v149, s[56:59], s88 offen offset:1024 sc1 lds\n"
    "s_waitcnt vmcnt(21)\n"
    "ds_read_b64 v[32:33], v162 offset:12288\n"
    "ds_read_b64 v[34:35], v163 offset:12288\n"
    "ds_read_b64 v[36:37], v164 offset:12288\n"
    "ds_read_b64 v[38:39], v165 offset:12288\n"
    "ds_read_b64 v[40:41], v162 offset:12416\n"
    "ds_read_b64 v[42:43], v163 offset:12416\n"
    "ds_read_b64 v[44:45], v164 offset:12416\n"
    "ds_read_b64 v[46:47], v165 offset:12416\n"
    "ds_read_b64 v[48:49], v162 offset:12544\n"
    "ds_read_b64 v[50:51], v163 offset:12544\n"
    "ds_read_b64 v[52:53], v164 offset:12544\n"
    "ds_read_b64 v[54:55], v165 offset:12544\n"
    "s_waitcnt lgkmcnt(11)\n"
    "v_lshlrev_b32 v170, 16, v32\n"
    "v_and_b32 v171, s78, v32\n"
    "v_lshlrev_b32 v172, 16, v33\n"
    "v_and_b32 v173, s78, v33\n"
    "s_waitcnt lgkmcnt(10)\n"
    "v_lshlrev_b32 v174, 16, v34\n"
    "v_and_b32 v175, s78, v34\n"
    "v_lshlrev_b32 v176, 16, v35\n"
    "v_and_b32 v177, s78, v35\n"
    "v_pk_mul_f32 v[170:171], v[170:171], s[92:93] op_sel:[0,1] op_sel_hi:[1,1]\n"
    "v_pk_mul_f32 v[172:173], v[172:173], s[92:93] op_sel:[0,1] op_sel_hi:[1,1]\n"
    "v_pk_mul_f32 v[174:175], v[174:175], s[92:93] op_sel:[0,1] op_sel_hi:[1,1]\n"
    "v_pk_mul_f32 v[176:177], v[176:177], s[92:93] op_sel:[0,1] op_sel_hi:[1,1]\n"
    "v_cvt_pk_bf16_f32 v32, v170, v171\n"
    "v_cvt_pk_bf16_f32 v33, v172, v173\n"
    "v_cvt_pk_bf16_f32 v34, v174, v175\n"
    "v_cvt_pk_bf16_f32 v35, v176, v177\n"
    "v_accvgpr_write_b32 a240, v32\n"
    "v_accvgpr_write_b32 a241, v33\n"
    "v_accvgpr_write_b32 a242, v34\n"
    "v_accvgpr_write_b32 a243, v35\n"
    "s_waitcnt lgkmcnt(9)\n"
    "v_lshlrev_b32 v170, 16, v36\n"
    "v_and_b32 v171, s78, v36\n"
    "v_lshlrev_b32 v172, 16, v37\n"
    "v_and_b32 v173, s78, v37\n"
    "s_waitcnt lgkmcnt(8)\n"
    "v_lshlrev_b32 v174, 16, v38\n"
    "v_and_b32 v175, s78, v38\n"
    "v_lshlrev_b32 v176, 16, v39\n"
    "v_and_b32 v177, s78, v39\n"
    "v_pk_mul_f32 v[170:171], v[170:171], s[92:93] op_sel:[0,1] op_sel_hi:[1,1]\n"
    "v_pk_mul_f32 v[172:173], v[172:173], s[92:93] op_sel:[0,1] op_sel_hi:[1,1]\n"
    "v_pk_mul_f32 v[174:175], v[174:175], s[92:93] op_sel:[0,1] op_sel_hi:[1,1]\n"
    "v_pk_mul_f32 v[176:177], v[176:177], s[92:93] op_sel:[0,1] op_sel_hi:[1,1]\n"
    "v_cvt_pk_bf16_f32 v36, v170, v171\n"
    "v_cvt_pk_bf16_f32 v37, v172, v173\n"
    "v_cvt_pk_bf16_f32 v38, v174, v175\n"
    "v_cvt_pk_bf16_f32 v39, v176, v177\n"
    "v_accvgpr_write_b32 a244, v36\n"
    "v_accvgpr_write_b32 a245, v37\n"
    "v_accvgpr_write_b32 a246, v38\n"
    "v_accvgpr_write_b32 a247, v39\n"
    "s_waitcnt lgkmcnt(7)\n"
    "v_lshlrev_b32 v170, 16, v40\n"
    "v_and_b32 v171, s78, v40\n"
    "v_lshlrev_b32 v172, 16, v41\n"
    "v_and_b32 v173, s78, v41\n"
    "s_waitcnt lgkmcnt(6)\n"
    "v_lshlrev_b32 v174, 16, v42\n"
    "v_and_b32 v175, s78, v42\n"
    "v_lshlrev_b32 v176, 16, v43\n"
    "v_and_b32 v177, s78, v43\n"
    "v_pk_mul_f32 v[170:171], v[170:171], s[92:93] op_sel:[0,1] op_sel_hi:[1,1]\n"
    "v_pk_mul_f32 v[172:173], v[172:173], s[92:93] op_sel:[0,1] op_sel_hi:[1,1]\n"
    "v_pk_mul_f32 v[174:175], v[174:175], s[92:93] op_sel:[0,1] op_sel_hi:[1,1]\n"
    "v_pk_mul_f32 v[176:177], v[176:177], s[92:93] op_sel:[0,1] op_sel_hi:[1,1]\n"
    "v_cvt_pk_bf16_f32 v40, v170, v171\n"
    "v_cvt_pk_bf16_f32 v41, v172, v173\n"
    "v_cvt_pk_bf16_f32 v42, v174, v175\n"
    "v_cvt_pk_bf16_f32 v43, v176, v177\n"
    "v_accvgpr_write_b32 a248, v40\n"
    "v_accvgpr_write_b32 a249, v41\n"
    "v_accvgpr_write_b32 a250, v42\n"
    "v_accvgpr_write_b32 a251, v43\n"
    "s_waitcnt lgkmcnt(5)\n"
    "v_lshlrev_b32 v170, 16, v44\n"
    "v_and_b32 v171, s78, v44\n"
    "v_lshlrev_b32 v172, 16, v45\n"
    "v_and_b32 v173, s78, v45\n"
    "s_waitcnt lgkmcnt(4)\n"
    "v_lshlrev_b32 v174, 16, v46\n"
    "v_and_b32 v175, s78, v46\n"
    "v_lshlrev_b32 v176, 16, v47\n"
    "v_and_b32 v177, s78, v47\n"
    "v_pk_mul_f32 v[170:171], v[170:171], s[92:93] op_sel:[0,1] op_sel_hi:[1,1]\n"
    "v_pk_mul_f32 v[172:173], v[172:173], s[92:93] op_sel:[0,1] op_sel_hi:[1,1]\n"
    "v_pk_mul_f32 v[174:175], v[174:175], s[92:93] op_sel:[0,1] op_sel_hi:[1,1]\n"
    "v_pk_mul_f32 v[176:177], v[176:177], s[92:93] op_sel:[0,1] op_sel_hi:[1,1]\n"
    "v_cvt_pk_bf16_f32 v44, v170, v171\n"
    "v_cvt_pk_bf16_f32 v45, v172, v173\n"
    "v_cvt_pk_bf16_f32 v46, v174, v175\n"
    "v_cvt_pk_bf16_f32 v47, v176, v177\n"
    "v_accvgpr_write_b32 a252, v44\n"
    "v_accvgpr_write_b32 a253, v45\n"
    "v_accvgpr_write_b32 a254, v46\n"
    "v_accvgpr_write_b32 a255, v47\n"
    "s_waitcnt lgkmcnt(3)\n"
    "v_lshlrev_b32 v170, 16, v48\n"
    "v_and_b32 v171, s78, v48\n"
    "v_lshlrev_b32 v172, 16, v49\n"
    "v_and_b32 v173, s78, v49\n"
    "s_waitcnt lgkmcnt(2)\n"
    "v_lshlrev_b32 v174, 16, v50\n"
    "v_and_b32 v175, s78, v50\n"
    "v_lshlrev_b32 v176, 16, v51\n"
    "v_and_b32 v177, s78, v51\n"
    "v_pk_mul_f32 v[170:171], v[170:171], s[92:93] op_sel:[0,1] op_sel_hi:[1,1]\n"
    "v_pk_mul_f32 v[172:173], v[172:173], s[92:93] op_sel:[0,1] op_sel_hi:[1,1]\n"
    "v_pk_mul_f32 v[174:175], v[174:175], s[92:93] op_sel:[0,1] op_sel_hi:[1,1]\n"
    "v_pk_mul_f32 v[176:177], v[176:177], s[92:93] op_sel:[0,1] op_sel_hi:[1,1]\n"
    "v_cvt_pk_bf16_f32 v0, v170, v171\n"
    "v_cvt_pk_bf16_f32 v1, v172, v173\n"
    "v_cvt_pk_bf16_f32 v2, v174, v175\n"
    "v_cvt_pk_bf16_f32 v3, v176, v177\n"
    "s_waitcnt lgkmcnt(1)\n"
    "v_lshlrev_b32 v170, 16, v52\n"
    "v_and_b32 v171, s78, v52\n"
    "v_lshlrev_b32 v172, 16, v53\n"
    "v_and_b32 v173, s78, v53\n"
    "s_waitcnt lgkmcnt(0)\n"
    "v_lshlrev_b32 v174, 16, v54\n"
    "v_and_b32 v175, s78, v54\n"
    "v_lshlrev_b32 v176, 16, v55\n"
    "v_and_b32 v177, s78, v55\n"
    "v_pk_mul_f32 v[170:171], v[170:171], s[92:93] op_sel:[0,1] op_sel_hi:[1,1]\n"
    "v_pk_mul_f32 v[172:173], v[172:173], s[92:93] op_sel:[0,1] op_sel_hi:[1,1]\n"
    "v_pk_mul_f32 v[174:175], v[174:175], s[92:93] op_sel:[0,1] op_sel_hi:[1,1]\n"
    "v_pk_mul_f32 v[176:177], v[176:177], s[92:93] op_sel:[0,1] op_sel_hi:[1,1]\n"
    "v_cvt_pk_bf16_f32 v4, v170, v171\n"
    "v_cvt_pk_bf16_f32 v5, v172, v173\n"
    "v_cvt_pk_bf16_f32 v6, v174, v175\n"
    "v_cvt_pk_bf16_f32 v7, v176, v177\n"
    "s_waitcnt vmcnt(12)\n"
    "buffer_store_dwordx2 v[182:183], off, s[64:67], s86 offset:512 sc1\n"
    "buffer_load_dwordx2 v[252:253], v250, s[64:67], s87 offen sc1\n"
    "s_add_u32 s86, s86, 768\n"
    "s_cmp_ge_u32 %11, 2\n"
    "s_cselect_b64 s[88:89], s[60:61], s[56:57]\n"
    "s_cselect_b32 s91, 24576, 0\n"
    "s_sub_u32 s88, s88, s91\n"
    "s_subb_u32 s89, s89, 0\n"
    "s_add_u32 s90, s91, 786432\n"
    "s_mul_i32 s91, s97, 12288\n"
    "s_add_u32 s88, s88, s91\n"
    "s_addc_u32 s89, s89, 0\n"
    "s_sub_u32 s90, s90, s91\n"
    "s_mul_i32 s75, %11, 12288\n"
    "s_mov_b32 s73, 0xfffe8000\n"
    "s_mov_b32 s72, 49152\n"
    "s_add_u32 s75, s75, 98304\n"
    "s_mov_b32 s74, s75\n"
    "s_and_b32 s84, %12, 3\n"
    "s_add_u32 s84, s84, 4\n"
    "s_mul_i32 s85, s84, 24576\n"
    "s_mul_i32 s91, %11, 6144\n"
    "s_add_u32 s85, s85, s91\n"
    "s_lshl_b32 s84, s84, 1\n"
    "s_add_u32 s84, s84, s79\n"
    "s_mul_i32 s82, s84, 24576\n"
    "s_cmp_ge_u32 %12, 4\n"
    "s_cbranch_scc1 PROV%=\n"
    "s_mul_i32 s91, s97, 3072\n"
    "s_add_u32 s82, s82, s91\n"
    "s_add_u32 s83, s82, 12288\n"
    "s_mul_i32 s84, s84, 384\n"
    "v_mov_b32 v248, v178\n"
    "v_mov_b32 v249, v179\n"
    "s_mov_b64 s[52:53], s[56:57]\n"
    "s_mov_b64 s[54:55], s[58:59]\n"
    "s_mov_b64 s[36:37], s[88:89]\n"
    "s_mov_b32 s38, s90\n"
    "s_mov_b32 s39, 0x20000\n"
    "buffer_load_dwordx4 v[190:193], v248, s[40:43], s82 offen offset:0 nt\n"
    "buffer_load_dwordx4 v[194:197], v248, s[40:43], s82 offen offset:128 nt\n"
    "buffer_load_dwordx4 v[198:201], v248, s[40:43], s83 offen offset:0 nt\n"
    "buffer_load_dwordx4 v[202:205], v248, s[40:43], s83 offen offset:128 nt\n"
    "buffer_load_dwordx2 v[206:207], v249, s[44:47], s84 offen offset:0 nt\n"
    "buffer_load_dwordx2 v[208:209], v249, s[44:47], s84 offen offset:64 nt\n"
    "buffer_load_dwordx2 v[210:211], v249, s[48:51], s84 offen offset:0 nt\n"
    "buffer_load_dwordx2 v[212:213], v249, s[48:51], s84 offen offset:64 nt\n"
    "s_waitcnt vmcnt(24)\n"
    "ds_read_b64 v[32:33], v162 offset:18432\n"
    "ds_read_b64 v[34:35], v163 offset:18432\n"
    "ds_read_b64 v[36:37], v164 offset:18432\n"
    "ds_read_b64 v[38:39], v165 offset:18432\n"
    "ds_read_b64 v[40:41], v162 offset:18560\n"
    "ds_read_b64 v[42:43], v163 offset:18560\n"
    "ds_read_b64 v[44:45], v164 offset:18560\n"
    "ds_read_b64 v[46:47], v165 offset:18560\n"
    "ds_read_b64 v[48:49], v162 offset:18688\n"
    "ds_read_b64 v[50:51], v163 offset:18688\n"
    "ds_read_b64 v[52:53], v164 offset:18688\n"
    "ds_read_b64 v[54:55], v165 offset:18688\n"
    "s_waitcnt lgkmcnt(11)\n"
    "v_lshlrev_b32 v170, 16, v32\n"
    "v_and_b32 v171, s78, v32\n"
    "v_lshlrev_b32 v172, 16, v33\n"
    "v_and_b32 v173, s78, v33\n"
    "s_waitcnt lgkmcnt(10)\n"
    "v_lshlrev_b32 v174, 16, v34\n"
    "v_and_b32 v175, s78, v34\n"
    "v_lshlrev_b32 v176, 16, v35\n"
    "v_and_b32 v177, s78, v35\n"
    "v_pk_mul_f32 v[170:171], v[170:171], s[92:93] op_sel:[0,1] op_sel_hi:[1,1]\n"
    "v_pk_mul_f32 v[172:173], v[172:173], s[92:93] op_sel:[0,1] op_sel_hi:[1,1]\n"
    "v_pk_mul_f32 v[174:175], v[174:175], s[92:93] op_sel:[0,1] op_sel_hi:[1,1]\n"
    "v_pk_mul_f32 v[176:177], v[176:177], s[92:93] op_sel:[0,1] op_sel_hi:[1,1]\n"
    "v_cvt_pk_bf16_f32 v8, v170, v171\n"
    "v_cvt_pk_bf16_f32 v9, v172, v173\n"
    "v_cvt_pk_bf16_f32 v10, v174, v175\n"
    "v_cvt_pk_bf16_f32 v11, v176, v177\n"
    "s_waitcnt lgkmcnt(9)\n"
    "v_lshlrev_b32 v170, 16, v36\n"
    "v_and_b32 v171, s78, v36\n"
    "v_lshlrev_b32 v172, 16, v37\n"
    "v_and_b32 v173, s78, v37\n"
    "s_waitcnt lgkmcnt(8)\n"
    "v_lshlrev_b32 v174, 16, v38\n"
    "v_and_b32 v175, s78, v38\n"
    "v_lshlrev_b32 v176, 16, v39\n"
    "v_and_b32 v177, s78, v39\n"
    "v_pk_mul_f32 v[170:171], v[170:171], s[92:93] op_sel:[0,1] op_sel_hi:[1,1]\n"
    "v_pk_mul_f32 v[172:173], v[172:173], s[92:93] op_sel:[0,1] op_sel_hi:[1,1]\n"
    "v_pk_mul_f32 v[174:175], v[174:175], s[92:93] op_sel:[0,1] op_sel_hi:[1,1]\n"
    "v_pk_mul_f32 v[176:177], v[176:177], s[92:93] op_sel:[0,1] op_sel_hi:[1,1]\n"
    "v_cvt_pk_bf16_f32 v12, v170, v171\n"
    "v_cvt_pk_bf16_f32 v13, v172, v173\n"
    "v_cvt_pk_bf16_f32 v14, v174, v175\n"
    "v_cvt_pk_bf16_f32 v15, v176, v177\n"
    "s_waitcnt lgkmcnt(7)\n"
    "v_lshlrev_b32 v170, 16, v40\n"
    "v_and_b32 v171, s78, v40\n"
    "v_lshlrev_b32 v172, 16, v41\n"
    "v_and_b32 v173, s78, v41\n"
    "s_waitcnt lgkmcnt(6)\n"
    "v_lshlrev_b32 v174, 16, v42\n"
    "v_and_b32 v175, s78, v42\n"
    "v_lshlrev_b32 v176, 16, v43\n"
    "v_and_b32 v177, s78, v43\n"
    "v_pk_mul_f32 v[170:171], v[170:171], s[92:93] op_sel:[0,1] op_sel_hi:[1,1]\n"
    "v_pk_mul_f32 v[172:173], v[172:173], s[92:93] op_sel:[0,1] op_sel_hi:[1,1]\n"
    "v_pk_mul_f32 v[174:175], v[174:175], s[92:93] op_sel:[0,1] op_sel_hi:[1,1]\n"
    "v_pk_mul_f32 v[176:177], v[176:177], s[92:93] op_sel:[0,1] op_sel_hi:[1,1]\n"
    "v_cvt_pk_bf16_f32 v16, v170, v171\n"
    "v_cvt_pk_bf16_f32 v17, v172, v173\n"
    "v_cvt_pk_bf16_f32 v18, v174, v175\n"
    "v_cvt_pk_bf16_f32 v19, v176, v177\n"
    "s_waitcnt lgkmcnt(5)\n"
    "v_lshlrev_b32 v170, 16, v44\n"
    "v_and_b32 v171, s78, v44\n"
    "v_lshlrev_b32 v172, 16, v45\n"
    "v_and_b32 v173, s78, v45\n"
    "s_waitcnt lgkmcnt(4)\n"
    "v_lshlrev_b32 v174, 16, v46\n"
    "v_and_b32 v175, s78, v46\n"
    "v_lshlrev_b32 v176, 16, v47\n"
    "v_and_b32 v177, s78, v47\n"
    "v_pk_mul_f32 v[170:171], v[170:171], s[92:93] op_sel:[0,1] op_sel_hi:[1,1]\n"
    "v_pk_mul_f32 v[172:173], v[172:173], s[92:93] op_sel:[0,1] op_sel_hi:[1,1]\n"
    "v_pk_mul_f32 v[174:175], v[174:175], s[92:93] op_sel:[0,1] op_sel_hi:[1,1]\n"
    "v_pk_mul_f32 v[176:177], v[176:177], s[92:93] op_sel:[0,1] op_sel_hi:[1,1]\n"
    "v_cvt_pk_bf16_f32 v20, v170, v171\n"
    "v_cvt_pk_bf16_f32 v21, v172, v173\n"
    "v_cvt_pk_bf16_f32 v22, v174, v175\n"
    "v_cvt_pk_bf16_f32 v23, v176, v177\n"
    "s_waitcnt lgkmcnt(3)\n"
    "v_lshlrev_b32 v170, 16, v48\n"
    "v_and_b32 v171, s78, v48\n"
    "v_lshlrev_b32 v172, 16, v49\n"
    "v_and_b32 v173, s78, v49\n"
    "s_waitcnt lgkmcnt(2)\n"
    "v_lshlrev_b32 v174, 16, v50\n"
    "v_and_b32 v175, s78, v50\n"
    "v_lshlrev_b32 v176, 16, v51\n"
    "v_and_b32 v177, s78, v51\n"
    "v_pk_mul_f32 v[170:171], v[170:171], s[92:93] op_sel:[0,1] op_sel_hi:[1,1]\n"
    "v_pk_mul_f32 v[172:173], v[172:173], s[92:93] op_sel:[0,1] op_sel_hi:[1,1]\n"
    "v_pk_mul_f32 v[174:175], v[174:175], s[92:93] op_sel:[0,1] op_sel_hi:[1,1]\n"
    "v_pk_mul_f32 v[176:177], v[176:177], s[92:93] op_sel:[0,1] op_sel_hi:[1,1]\n"
    "v_cvt_pk_bf16_f32 v24, v170, v171\n"
    "v_cvt_pk_bf16_f32 v25, v172, v173\n"
    "v_cvt_pk_bf16_f32 v26, v174, v175\n"
    "v_cvt_pk_bf16_f32 v27, v176, v177\n"
    "s_waitcnt lgkmcnt(1)\n"
    "v_lshlrev_b32 v170, 16, v52\n"
    "v_and_b32 v171, s78, v52\n"
    "v_lshlrev_b32 v172, 16, v53\n"
    "v_and_b32 v173, s78, v53\n"
    "s_waitcnt lgkmcnt(0)\n"
    "v_lshlrev_b32 v174, 16, v54\n"
    "v_and_b32 v175, s78, v54\n"
    "v_lshlrev_b32 v176, 16, v55\n"
    "v_and_b32 v177, s78, v55\n"
    "v_pk_mul_f32 v[170:171], v[170:171], s[92:93] op_sel:[0,1] op_sel_hi:[1,1]\n"
    "v_pk_mul_f32 v[172:173], v[172:173], s[92:93] op_sel:[0,1] op_sel_hi:[1,1]\n"
    "v_pk_mul_f32 v[174:175], v[174:175], s[92:93] op_sel:[0,1] op_sel_hi:[1,1]\n"
    "v_pk_mul_f32 v[176:177], v[176:177], s[92:93] op_sel:[0,1] op_sel_hi:[1,1]\n"
    "v_cvt_pk_bf16_f32 v28, v170, v171\n"
    "v_cvt_pk_bf16_f32 v29, v172, v173\n"
    "v_cvt_pk_bf16_f32 v30, v174, v175\n"
    "v_cvt_pk_bf16_f32 v31, v176, v177\n"
    "buffer_load_dwordx4 v[214:217], v248, s[40:43], s82 offen offset:256 nt\n"
    "buffer_load_dwordx4 v[218:221], v248, s[40:43], s82 offen offset:384 nt\n"
    "buffer_load_dwordx4 v[222:225], v248, s[40:43], s83 offen offset:256 nt\n"
    "buffer_load_dwordx4 v[226:229], v248, s[40:43], s83 offen offset:384 nt\n"
    "buffer_load_dwordx2 v[230:231], v249, s[44:47], s84 offen offset:128 nt\n"
    "buffer_load_dwordx2 v[232:233], v249, s[44:47], s84 offen offset:192 nt\n"
    "buffer_load_dwordx2 v[234:235], v249, s[48:51], s84 offen offset:128 nt\n"
    "buffer_load_dwordx2 v[236:237], v249, s[48:51], s84 offen offset:192 nt\n"
    "s_waitcnt vmcnt(18)\n"
    "s_waitcnt lgkmcnt(0)\n"
    "s_barrier\n"
    "ds_read_b128 v[150:153], v254 offset:0\n"
    "ds_read_b128 v[154:157], v251 offset:0\n"
    "ds_read_b128 v[158:161], v254 offset:2048\n"
    "ds_read_b128 v[162:165], v251 offset:2048\n"
    "ds_read_b128 v[166:169], v254 offset:4096\n"
    "ds_read_b128 v[170:173], v251 offset:4096\n"
    "ds_read_b128 v[174:177], v254 offset:6144\n"
    "s_waitcnt lgkmcnt(6)\n"
    "v_mfma_f32_16x16x32_bf16 v[64:67], v[150:153], a[192:195], 0\n"
    "s_waitcnt lgkmcnt(5)\n"
    "v_mfma_f32_16x16x32_bf16 v[64:67], v[154:157], a[196:199], v[64:67]\n"
    "s_waitcnt lgkmcnt(4)\n"
    "v_mfma_f32_16x16x32_bf16 v[64:67], v[158:161], a[200:203], v[64:67]\n"
    "s_waitcnt lgkmcnt(3)\n"
    "v_mfma_f32_16x16x32_bf16 v[64:67], v[162:165], a[204:207], v[64:67]\n"
    "s_waitcnt lgkmcnt(2)\n"
    "v_mfma_f32_16x16x32_bf16 v[64:67], v[166:169], a[208:211], v[64:67]\n"
    "s_waitcnt lgkmcnt(1)\n"
    "v_mfma_f32_16x16x32_bf16 v[64:67], v[170:173], a[212:215], v[64:67]\n"
    "v_mfma_f32_16x16x32_bf16 v[68:71], v[150:153], a[216:219], 0\n"
    "v_mfma_f32_16x16x32_bf16 v[68:71], v[154:157], a[220:223], v[68:71]\n"
    "v_mfma_f32_16x16x32_bf16 v[68:71], v[158:161], a[224:227], v[68:71]\n"
    "v_mfma_f32_16x16x32_bf16 v[68:71], v[162:165], a[228:231], v[68:71]\n"
    "v_mfma_f32_16x16x32_bf16 v[68:71], v[166:169], a[232:235], v[68:71]\n"
    "v_mfma_f32_16x16x32_bf16 v[68:71], v[170:173], a[236:239], v[68:71]\n"
    "v_mfma_f32_16x16x32_bf16 v[72:75], v[150:153], a[240:243], 0\n"
    "v_mfma_f32_16x16x32_bf16 v[72:75], v[154:157], a[244:247], v[72:75]\n"
    "v_mfma_f32_16x16x32_bf16 v[72:75], v[158:161], a[248:251], v[72:75]\n"
    "v_mfma_f32_16x16x32_bf16 v[72:75], v[162:165], a[252:255], v[72:75]\n"
    "v_mfma_f32_16x16x32_bf16 v[72:75], v[166:169], v[0:3], v[72:75]\n"
    "v_mfma_f32_16x16x32_bf16 v[72:75], v[170:173], v[4:7], v[72:75]\n"
    "v_mfma_f32_16x16x32_bf16 v[76:79], v[150:153], v[8:11], 0\n"
    "v_mfma_f32_16x16x32_bf16 v[76:79], v[154:157], v[12:15], v[76:79]\n"
    "v_mfma_f32_16x16x32_bf16 v[76:79], v[158:161], v[16:19], v[76:79]\n"
    "v_mfma_f32_16x16x32_bf16 v[76:79], v[162:165], v[20:23], v[76:79]\n"
    "v_mfma_f32_16x16x32_bf16 v[76:79], v[166:169], v[24:27], v[76:79]\n"
    "v_mfma_f32_16x16x32_bf16 v[76:79], v[170:173], v[28:31], v[76:79]\n"
    "v_max3_f32 v56, v64, v65, v66\n"
    "v_max_f32 v56, v56, v67\n"
    "v_max3_f32 v58, v68, v69, v70\n"
    "v_max_f32 v58, v58, v71\n"
    "v_max3_f32 v60, v72, v73, v74\n"
    "v_max_f32 v60, v60, v75\n"
    "s_nop 1\n"
    "v_max3_f32 v62, v76, v77, v78\n"
    "v_max_f32 v62, v62, v79\n"
    "v_mov_b32 v57, v56\n"
    "v_mov_b32 v59, v58\n"
    "v_mov_b32 v61, v60\n"
    "v_mov_b32 v63, v62\n"
    "v_permlane16_swap_b32 v56, v57\n"
    "v_permlane16_swap_b32 v58, v59\n"
    "v_permlane16_swap_b32 v60, v61\n"
    "v_permlane16_swap_b32 v62, v63\n"
    "v_max_f32 v56, v56, v57\n"
    "v_max_f32 v58, v58, v59\n"
    "v_max_f32 v60, v60, v61\n"
    "v_max_f32 v62, v62, v63\n"
    "v_mov_b32 v57, v56\n"
    "v_mov_b32 v59, v58\n"
    "v_mov_b32 v61, v60\n"
    "v_mov_b32 v63, v62\n"
    "v_permlane32_swap_b32 v56, v57\n"
    "v_permlane32_swap_b32 v58, v59\n"
    "v_permlane32_swap_b32 v60, v61\n"
    "v_permlane32_swap_b32 v62, v63\n"
    "v_max_f32 v56, v56, v57\n"
    "v_max_f32 v58, v58, v59\n"
    "v_max_f32 v60, v60, v61\n"
    "v_max_f32 v62, v62, v63\n"
    "v_sub_f32 v128, 0, v56\n"
    "v_sub_f32 v129, 0, v56\n"
    "v_sub_f32 v130, 0, v56\n"
    "v_sub_f32 v131, 0, v56\n"
    "v_sub_f32 v132, 0, v58\n"
    "v_sub_f32 v133, 0, v58\n"
    "v_sub_f32 v134, 0, v58\n"
    "v_sub_f32 v135, 0, v58\n"
    "v_sub_f32 v136, 0, v60\n"
    "v_sub_f32 v137, 0, v60\n"
    "v_sub_f32 v138, 0, v60\n"
    "v_sub_f32 v139, 0, v60\n"
    "v_sub_f32 v140, 0, v62\n"
    "v_sub_f32 v141, 0, v62\n"
    "v_sub_f32 v142, 0, v62\n"
    "v_sub_f32 v143, 0, v62\n"
    "v_mfma_f32_16x16x32_bf16 v[64:67], v[150:153], a[192:195], v[128:131]\n"
    "v_mfma_f32_16x16x32_bf16 v[64:67], v[154:157], a[196:199], v[64:67]\n"
    "ds_read_b128 v[178:181], v251 offset:6144\n"
    "v_mfma_f32_16x16x32_bf16 v[64:67], v[158:161], a[200:203], v[64:67]\n"
    "ds_read_b128 v[182:185], v254 offset:8192\n"
    "ds_read_b128 v[186:189], v251 offset:8192\n"
    "v_mfma_f32_16x16x32_bf16 v[64:67], v[162:165], a[204:207], v[64:67]\n"
    "v_mfma_f32_16x16x32_bf16 v[64:67], v[166:169], a[208:211], v[64:67]\n"
    "v_mfma_f32_16x16x32_bf16 v[64:67], v[170:173], a[212:215], v[64:67]\n"
    "v_mfma_f32_16x16x32_bf16 v[68:71], v[170:173], a[236:239], v[132:135]\n"
    "s_waitcnt vmcnt(16)\n"
    "v_cmp_ne_u64 vcc, s[80:81], v[252:253]\n"
    "s_nop 3\n"
    "s_cbranch_vccnz SPBK%=\n"
    "RTBK%=:\n"
    "s_mov_b32 s87, 512\n"
    "s_mul_i32 s89, %11, 6144\n"
    "s_add_u32 s88, s89, 0\n"
    "s_add_u32 m0, s89, 73728\n"
    "s_nop 0\n"
    "buffer_load_dwordx4 v149, s[60:63], s88 offen offset:0 sc1 lds\n"
    "v_mfma_f32_16x16x32_bf16 v[68:71], v[166:169], a[232:235], v[68:71]\n"
    "v_mfma_f32_16x16x32_bf16 v[68:71], v[162:165], a[228:231], v[68:71]\n"
    "buffer_load_dwordx4 v149, s[60:63], s88 offen offset:1024 sc1 lds\n"
    "v_mfma_f32_16x16x32_bf16 v[68:71], v[158:161], a[224:227], v[68:71]\n"
    "v_mfma_f32_16x16x32_bf16 v[68:71], v[154:157], a[220:223], v[68:71]\n"
    "buffer_load_dwordx4 v149, s[60:63], s88 offen offset:2048 sc1 lds\n"
    "v_mfma_f32_16x16x32_bf16 v[68:71], v[150:153], a[216:219], v[68:71]\n"
    "v_mfma_f32_16x16x32_bf16 v[72:75], v[150:153], a[240:243], v[136:139]\n"
    "v_exp_f32 v64, v64\n"
    "buffer_load_dwordx4 v149, s[60:63], s88 offen offset:3072 sc1 lds\n"
    "v_mfma_f32_16x16x32_bf16 v[72:75], v[154:157], a[244:247], v[72:75]\n"
    "v_add_f32 v144, v144, v64\n"
    "v_mfma_f32_16x16x32_bf16 v[72:75], v[158:161], a[248:251], v[72:75]\n"
    "s_add_u32 m0, m0, 4096\n"
    "s_add_u32 s88, s88, 4096\n"
    "buffer_load_dwordx4 v149, s[60:63], s88 offen offset:0 sc1 lds\n"
    "v_mfma_f32_16x16x32_bf16 v[72:75], v[162:165], a[252:255], v[72:75]\n"
    "v_exp_f32 v65, v65\n"
    "v_mfma_f32_16x16x32_bf16 v[72:75], v[166:169], v[0:3], v[72:75]\n"
    "v_add_f32 v144, v144, v65\n"
    "buffer_load_dwordx4 v149, s[60:63], s88 offen offset:1024 sc1 lds\n"
    "v_mfma_f32_16x16x32_bf16 v[72:75], v[170:173], v[4:7], v[72:75]\n"
    "v_mfma_f32_16x16x32_bf16 v[76:79], v[170:173], v[28:31], v[140:143]\n"
    "v_exp_f32 v66, v66\n"
    "buffer_load_dwordx2 v[252:253], v250, s[64:67], s87 offen sc1\n"
    "v_mfma_f32_16x16x32_bf16 v[76:79], v[166:169], v[24:27], v[76:79]\n"
    "ds_read_b128 v[170:173], v254 offset:10240\n"
    "ds_read_b128 v[166:169], v251 offset:10240\n"
    "v_mfma_f32_16x16x32_bf16 v[76:79], v[162:165], v[20:23], v[76:79]\n"
    "v_add_f32 v144, v144, v66\n"
    "buffer_load_dwordx4 v[32:35], v248, s[40:43], s82 offen offset:512 nt\n"
    "v_mfma_f32_16x16x32_bf16 v[76:79], v[158:161], v[16:19], v[76:79]\n"
    "v_exp_f32 v67, v67\n"
    "v_mfma_f32_16x16x32_bf16 v[76:79], v[154:157], v[12:15], v[76:79]\n"
    "v_add_f32 v144, v144, v67\n"
    "buffer_load_dwordx4 v[36:39], v248, s[40:43], s82 offen offset:640 nt\n"
    "v_mfma_f32_16x16x32_bf16 v[76:79], v[150:153], v[8:11], v[76:79]\n"
    "ds_read_b128 v[162:165], v254 offset:12288\n"
    "s_waitcnt lgkmcnt(6)\n"
    "v_mfma_f32_16x16x32_bf16 v[80:83], v[174:177], a[192:195], v[128:131]\n"
    "v_exp_f32 v68, v68\n"
    "buffer_load_dwordx4 v[40:43], v248, s[40:43], s83 offen offset:512 nt\n"
    "s_waitcnt lgkmcnt(5)\n"
    "v_mfma_f32_16x16x32_bf16 v[80:83], v[178:181], a[196:199], v[80:83]\n"
    "ds_read_b128 v[158:161], v251 offset:12288\n"
    "s_waitcnt lgkmcnt(5)\n"
    "v_mfma_f32_16x16x32_bf16 v[80:83], v[182:185], a[200:203], v[80:83]\n"
    "ds_read_b128 v[154:157], v254 offset:14336\n"
    "ds_read_b128 v[150:153], v251 offset:14336\n"
    "buffer_load_dwordx4 v[44:47], v248, s[40:43], s83 offen offset:640 nt\n"
    "s_waitcnt lgkmcnt(6)\n"
    "v_mfma_f32_16x16x32_bf16 v[80:83], v[186:189], a[204:207], v[80:83]\n"
    "v_exp_f32 v69, v69\n"
    "s_waitcnt lgkmcnt(5)\n"
    "v_mfma_f32_16x16x32_bf16 v[80:83], v[170:173], a[208:211], v[80:83]\n"
    "v_add_f32 v145, v145, v68\n"
    "buffer_load_dwordx2 v[48:49], v249, s[44:47], s84 offen offset:256 nt\n"
    "s_waitcnt lgkmcnt(4)\n"
    "v_mfma_f32_16x16x32_bf16 v[80:83], v[166:169], a[212:215], v[80:83]\n"
    "v_add_f32 v145, v145, v69\n"
    "v_mfma_f32_16x16x32_bf16 v[84:87], v[166:169], a[236:239], v[132:135]\n"
    "v_exp_f32 v70, v70\n"
    "buffer_load_dwordx2 v[50:51], v249, s[44:47], s84 offen offset:320 nt\n"
    "v_mfma_f32_16x16x32_bf16 v[84:87], v[170:173], a[232:235], v[84:87]\n"
    "v_add_f32 v145, v145, v70\n"
    "v_mfma_f32_16x16x32_bf16 v[84:87], v[186:189], a[228:231], v[84:87]\n"
    "buffer_load_dwordx2 v[52:53], v249, s[48:51], s84 offen offset:256 nt\n"
    "v_mfma_f32_16x16x32_bf16 v[84:87], v[182:185], a[224:227], v[84:87]\n"
    "v_exp_f32 v71, v71\n"
    "v_mfma_f32_16x16x32_bf16 v[84:87], v[178:181], a[220:223], v[84:87]\n"
    "v_add_f32 v145, v145, v71\n"
    "buffer_load_dwordx2 v[54:55], v249, s[48:51], s84 offen offset:320 nt\n"
    "v_mfma_f32_16x16x32_bf16 v[84:87], v[174:177], a[216:219], v[84:87]\n"
    "v_mfma_f32_16x16x32_bf16 v[88:91], v[174:177], a[240:243], v[136:139]\n"
    "v_exp_f32 v72, v72\n"
    "s_add_u32 s82, s82, 196608\n"
    "s_add_u32 s83, s83, 196608\n"
    "s_add_u32 s84, s84, 3072\n"
    "v_mfma_f32_16x16x32_bf16 v[88:91], v[178:181], a[244:247], v[88:91]\n"
    "v_add_f32 v146, v146, v72\n"
    "v_mfma_f32_16x16x32_bf16 v[88:91], v[182:185], a[248:251], v[88:91]\n"
    "v_mfma_f32_16x16x32_bf16 v[88:91], v[186:189], a[252:255], v[88:91]\n"
    "v_exp_f32 v73, v73\n"
    "v_mfma_f32_16x16x32_bf16 v[88:91], v[170:173], v[0:3], v[88:91]\n"
    "v_add_f32 v146, v146, v73\n"
    "v_mfma_f32_16x16x32_bf16 v[88:91], v[166:169], v[4:7], v[88:91]\n"
    "v_mfma_f32_16x16x32_bf16 v[92:95], v[166:169], v[28:31], v[140:143]\n"
    "v_exp_f32 v74, v74\n"
    "v_mfma_f32_16x16x32_bf16 v[92:95], v[170:173], v[24:27], v[92:95]\n"
    "ds_read_b128 v[166:169], v254 offset:16384\n"
    "ds_read_b128 v[170:173], v251 offset:16384\n"
    "v_mfma_f32_16x16x32_bf16 v[92:95], v[186:189], v[20:23], v[92:95]\n"
    "v_add_f32 v146, v146, v74\n"
    "v_mfma_f32_16x16x32_bf16 v[92:95], v[182:185], v[16:19], v[92:95]\n"
    "v_exp_f32 v75, v75\n"
    "v_mfma_f32_16x16x32_bf16 v[92:95], v[178:181], v[12:15], v[92:95]\n"
    "v_add_f32 v146, v146, v75\n"
    "v_mfma_f32_16x16x32_bf16 v[92:95], v[174:177], v[8:11], v[92:95]\n"
    "ds_read_b128 v[186:189], v254 offset:18432\n"
    "s_waitcnt lgkmcnt(6)\n"
    "v_mfma_f32_16x16x32_bf16 v[96:99], v[162:165], a[192:195], v[128:131]\n"
    "v_exp_f32 v76, v76\n"
    "s_waitcnt lgkmcnt(5)\n"
    "v_mfma_f32_16x16x32_bf16 v[96:99], v[158:161], a[196:199], v[96:99]\n"
    "ds_read_b128 v[182:185], v251 offset:18432\n"
    "s_waitcnt lgkmcnt(5)\n"
    "v_mfma_f32_16x16x32_bf16 v[96:99], v[154:157], a[200:203], v[96:99]\n"
    "ds_read_b128 v[178:181], v254 offset:20480\n"
    "ds_read_b128 v[174:177], v251 offset:20480\n"
    "s_waitcnt lgkmcnt(6)\n"
    "v_mfma_f32_16x16x32_bf16 v[96:99], v[150:153], a[204:207], v[96:99]\n"
    "v_exp_f32 v77, v77\n"
    "s_waitcnt lgkmcnt(5)\n"
    "v_mfma_f32_16x16x32_bf16 v[96:99], v[166:169], a[208:211], v[96:99]\n"
    "v_add_f32 v147, v147, v76\n"
    "s_waitcnt lgkmcnt(4)\n"
    "v_mfma_f32_16x16x32_bf16 v[96:99], v[170:173], a[212:215], v[96:99]\n"
    "v_add_f32 v147, v147, v77\n"
    "v_mfma_f32_16x16x32_bf16 v[100:103], v[170:173], a[236:239], v[132:135]\n"
    "v_exp_f32 v78, v78\n"
    "v_mfma_f32_16x16x32_bf16 v[100:103], v[166:169], a[232:235], v[100:103]\n"
    "v_add_f32 v147, v147, v78\n"
    "v_mfma_f32_16x16x32_bf16 v[100:103], v[150:153], a[228:231], v[100:103]\n"
    "v_mfma_f32_16x16x32_bf16 v[100:103], v[154:157], a[224:227], v[100:103]\n"
    "v_exp_f32 v79, v79\n"
    "v_mfma_f32_16x16x32_bf16 v[100:103], v[158:161], a[220:223], v[100:103]\n"
    "v_add_f32 v147, v147, v79\n"
    "v_mfma_f32_16x16x32_bf16 v[100:103], v[162:165], a[216:219], v[100:103]\n"
    "v_mfma_f32_16x16x32_bf16 v[104:107], v[162:165], a[240:243], v[136:139]\n"
    "v_exp_f32 v80, v80\n"
    "v_mfma_f32_16x16x32_bf16 v[104:107], v[158:161], a[244:247], v[104:107]\n"
    "v_add_f32 v144, v144, v80\n"
    "v_mfma_f32_16x16x32_bf16 v[104:107], v[154:157], a[248:251], v[104:107]\n"
    "v_mfma_f32_16x16x32_bf16 v[104:107], v[150:153], a[252:255], v[104:107]\n"
    "v_exp_f32 v81, v81\n"
    "v_mfma_f32_16x16x32_bf16 v[104:107], v[166:169], v[0:3], v[104:107]\n"
    "v_add_f32 v144, v144, v81\n"
    "v_mfma_f32_16x16x32_bf16 v[104:107], v[170:173], v[4:7], v[104:107]\n"
    "v_mfma_f32_16x16x32_bf16 v[108:111], v[170:173], v[28:31], v[140:143]\n"
    "v_exp_f32 v82, v82\n"
    "v_mfma_f32_16x16x32_bf16 v[108:111], v[166:169], v[24:27], v[108:111]\n"
    "ds_read_b128 v[170:173], v254 offset:22528\n"
    "ds_read_b128 v[166:169], v251 offset:22528\n"
    "v_mfma_f32_16x16x32_bf16 v[108:111], v[150:153], v[20:23], v[108:111]\n"
    "v_mfma_f32_16x16x32_bf16 v[108:111], v[154:157], v[16:19], v[108:111]\n"
    "v_exp_f32 v83, v83\n"
    "v_mfma_f32_16x16x32_bf16 v[108:111], v[158:161], v[12:15], v[108:111]\n"
    "v_mfma_f32_16x16x32_bf16 v[108:111], v[162:165], v[8:11], v[108:111]\n"
    "s_waitcnt lgkmcnt(5)\n"
    "v_mfma_f32_16x16x32_bf16 v[112:115], v[186:189], a[192:195], v[128:131]\n"
    "v_exp_f32 v84, v84\n"
    "s_waitcnt lgkmcnt(4)\n"
    "v_mfma_f32_16x16x32_bf16 v[112:115], v[182:185], a[196:199], v[112:115]\n"
    "s_waitcnt lgkmcnt(3)\n"
    "v_mfma_f32_16x16x32_bf16 v[112:115], v[178:181], a[200:203], v[112:115]\n"
    "s_waitcnt lgkmcnt(2)\n"
    "v_mfma_f32_16x16x32_bf16 v[112:115], v[174:177], a[204:207], v[112:115]\n"
    "v_exp_f32 v85, v85\n"
    "s_waitcnt lgkmcnt(1)\n"
    "v_mfma_f32_16x16x32_bf16 v[112:115], v[170:173], a[208:211], v[112:115]\n"
    "v_add_f32 v144, v144, v82\n"
    "s_waitcnt lgkmcnt(0)\n"
    "v_mfma_f32_16x16x32_bf16 v[112:115], v[166:169], a[212:215], v[112:115]\n"
    "v_add_f32 v144, v144, v83\n"
    "v_mfma_f32_16x16x32_bf16 v[116:119], v[166:169], a[236:239], v[132:135]\n"
    "v_exp_f32 v86, v86\n"
    "v_mfma_f32_16x16x32_bf16 v[116:119], v[170:173], a[232:235], v[116:119]\n"
    "v_mfma_f32_16x16x32_bf16 v[116:119], v[174:177], a[228:231], v[116:119]\n"
    "v_mfma_f32_16x16x32_bf16 v[116:119], v[178:181], a[224:227], v[116:119]\n"
    "v_exp_f32 v87, v87\n"
    "v_mfma_f32_16x16x32_bf16 v[116:119], v[182:185], a[220:223], v[116:119]\n"
    "v_add_f32 v145, v145, v84\n"
    "v_mfma_f32_16x16x32_bf16 v[116:119], v[186:189], a[216:219], v[116:119]\n"
    "v_add_f32 v145, v145, v85\n"
    "v_mfma_f32_16x16x32_bf16 v[120:123], v[186:189], a[240:243], v[136:139]\n"
    "v_exp_f32 v88, v88\n"
    "v_mfma_f32_16x16x32_bf16 v[120:123], v[182:185], a[244:247], v[120:123]\n"
    "v_mfma_f32_16x16x32_bf16 v[120:123], v[178:181], a[248:251], v[120:123]\n"
    "v_add_f32 v145, v145, v86\n"
    "v_add_f32 v145, v145, v87\n"
    "v_mfma_f32_16x16x32_bf16 v[120:123], v[174:177], a[252:255], v[120:123]\n"
    "v_exp_f32 v89, v89\n"
    "v_mfma_f32_16x16x32_bf16 v[120:123], v[170:173], v[0:3], v[120:123]\n"
    "v_add_f32 v146, v146, v88\n"
    "v_add_f32 v146, v146, v89\n"
    "v_mfma_f32_16x16x32_bf16 v[120:123], v[166:169], v[4:7], v[120:123]\n"
    "v_mfma_f32_16x16x32_bf16 v[124:127], v[166:169], v[28:31], v[140:143]\n"
    "v_exp_f32 v90, v90\n"
    "v_mfma_f32_16x16x32_bf16 v[124:127], v[170:173], v[24:27], v[124:127]\n"
    "v_mfma_f32_16x16x32_bf16 v[124:127], v[174:177], v[20:23], v[124:127]\n"
    "v_mfma_f32_16x16x32_bf16 v[124:127], v[178:181], v[16:19], v[124:127]\n"
    "v_exp_f32 v91, v91\n"
    "v_mfma_f32_16x16x32_bf16 v[124:127], v[182:185], v[12:15], v[124:127]\n"
    "v_add_f32 v146, v146, v90\n"
    "v_mfma_f32_16x16x32_bf16 v[124:127], v[186:189], v[8:11], v[124:127]\n"
    "v_exp_f32 v92, v92\n"
    "v_add_f32 v146, v146, v91\n"
    "v_add_f32 v147, v147, v92\n"
    "v_exp_f32 v93, v93\n"
    "s_nop 0\n"
    "v_add_f32 v147, v147, v93\n"
    "v_exp_f32 v94, v94\n"
    "v_exp_f32 v95, v95\n"
    "v_add_f32 v147, v147, v94\n"
    "v_add_f32 v147, v147, v95\n"
    "v_exp_f32 v96, v96\n"
    "s_nop 0\n"
    "v_add_f32 v144, v144, v96\n"
    "v_exp_f32 v97, v97\n"
    "s_nop 0\n"
    "v_add_f32 v144, v144, v97\n"
    "v_exp_f32 v98, v98\n"
    "s_nop 0\n"
    "v_add_f32 v144, v144, v98\n"
    "v_exp_f32 v99, v99\n"
    "s_nop 0\n"
    "v_add_f32 v144, v144, v99\n"
    "v_exp_f32 v100, v100\n"
    "s_nop 0\n"
    "v_add_f32 v145, v145, v100\n"
    "v_exp_f32 v101, v101\n"
    "s_nop 0\n"
    "v_add_f32 v145, v145, v101\n"
    "v_exp_f32 v102, v102\n"
    "s_nop 0\n"
    "v_add_f32 v145, v145, v102\n"
    "v_exp_f32 v103, v103\n"
    "s_nop 0\n"
    "v_add_f32 v145, v145, v103\n"
    "v_exp_f32 v104, v104\n"
    "s_nop 0\n"
    "v_add_f32 v146, v146, v104\n"
    "v_exp_f32 v105, v105\n"
    "s_nop 0\n"
    "v_add_f32 v146, v146, v105\n"
    "v_exp_f32 v106, v106\n"
    "s_nop 0\n"
    "v_add_f32 v146, v146, v106\n"
    "v_exp_f32 v107, v107\n"
    "v_exp_f32 v108, v108\n"
    "v_add_f32 v146, v146, v107\n"
    "v_add_f32 v147, v147, v108\n"
    "v_exp_f32 v109, v109\n"
    "s_nop 0\n"
    "v_add_f32 v147, v147, v109\n"
    "v_exp_f32 v110, v110\n"
    "v_exp_f32 v111, v111\n"
    "v_add_f32 v147, v147, v110\n"
    "v_add_f32 v147, v147, v111\n"
    "v_exp_f32 v112, v112\n"
    "s_nop 0\n"
    "v_add_f32 v144, v144, v112\n"
    "v_exp_f32 v113, v113\n"
    "s_nop 0\n"
    "v_add_f32 v144, v144, v113\n"
    "v_exp_f32 v114, v114\n"
    "s_waitcnt vmcnt(23)\n"
    "s_mov_b32 m0, s74\n"
    "s_add_u32 s74, s74, s73\n"
    "s_cmp_ge_u32 s74, s75\n"
    "s_cselect_b32 s73, 0xfffe8000, s76\n"
    "buffer_load_dwordx4 v149, s[36:39], s72 offen offset:0 sc1 lds\n"
    "buffer_load_dwordx4 v149, s[36:39], s72 offen offset:1024 sc1 lds\n"
    "buffer_load_dwordx4 v149, s[36:39], s72 offen offset:2048 sc1 lds\n"
    "buffer_load_dwordx4 v149, s[36:39], s72 offen offset:3072 sc1 lds\n"
    "s_add_u32 s72, s72, 4096\n"
    "s_add_u32 m0, m0, 4096\n"
    "s_nop 0\n"
    "buffer_load_dwordx4 v149, s[36:39], s72 offen offset:0 sc1 lds\n"
    "buffer_load_dwordx4 v149, s[36:39], s72 offen offset:1024 sc1 lds\n"
    "buffer_load_dwordx4 v149, s[36:39], s72 offen offset:2048 sc1 lds\n"
    "buffer_load_dwordx4 v149, s[36:39], s72 offen offset:3072 sc1 lds\n"
    "s_add_u32 s72, s72, 4096\n"
    "s_add_u32 m0, m0, 4096\n"
    "s_nop 0\n"
    "buffer_load_dwordx4 v149, s[36:39], s72 offen offset:0 sc1 lds\n"
    "buffer_load_dwordx4 v149, s[36:39], s72 offen offset:1024 sc1 lds\n"
    "buffer_load_dwordx4 v149, s[36:39], s72 offen offset:2048 sc1 lds\n"
    "buffer_load_dwordx4 v149, s[36:39], s72 offen offset:3072 sc1 lds\n"
    "s_add_u32 s72, s72, 16384\n"
    "v_cvt_f32_i32 v190, v190\n"
    "v_cvt_f32_i32 v191, v191\n"
    "v_cvt_f32_i32 v198, v198\n"
    "v_cvt_f32_i32 v199, v199\n"
    "v_lshlrev_b32 v246, 16, v210\n"
    "v_and_b32 v210, s78, v210\n"
    "v_lshlrev_b32 v247, 16, v206\n"
    "v_and_b32 v206, s78, v206\n"
    "v_mul_f32 v247, v247, v246\n"
    "v_mul_f32 v206, v206, v210\n"
    "v_fma_f32 v190, v190, v246, -v247\n"
    "v_fma_f32 v191, v191, v210, -v206\n"
    "v_fma_f32 v198, v198, v246, -v247\n"
    "v_fma_f32 v199, v199, v210, -v206\n"
    "v_cvt_pk_bf16_f32 v238, v190, v191\n"
    "v_cvt_pk_bf16_f32 v242, v198, v199\n"
    "v_cvt_f32_i32 v192, v192\n"
    "v_cvt_f32_i32 v193, v193\n"
    "v_cvt_f32_i32 v200, v200\n"
    "v_cvt_f32_i32 v201, v201\n"
    "v_lshlrev_b32 v246, 16, v211\n"
    "v_and_b32 v211, s78, v211\n"
    "v_lshlrev_b32 v247, 16, v207\n"
    "v_and_b32 v207, s78, v207\n"
    "v_mul_f32 v247, v247, v246\n"
    "v_mul_f32 v207, v207, v211\n"
    "v_fma_f32 v192, v192, v246, -v247\n"
    "v_fma_f32 v193, v193, v211, -v207\n"
    "v_fma_f32 v200, v200, v246, -v247\n"
    "v_fma_f32 v201, v201, v211, -v207\n"
    "v_cvt_pk_bf16_f32 v239, v192, v193\n"
    "v_cvt_pk_bf16_f32 v243, v200, v201\n"
    "v_cvt_f32_i32 v194, v194\n"
    "v_cvt_f32_i32 v195, v195\n"
    "v_cvt_f32_i32 v202, v202\n"
    "v_cvt_f32_i32 v203, v203\n"
    "v_lshlrev_b32 v246, 16, v212\n"
    "v_and_b32 v212, s78, v212\n"
    "v_lshlrev_b32 v247, 16, v208\n"
    "v_and_b32 v208, s78, v208\n"
    "v_mul_f32 v247, v247, v246\n"
    "v_mul_f32 v208, v208, v212\n"
    "v_fma_f32 v194, v194, v246, -v247\n"
    "v_fma_f32 v195, v195, v212, -v208\n"
    "v_fma_f32 v202, v202, v246, -v247\n"
    "v_fma_f32 v203, v203, v212, -v208\n"
    "v_cvt_pk_bf16_f32 v240, v194, v195\n"
    "v_cvt_pk_bf16_f32 v244, v202, v203\n"
    "v_cvt_f32_i32 v196, v196\n"
    "v_cvt_f32_i32 v197, v197\n"
    "v_cvt_f32_i32 v204, v204\n"
    "v_cvt_f32_i32 v205, v205\n"
    "v_lshlrev_b32 v246, 16, v213\n"
    "v_and_b32 v213, s78, v213\n"
    "v_lshlrev_b32 v247, 16, v209\n"
    "v_and_b32 v209, s78, v209\n"
    "v_mul_f32 v247, v247, v246\n"
    "v_mul_f32 v209, v209, v213\n"
    "v_fma_f32 v196, v196, v246, -v247\n"
    "v_fma_f32 v197, v197, v213, -v209\n"
    "v_fma_f32 v204, v204, v246, -v247\n"
    "v_fma_f32 v205, v205, v213, -v209\n"
    "v_cvt_pk_bf16_f32 v241, v196, v197\n"
    "v_cvt_pk_bf16_f32 v245, v204, v205\n"
    "buffer_store_dwordx4 v[238:241], v149, s[52:55], s85 offen offset:0 sc1\n"
    "buffer_store_dwordx4 v[242:245], v149, s[52:55], s85 offen offset:1024 sc1\n"
    "s_waitcnt vmcnt(29)\n"
    "v_cvt_f32_i32 v214, v214\n"
    "v_cvt_f32_i32 v215, v215\n"
    "v_cvt_f32_i32 v222, v222\n"
    "v_cvt_f32_i32 v223, v223\n"
    "v_lshlrev_b32 v246, 16, v234\n"
    "v_and_b32 v234, s78, v234\n"
    "v_lshlrev_b32 v247, 16, v230\n"
    "v_and_b32 v230, s78, v230\n"
    "v_mul_f32 v247, v247, v246\n"
    "v_mul_f32 v230, v230, v234\n"
    "v_fma_f32 v214, v214, v246, -v247\n"
    "v_fma_f32 v215, v215, v234, -v230\n"
    "v_fma_f32 v222, v222, v246, -v247\n"
    "v_fma_f32 v223, v223, v234, -v230\n"
    "v_cvt_pk_bf16_f32 v190, v214, v215\n"
    "v_cvt_pk_bf16_f32 v194, v222, v223\n"
    "v_cvt_f32_i32 v216, v216\n"
    "v_cvt_f32_i32 v217, v217\n"
    "v_cvt_f32_i32 v224, v224\n"
    "v_cvt_f32_i32 v225, v225\n"
    "v_lshlrev_b32 v246, 16, v235\n"
    "v_and_b32 v235, s78, v235\n"
    "v_lshlrev_b32 v247, 16, v231\n"
    "v_and_b32 v231, s78, v231\n"
    "v_mul_f32 v247, v247, v246\n"
    "v_mul_f32 v231, v231, v235\n"
    "v_fma_f32 v216, v216, v246, -v247\n"
    "v_fma_f32 v217, v217, v235, -v231\n"
    "v_fma_f32 v224, v224, v246, -v247\n"
    "v_fma_f32 v225, v225, v235, -v231\n"
    "v_cvt_pk_bf16_f32 v191, v216, v217\n"
    "v_cvt_pk_bf16_f32 v195, v224, v225\n"
    "v_cvt_f32_i32 v218, v218\n"
    "v_cvt_f32_i32 v219, v219\n"
    "v_cvt_f32_i32 v226, v226\n"
    "v_cvt_f32_i32 v227, v227\n"
    "v_lshlrev_b32 v246, 16, v236\n"
    "v_and_b32 v236, s78, v236\n"
    "v_lshlrev_b32 v247, 16, v232\n"
    "v_and_b32 v232, s78, v232\n"
    "v_mul_f32 v247, v247, v246\n"
    "v_mul_f32 v232, v232, v236\n"
    "v_fma_f32 v218, v218, v246, -v247\n"
    "v_fma_f32 v219, v219, v236, -v232\n"
    "v_fma_f32 v226, v226, v246, -v247\n"
    "v_fma_f32 v227, v227, v236, -v232\n"
    "v_cvt_pk_bf16_f32 v192, v218, v219\n"
    "v_cvt_pk_bf16_f32 v196, v226, v227\n"
    "v_cvt_f32_i32 v220, v220\n"
    "v_cvt_f32_i32 v221, v221\n"
    "v_cvt_f32_i32 v228, v228\n"
    "v_cvt_f32_i32 v229, v229\n"
    "v_lshlrev_b32 v246, 16, v237\n"
    "v_and_b32 v237, s78, v237\n"
    "v_lshlrev_b32 v247, 16, v233\n"
    "v_and_b32 v233, s78, v233\n"
    "v_mul_f32 v247, v247, v246\n"
    "v_mul_f32 v233, v233, v237\n"
    "v_fma_f32 v220, v220, v246, -v247\n"
    "v_fma_f32 v221, v221, v237, -v233\n"
    "v_fma_f32 v228, v228, v246, -v247\n"
    "v_fma_f32 v229, v229, v237, -v233\n"
    "v_cvt_pk_bf16_f32 v193, v220, v221\n"
    "v_cvt_pk_bf16_f32 v197, v228, v229\n"
    "buffer_store_dwordx4 v[190:193], v149, s[52:55], s85 offen offset:2048 sc1\n"
    "buffer_store_dwordx4 v[194:197], v149, s[52:55], s85 offen offset:3072 sc1\n"
    "s_waitcnt vmcnt(16)\n"
    "v_cvt_f32_i32 v32, v32\n"
    "v_cvt_f32_i32 v33, v33\n"
    "v_cvt_f32_i32 v40, v40\n"
    "v_cvt_f32_i32 v41, v41\n"
    "v_lshlrev_b32 v246, 16, v52\n"
    "v_and_b32 v52, s78, v52\n"
    "v_lshlrev_b32 v247, 16, v48\n"
    "v_and_b32 v48, s78, v48\n"
    "v_mul_f32 v247, v247, v246\n"
    "v_mul_f32 v48, v48, v52\n"
    "v_fma_f32 v32, v32, v246, -v247\n"
    "v_fma_f32 v33, v33, v52, -v48\n"
    "v_fma_f32 v40, v40, v246, -v247\n"
    "v_fma_f32 v41, v41, v52, -v48\n"
    "v_cvt_pk_bf16_f32 v198, v32, v33\n"
    "v_cvt_pk_bf16_f32 v202, v40, v41\n"
    "v_cvt_f32_i32 v34, v34\n"
    "v_cvt_f32_i32 v35, v35\n"
    "v_cvt_f32_i32 v42, v42\n"
    "v_cvt_f32_i32 v43, v43\n"
    "v_lshlrev_b32 v246, 16, v53\n"
    "v_and_b32 v53, s78, v53\n"
    "v_lshlrev_b32 v247, 16, v49\n"
    "v_and_b32 v49, s78, v49\n"
    "v_mul_f32 v247, v247, v246\n"
    "v_mul_f32 v49, v49, v53\n"
    "v_fma_f32 v34, v34, v246, -v247\n"
    "v_fma_f32 v35, v35, v53, -v49\n"
    "v_fma_f32 v42, v42, v246, -v247\n"
    "v_fma_f32 v43, v43, v53, -v49\n"
    "v_cvt_pk_bf16_f32 v199, v34, v35\n"
    "v_cvt_pk_bf16_f32 v203, v42, v43\n"
    "v_cvt_f32_i32 v36, v36\n"
    "v_cvt_f32_i32 v37, v37\n"
    "v_cvt_f32_i32 v44, v44\n"
    "v_cvt_f32_i32 v45, v45\n"
    "v_lshlrev_b32 v246, 16, v54\n"
    "v_and_b32 v54, s78, v54\n"
    "v_lshlrev_b32 v247, 16, v50\n"
    "v_and_b32 v50, s78, v50\n"
    "v_mul_f32 v247, v247, v246\n"
    "v_mul_f32 v50, v50, v54\n"
    "v_fma_f32 v36, v36, v246, -v247\n"
    "v_fma_f32 v37, v37, v54, -v50\n"
    "v_fma_f32 v44, v44, v246, -v247\n"
    "v_fma_f32 v45, v45, v54, -v50\n"
    "v_cvt_pk_bf16_f32 v200, v36, v37\n"
    "v_cvt_pk_bf16_f32 v204, v44, v45\n"
    "v_cvt_f32_i32 v38, v38\n"
    "v_cvt_f32_i32 v39, v39\n"
    "v_cvt_f32_i32 v46, v46\n"
    "v_cvt_f32_i32 v47, v47\n"
    "v_lshlrev_b32 v246, 16, v55\n"
    "v_and_b32 v55, s78, v55\n"
    "v_lshlrev_b32 v247, 16, v51\n"
    "v_and_b32 v51, s78, v51\n"
    "v_mul_f32 v247, v247, v246\n"
    "v_mul_f32 v51, v51, v55\n"
    "v_fma_f32 v38, v38, v246, -v247\n"
    "v_fma_f32 v39, v39, v55, -v51\n"
    "v_fma_f32 v46, v46, v246, -v247\n"
    "v_fma_f32 v47, v47, v55, -v51\n"
    "v_cvt_pk_bf16_f32 v201, v38, v39\n"
    "v_cvt_pk_bf16_f32 v205, v46, v47\n"
    "s_add_u32 s89, s85, 4096\n"
    "buffer_store_dwordx4 v[198:201], v149, s[52:55], s89 offen offset:0 sc1\n"
    "buffer_store_dwordx4 v[202:205], v149, s[52:55], s89 offen offset:1024 sc1\n"
    "s_add_u32 s85, s85, 98304\n"
    "s_lshl_b32 s91, %11, 11\n"
    "s_add_u32 s91, s91, 149504\n"
    "v_lshrrev_b32 v246, 1, v149\n"
    "v_add_u32 v247, s91, v149\n"
    "v_lshl_add_u32 v249, v249, 1, s91\n"
    "v_cmp_ne_u64 vcc, s[80:81], v[252:253]\n"
    "s_nop 3\n"
    "s_cbranch_vccnz SPCK%=\n"
    "RTCK%=:\n"
    "s_mov_b32 s87, 768\n"
    "s_waitcnt vmcnt(27) lgkmcnt(0)\n"
    "s_barrier\n"
    "v_add_u32 v254, s73, v254\n"
    "v_add_u32 v251, s73, v251\n"
    "v_exp_f32 v115, v115\n"
    "buffer_load_dwordx2 v[206:207], v246, s[44:47], s84 offen nt\n"
    "ds_read_b128 v[150:153], v254 offset:0\n"
    "v_add_f32 v144, v144, v114\n"
    "v_exp_f32 v116, v116\n"
    "ds_read_b128 v[154:157], v251 offset:0\n"
    "v_add_f32 v144, v144, v115\n"
    "buffer_load_dwordx2 v[208:209], v246, s[48:51], s84 offen nt\n"
    "v_exp_f32 v117, v117\n"
    "buffer_load_dwordx4 v[190:193], v248, s[40:43], s82 offen offset:0 nt\n"
    "v_add_f32 v145, v145, v116\n"
    "v_add_f32 v145, v145, v117\n"
    "v_exp_f32 v118, v118\n"
    "ds_read_b128 v[158:161], v254 offset:2048\n"
    "ds_read_b128 v[162:165], v251 offset:2048\n"
    "v_add_u32 v148, s73, v148\n"
    "ds_read_b128 v[166:169], v254 offset:4096\n"
    "v_exp_f32 v119, v119\n"
    "ds_read_b128 v[170:173], v251 offset:4096\n"
    "v_add_f32 v145, v145, v118\n"
    "buffer_load_dwordx4 v[194:197], v248, s[40:43], s82 offen offset:128 nt\n"
    "v_exp_f32 v120, v120\n"
    "v_add_f32 v145, v145, v119\n"
    "v_add_f32 v146, v146, v120\n"
    "v_exp_f32 v121, v121\n"
    "v_cvt_pk_bf16_f32 v32, v64, v65\n"
    "v_add_f32 v146, v146, v121\n"
    "buffer_load_dwordx4 v[198:201], v248, s[40:43], s83 offen offset:0 nt\n"
    "v_exp_f32 v122, v122\n"
    "v_cvt_pk_bf16_f32 v33, v66, v67\n"
    "v_add_f32 v146, v146, v122\n"
    "buffer_load_dwordx4 v[202:205], v248, s[40:43], s83 offen offset:128 nt\n"
    "v_exp_f32 v123, v123\n"
    "v_cvt_pk_bf16_f32 v40, v68, v69\n"
    "v_add_f32 v146, v146, v123\n"
    "ds_read_b128 v[174:177], v254 offset:6144\n"
    ".p2align 6\n"
    "SUPK%=:\n"
    "s_waitcnt lgkmcnt(1)\n"
    "v_mfma_f32_16x16x32_bf16 v[64:67], v[150:153], a[192:195], v[128:131]\n"
    "v_exp_f32 v124, v124\n"
    "v_mfma_f32_16x16x32_bf16 v[64:67], v[154:157], a[196:199], v[64:67]\n"
    "ds_read_b128 v[178:181], v251 offset:6144\n"
    "v_cvt_pk_bf16_f32 v41, v70, v71\n"
    "v_mfma_f32_16x16x32_bf16 v[64:67], v[158:161], a[200:203], v[64:67]\n"
    "ds_read_b128 v[182:185], v254 offset:8192\n"
    "ds_read_b128 v[186:189], v251 offset:8192\n"
    "v_mfma_f32_16x16x32_bf16 v[64:67], v[162:165], a[204:207], v[64:67]\n"
    "v_exp_f32 v125, v125\n"
    "v_mfma_f32_16x16x32_bf16 v[64:67], v[166:169], a[208:211], v[64:67]\n"
    "v_cvt_pk_bf16_f32 v48, v72, v73\n"
    "v_add_f32 v147, v147, v124\n"
    "v_mfma_f32_16x16x32_bf16 v[64:67], v[170:173], a[212:215], v[64:67]\n"
    "v_cvt_pk_bf16_f32 v49, v74, v75\n"
    "v_add_f32 v147, v147, v125\n"
    "v_mfma_f32_16x16x32_bf16 v[68:71], v[170:173], a[236:239], v[132:135]\n"
    "v_exp_f32 v126, v126\n"
    "v_mfma_f32_16x16x32_bf16 v[68:71], v[166:169], a[232:235], v[68:71]\n"
    "v_add_f32 v147, v147, v126\n"
    "v_mfma_f32_16x16x32_bf16 v[68:71], v[162:165], a[228:231], v[68:71]\n"
    "v_cvt_pk_bf16_f32 v56, v76, v77\n"
    "v_mfma_f32_16x16x32_bf16 v[68:71], v[158:161], a[224:227], v[68:71]\n"
    "v_exp_f32 v127, v127\n"
    "v_mfma_f32_16x16x32_bf16 v[68:71], v[154:157], a[220:223], v[68:71]\n"
    "v_add_f32 v147, v147, v127\n"
    "v_mfma_f32_16x16x32_bf16 v[68:71], v[150:153], a[216:219], v[68:71]\n"
    "v_cvt_pk_bf16_f32 v57, v78, v79\n"
    "v_mfma_f32_16x16x32_bf16 v[72:75], v[150:153], a[240:243], v[136:139]\n"
    "v_exp_f32 v64, v64\n"
    "v_mfma_f32_16x16x32_bf16 v[72:75], v[154:157], a[244:247], v[72:75]\n"
    "v_add_f32 v144, v144, v64\n"
    "v_mfma_f32_16x16x32_bf16 v[72:75], v[158:161], a[248:251], v[72:75]\n"
    "v_cvt_pk_bf16_f32 v34, v80, v81\n"
    "v_mfma_f32_16x16x32_bf16 v[72:75], v[162:165], a[252:255], v[72:75]\n"
    "v_exp_f32 v65, v65\n"
    "v_mfma_f32_16x16x32_bf16 v[72:75], v[166:169], v[0:3], v[72:75]\n"
    "v_add_f32 v144, v144, v65\n"
    "v_mfma_f32_16x16x32_bf16 v[72:75], v[170:173], v[4:7], v[72:75]\n"
    "v_cvt_pk_bf16_f32 v35, v82, v83\n"
    "v_mfma_f32_16x16x32_bf16 v[76:79], v[170:173], v[28:31], v[140:143]\n"
    "v_exp_f32 v66, v66\n"
    "v_mfma_f32_16x16x32_bf16 v[76:79], v[166:169], v[24:27], v[76:79]\n"
    "ds_read_b128 v[170:173], v254 offset:10240\n"
    "ds_read_b128 v[166:169], v251 offset:10240\n"
    "v_mfma_f32_16x16x32_bf16 v[76:79], v[162:165], v[20:23], v[76:79]\n"
    "v_add_f32 v144, v144, v66\n"
    "v_cvt_pk_bf16_f32 v42, v84, v85\n"
    "v_mfma_f32_16x16x32_bf16 v[76:79], v[158:161], v[16:19], v[76:79]\n"
    "v_exp_f32 v67, v67\n"
    "v_mfma_f32_16x16x32_bf16 v[76:79], v[154:157], v[12:15], v[76:79]\n"
    "v_add_f32 v144, v144, v67\n"
    "v_cvt_pk_bf16_f32 v43, v86, v87\n"
    "v_mfma_f32_16x16x32_bf16 v[76:79], v[150:153], v[8:11], v[76:79]\n"
    "ds_read_b128 v[162:165], v254 offset:12288\n"
    "s_waitcnt lgkmcnt(3)\n"
    "v_mfma_f32_16x16x32_bf16 v[80:83], v[174:177], a[192:195], v[128:131]\n"
    "v_exp_f32 v68, v68\n"
    "v_mfma_f32_16x16x32_bf16 v[80:83], v[178:181], a[196:199], v[80:83]\n"
    "ds_read_b128 v[158:161], v251 offset:12288\n"
    "v_add_f32 v145, v145, v68\n"
    "v_mfma_f32_16x16x32_bf16 v[80:83], v[182:185], a[200:203], v[80:83]\n"
    "ds_read_b128 v[154:157], v254 offset:14336\n"
    "ds_read_b128 v[150:153], v251 offset:14336\n"
    "v_mfma_f32_16x16x32_bf16 v[80:83], v[186:189], a[204:207], v[80:83]\n"
    "v_exp_f32 v69, v69\n"
    "s_waitcnt lgkmcnt(4)\n"
    "v_mfma_f32_16x16x32_bf16 v[80:83], v[170:173], a[208:211], v[80:83]\n"
    "v_add_f32 v145, v145, v69\n"
    "v_cvt_pk_bf16_f32 v50, v88, v89\n"
    "v_mfma_f32_16x16x32_bf16 v[80:83], v[166:169], a[212:215], v[80:83]\n"
    "v_cvt_pk_bf16_f32 v51, v90, v91\n"
    "v_mfma_f32_16x16x32_bf16 v[84:87], v[166:169], a[236:239], v[132:135]\n"
    "v_exp_f32 v70, v70\n"
    "v_mfma_f32_16x16x32_bf16 v[84:87], v[170:173], a[232:235], v[84:87]\n"
    "v_add_f32 v145, v145, v70\n"
    "v_mfma_f32_16x16x32_bf16 v[84:87], v[186:189], a[228:231], v[84:87]\n"
    "v_cvt_pk_bf16_f32 v58, v92, v93\n"
    "v_mfma_f32_16x16x32_bf16 v[84:87], v[182:185], a[224:227], v[84:87]\n"
    "v_exp_f32 v71, v71\n"
    "v_mfma_f32_16x16x32_bf16 v[84:87], v[178:181], a[220:223], v[84:87]\n"
    "v_add_f32 v145, v145, v71\n"
    "v_mfma_f32_16x16x32_bf16 v[84:87], v[174:177], a[216:219], v[84:87]\n"
    "v_cvt_pk_bf16_f32 v59, v94, v95\n"
    "v_mfma_f32_16x16x32_bf16 v[88:91], v[174:177], a[240:243], v[136:139]\n"
    "v_exp_f32 v72, v72\n"
    "v_mfma_f32_16x16x32_bf16 v[88:91], v[178:181], a[244:247], v[88:91]\n"
    "v_add_f32 v146, v146, v72\n"
    "v_mfma_f32_16x16x32_bf16 v[88:91], v[182:185], a[248:251], v[88:91]\n"
    "v_cvt_pk_bf16_f32 v36, v96, v97\n"
    "v_mfma_f32_16x16x32_bf16 v[88:91], v[186:189], a[252:255], v[88:91]\n"
    "v_exp_f32 v73, v73\n"
    "v_mfma_f32_16x16x32_bf16 v[88:91], v[170:173], v[0:3], v[88:91]\n"
    "v_add_f32 v146, v146, v73\n"
    "v_mfma_f32_16x16x32_bf16 v[88:91], v[166:169], v[4:7], v[88:91]\n"
    "v_cvt_pk_bf16_f32 v37, v98, v99\n"
    "v_mfma_f32_16x16x32_bf16 v[92:95], v[166:169], v[28:31], v[140:143]\n"
    "v_exp_f32 v74, v74\n"
    "v_mfma_f32_16x16x32_bf16 v[92:95], v[170:173], v[24:27], v[92:95]\n"
    "ds_read_b128 v[166:169], v254 offset:16384\n"
    "ds_read_b128 v[170:173], v251 offset:16384\n"
    "v_mfma_f32_16x16x32_bf16 v[92:95], v[186:189], v[20:23], v[92:95]\n"
    "v_add_f32 v146, v146, v74\n"
    "v_cvt_pk_bf16_f32 v44, v100, v101\n"
    "v_mfma_f32_16x16x32_bf16 v[92:95], v[182:185], v[16:19], v[92:95]\n"
    "v_exp_f32 v75, v75\n"
    "v_mfma_f32_16x16x32_bf16 v[92:95], v[178:181], v[12:15], v[92:95]\n"
    "v_add_f32 v146, v146, v75\n"
    "v_cvt_pk_bf16_f32 v45, v102, v103\n"
    "v_mfma_f32_16x16x32_bf16 v[92:95], v[174:177], v[8:11], v[92:95]\n"
    "ds_read_b128 v[186:189], v254 offset:18432\n"
    "s_waitcnt lgkmcnt(3)\n"
    "v_mfma_f32_16x16x32_bf16 v[96:99], v[162:165], a[192:195], v[128:131]\n"
    "v_exp_f32 v76, v76\n"
    "v_mfma_f32_16x16x32_bf16 v[96:99], v[158:161], a[196:199], v[96:99]\n"
    "ds_read_b128 v[182:185], v251 offset:18432\n"
    "s_waitcnt vmcnt(6)\n"
    "v_mfma_f32_16x16x32_bf16 v[96:99], v[154:157], a[200:203], v[96:99]\n"
    "ds_read_b128 v[178:181], v254 offset:20480\n"
    "ds_read_b128 v[174:177], v251 offset:20480\n"
    "v_mfma_f32_16x16x32_bf16 v[96:99], v[150:153], a[204:207], v[96:99]\n"
    "v_exp_f32 v77, v77\n"
    "s_waitcnt lgkmcnt(4)\n"
    "v_mfma_f32_16x16x32_bf16 v[96:99], v[166:169], a[208:211], v[96:99]\n"
    "buffer_store_dwordx2 v[252:253], off, s[64:67], s86 sc1\n"
    "v_mfma_f32_16x16x32_bf16 v[96:99], v[170:173], a[212:215], v[96:99]\n"
    "s_add_u32 s86, s86, 256\n"
    "v_add_f32 v147, v147, v76\n"
    "v_mfma_f32_16x16x32_bf16 v[100:103], v[170:173], a[236:239], v[132:135]\n"
    "v_exp_f32 v78, v78\n"
    "v_mfma_f32_16x16x32_bf16 v[100:103], v[166:169], a[232:235], v[100:103]\n"
    "v_add_f32 v147, v147, v77\n"
    "v_add_f32 v147, v147, v78\n"
    "v_mfma_f32_16x16x32_bf16 v[100:103], v[150:153], a[228:231], v[100:103]\n"
    "v_cvt_pk_bf16_f32 v52, v104, v105\n"
    "v_cvt_pk_bf16_f32 v53, v106, v107\n"
    "v_mfma_f32_16x16x32_bf16 v[100:103], v[154:157], a[224:227], v[100:103]\n"
    "v_exp_f32 v79, v79\n"
    "v_mfma_f32_16x16x32_bf16 v[100:103], v[158:161], a[220:223], v[100:103]\n"
    "v_add_f32 v147, v147, v79\n"
    "v_cvt_pk_bf16_f32 v60, v108, v109\n"
    "v_mfma_f32_16x16x32_bf16 v[100:103], v[162:165], a[216:219], v[100:103]\n"
    "v_cvt_pk_bf16_f32 v61, v110, v111\n"
    "v_mfma_f32_16x16x32_bf16 v[104:107], v[162:165], a[240:243], v[136:139]\n"
    "v_exp_f32 v80, v80\n"
    "v_mfma_f32_16x16x32_bf16 v[104:107], v[158:161], a[244:247], v[104:107]\n"
    "v_add_f32 v144, v144, v80\n"
    "v_mfma_f32_16x16x32_bf16 v[104:107], v[154:157], a[248:251], v[104:107]\n"
    "v_cvt_pk_bf16_f32 v38, v112, v113\n"
    "v_mfma_f32_16x16x32_bf16 v[104:107], v[150:153], a[252:255], v[104:107]\n"
    "v_exp_f32 v81, v81\n"
    "v_mfma_f32_16x16x32_bf16 v[104:107], v[166:169], v[0:3], v[104:107]\n"
    "v_add_f32 v144, v144, v81\n"
    "v_cvt_pk_bf16_f32 v46, v116, v117\n"
    "v_mfma_f32_16x16x32_bf16 v[104:107], v[170:173], v[4:7], v[104:107]\n"
    "s_mov_b32 m0, s74\n"
    "v_cvt_pk_bf16_f32 v39, v114, v115\n"
    "v_mfma_f32_16x16x32_bf16 v[108:111], v[170:173], v[28:31], v[140:143]\n"
    "v_exp_f32 v82, v82\n"
    "v_mfma_f32_16x16x32_bf16 v[108:111], v[166:169], v[24:27], v[108:111]\n"
    "ds_read_b128 v[170:173], v254 offset:22528\n"
    "ds_read_b128 v[166:169], v251 offset:22528\n"
    "v_mfma_f32_16x16x32_bf16 v[108:111], v[150:153], v[20:23], v[108:111]\n"
    "s_add_u32 s74, s74, s73\n"
    "v_add_f32 v144, v144, v82\n"
    "v_mfma_f32_16x16x32_bf16 v[108:111], v[154:157], v[16:19], v[108:111]\n"
    "v_exp_f32 v83, v83\n"
    "v_mfma_f32_16x16x32_bf16 v[108:111], v[158:161], v[12:15], v[108:111]\n"
    "s_cmp_ge_u32 s74, s75\n"
    "s_cselect_b32 s73, 0xfffe8000, s76\n"
    "v_mfma_f32_16x16x32_bf16 v[108:111], v[162:165], v[8:11], v[108:111]\n"
    "ds_read_b128 v[150:153], v148 offset:24576\n"
    "s_waitcnt lgkmcnt(3)\n"
    "v_mfma_f32_16x16x32_bf16 v[112:115], v[186:189], a[192:195], v[128:131]\n"
    "v_exp_f32 v84, v84\n"
    "v_mfma_f32_16x16x32_bf16 v[112:115], v[182:185], a[196:199], v[112:115]\n"
    "ds_read_b128 v[154:157], v148 offset:36864\n"
    "v_add_f32 v144, v144, v83\n"
    "v_mfma_f32_16x16x32_bf16 v[112:115], v[178:181], a[200:203], v[112:115]\n"
    "buffer_load_dwordx4 v149, s[36:39], s72 offen offset:0 sc1 lds\n"
    "v_mfma_f32_16x16x32_bf16 v[112:115], v[174:177], a[204:207], v[112:115]\n"
    "v_exp_f32 v85, v85\n"
    "s_waitcnt lgkmcnt(2)\n"
    "v_mfma_f32_16x16x32_bf16 v[112:115], v[170:173], a[208:211], v[112:115]\n"
    "v_add_f32 v145, v145, v84\n"
    "v_add_f32 v145, v145, v85\n"
    "v_mfma_f32_16x16x32_bf16 v[112:115], v[166:169], a[212:215], v[112:115]\n"
    "v_cvt_pk_bf16_f32 v47, v118, v119\n"
    "v_cvt_pk_bf16_f32 v54, v120, v121\n"
    "v_mfma_f32_16x16x32_bf16 v[116:119], v[166:169], a[236:239], v[132:135]\n"
    "v_exp_f32 v86, v86\n"
    "v_mfma_f32_16x16x32_bf16 v[116:119], v[170:173], a[232:235], v[116:119]\n"
    "buffer_load_dwordx4 v149, s[36:39], s72 offen offset:1024 sc1 lds\n"
    "v_mfma_f32_16x16x32_bf16 v[116:119], v[174:177], a[228:231], v[116:119]\n"
    "ds_read_b128 v[158:161], v148 offset:25600\n"
    "ds_read_b128 v[162:165], v148 offset:37888\n"
    "v_mfma_f32_16x16x32_bf16 v[116:119], v[178:181], a[224:227], v[116:119]\n"
    "v_exp_f32 v87, v87\n"
    "v_mfma_f32_16x16x32_bf16 v[116:119], v[182:185], a[220:223], v[116:119]\n"
    "v_add_f32 v145, v145, v86\n"
    "v_add_f32 v145, v145, v87\n"
    "v_mfma_f32_16x16x32_bf16 v[116:119], v[186:189], a[216:219], v[116:119]\n"
    "v_cvt_pk_bf16_f32 v55, v122, v123\n"
    "v_cvt_pk_bf16_f32 v62, v124, v125\n"
    "v_mfma_f32_16x16x32_bf16 v[120:123], v[186:189], a[240:243], v[136:139]\n"
    "v_exp_f32 v88, v88\n"
    "v_mfma_f32_16x16x32_bf16 v[120:123], v[182:185], a[244:247], v[120:123]\n"
    "buffer_load_dwordx4 v149, s[36:39], s72 offen offset:2048 sc1 lds\n"
    "v_mfma_f32_16x16x32_bf16 v[120:123], v[178:181], a[248:251], v[120:123]\n"
    "v_add_f32 v146, v146, v88\n"
    "v_cvt_pk_bf16_f32 v63, v126, v127\n"
    "v_mfma_f32_16x16x32_bf16 v[120:123], v[174:177], a[252:255], v[120:123]\n"
    "v_exp_f32 v89, v89\n"
    "v_mfma_f32_16x16x32_bf16 v[120:123], v[170:173], v[0:3], v[120:123]\n"
    "v_add_f32 v146, v146, v89\n"
    "v_mfma_f32_16x16x32_bf16 v[120:123], v[166:169], v[4:7], v[120:123]\n"
    "v_mfma_f32_16x16x32_bf16 v[124:127], v[166:169], v[28:31], v[140:143]\n"
    "v_exp_f32 v90, v90\n"
    "v_mfma_f32_16x16x32_bf16 v[124:127], v[170:173], v[24:27], v[124:127]\n"
    "ds_read_b128 v[166:169], v148 offset:26624\n"
    "ds_read_b128 v[170:173], v148 offset:38912\n"
    "v_mfma_f32_16x16x32_bf16 v[124:127], v[174:177], v[20:23], v[124:127]\n"
    "buffer_load_dwordx4 v149, s[36:39], s72 offen offset:3072 sc1 lds\n"
    "v_mfma_f32_16x16x32_bf16 v[124:127], v[178:181], v[16:19], v[124:127]\n"
    "v_exp_f32 v91, v91\n"
    "v_mfma_f32_16x16x32_bf16 v[124:127], v[182:185], v[12:15], v[124:127]\n"
    "s_add_u32 s72, s72, 4096\n"
    "v_add_f32 v146, v146, v90\n"
    "v_mfma_f32_16x16x32_bf16 v[124:127], v[186:189], v[8:11], v[124:127]\n"
    "ds_read_b128 v[174:177], v148 offset:27648\n"
    "s_waitcnt lgkmcnt(3)\n"
    "v_mfma_f32_16x16x32_bf16 a[0:3], v[32:35], v[150:153], a[0:3]\n"
    "v_exp_f32 v92, v92\n"
    "v_mfma_f32_16x16x32_bf16 a[0:3], v[36:39], v[154:157], a[0:3]\n"
    "ds_read_b128 v[178:181], v148 offset:39936\n"
    "s_waitcnt vmcnt(9)\n"
    "v_mfma_f32_16x16x32_bf16 a[4:7], v[44:47], v[154:157], a[4:7]\n"
    "s_add_u32 m0, m0, 4096\n"
    "v_add_f32 v146, v146, v91\n"
    "v_mfma_f32_16x16x32_bf16 a[4:7], v[40:43], v[150:153], a[4:7]\n"
    "v_exp_f32 v93, v93\n"
    "v_mfma_f32_16x16x32_bf16 a[8:11], v[48:51], v[150:153], a[8:11]\n"
    "v_add_f32 v147, v147, v92\n"
    "v_add_f32 v147, v147, v93\n"
    "v_mfma_f32_16x16x32_bf16 a[8:11], v[52:55], v[154:157], a[8:11]\n"
    "v_lshlrev_b32 v230, 16, v208\n"
    "v_and_b32 v231, s78, v208\n"
    "v_mfma_f32_16x16x32_bf16 a[12:15], v[60:63], v[154:157], a[12:15]\n"
    "v_exp_f32 v94, v94\n"
    "v_mfma_f32_16x16x32_bf16 a[12:15], v[56:59], v[150:153], a[12:15]\n"
    "buffer_load_dwordx4 v149, s[36:39], s72 offen offset:0 sc1 lds\n"
    "v_mfma_f32_16x16x32_bf16 a[16:19], v[32:35], v[158:161], a[16:19]\n"
    "ds_read_b128 v[182:185], v148 offset:28672\n"
    "ds_read_b128 v[186:189], v148 offset:40960\n"
    "v_mfma_f32_16x16x32_bf16 a[16:19], v[36:39], v[162:165], a[16:19]\n"
    "v_exp_f32 v95, v95\n"
    "v_mfma_f32_16x16x32_bf16 a[20:23], v[44:47], v[162:165], a[20:23]\n"
    "v_add_f32 v147, v147, v94\n"
    "v_add_f32 v147, v147, v95\n"
    "v_mfma_f32_16x16x32_bf16 a[20:23], v[40:43], v[158:161], a[20:23]\n"
    "v_lshlrev_b32 v234, 16, v206\n"
    "v_and_b32 v235, s78, v206\n"
    "v_mfma_f32_16x16x32_bf16 a[24:27], v[48:51], v[158:161], a[24:27]\n"
    "v_exp_f32 v96, v96\n"
    "v_mfma_f32_16x16x32_bf16 a[24:27], v[52:55], v[162:165], a[24:27]\n"
    "buffer_load_dwordx4 v149, s[36:39], s72 offen offset:1024 sc1 lds\n"
    "v_mfma_f32_16x16x32_bf16 a[28:31], v[60:63], v[162:165], a[28:31]\n"
    "v_add_f32 v144, v144, v96\n"
    "v_lshlrev_b32 v232, 16, v209\n"
    "v_mfma_f32_16x16x32_bf16 a[28:31], v[56:59], v[158:161], a[28:31]\n"
    "v_exp_f32 v97, v97\n"
    "s_waitcnt lgkmcnt(0)\n"
    "v_mfma_f32_16x16x32_bf16 a[32:35], v[32:35], v[166:169], a[32:35]\n"
    "ds_read_b128 v[150:153], v148 offset:29696\n"
    "v_add_f32 v144, v144, v97\n"
    "v_mfma_f32_16x16x32_bf16 a[32:35], v[36:39], v[170:173], a[32:35]\n"
    "ds_read_b128 v[154:157], v148 offset:41984\n"
    "v_and_b32 v233, s78, v209\n"
    "v_mfma_f32_16x16x32_bf16 a[36:39], v[44:47], v[170:173], a[36:39]\n"
    "v_exp_f32 v98, v98\n"
    "v_mfma_f32_16x16x32_bf16 a[36:39], v[40:43], v[166:169], a[36:39]\n"
    "buffer_load_dwordx4 v149, s[36:39], s72 offen offset:2048 sc1 lds\n"
    "v_mfma_f32_16x16x32_bf16 a[40:43], v[48:51], v[166:169], a[40:43]\n"
    "v_add_f32 v144, v144, v98\n"
    "v_lshlrev_b32 v236, 16, v207\n"
    "v_mfma_f32_16x16x32_bf16 a[40:43], v[52:55], v[170:173], a[40:43]\n"
    "v_exp_f32 v99, v99\n"
    "v_mfma_f32_16x16x32_bf16 a[44:47], v[60:63], v[170:173], a[44:47]\n"
    "v_add_f32 v144, v144, v99\n"
    "v_and_b32 v237, s78, v207\n"
    "v_mfma_f32_16x16x32_bf16 a[44:47], v[56:59], v[166:169], a[44:47]\n"
    "ds_read_b128 v[158:161], v148 offset:30720\n"
    "v_mul_f32 v234, v234, v230\n"
    "v_mfma_f32_16x16x32_bf16 a[48:51], v[32:35], v[174:177], a[48:51]\n"
    "v_exp_f32 v100, v100\n"
    "v_mfma_f32_16x16x32_bf16 a[48:51], v[36:39], v[178:181], a[48:51]\n"
    "ds_read_b128 v[162:165], v148 offset:43008\n"
    "v_add_f32 v145, v145, v100\n"
    "v_mfma_f32_16x16x32_bf16 a[52:55], v[44:47], v[178:181], a[52:55]\n"
    "buffer_load_dwordx4 v149, s[36:39], s72 offen offset:3072 sc1 lds\n"
    "v_mfma_f32_16x16x32_bf16 a[52:55], v[40:43], v[174:177], a[52:55]\n"
    "v_exp_f32 v101, v101\n"
    "v_mfma_f32_16x16x32_bf16 a[56:59], v[48:51], v[174:177], a[56:59]\n"
    "s_add_u32 s72, s72, 4096\n"
    "v_add_f32 v145, v145, v101\n"
    "v_mfma_f32_16x16x32_bf16 a[56:59], v[52:55], v[178:181], a[56:59]\n"
    "s_add_u32 m0, m0, 4096\n"
    "v_mul_f32 v235, v235, v231\n"
    "v_mfma_f32_16x16x32_bf16 a[60:63], v[60:63], v[178:181], a[60:63]\n"
    "v_exp_f32 v102, v102\n"
    "v_mfma_f32_16x16x32_bf16 a[60:63], v[56:59], v[174:177], a[60:63]\n"
    "v_add_f32 v145, v145, v102\n"
    "v_mul_f32 v236, v236, v232\n"
    "v_mfma_f32_16x16x32_bf16 a[64:67], v[32:35], v[182:185], a[64:67]\n"
    "ds_read_b128 v[166:169], v148 offset:31744\n"
    "ds_read_b128 v[170:173], v148 offset:44032\n"
    "v_mfma_f32_16x16x32_bf16 a[64:67], v[36:39], v[186:189], a[64:67]\n"
    "v_exp_f32 v103, v103\n"
    "v_mfma_f32_16x16x32_bf16 a[68:71], v[44:47], v[186:189], a[68:71]\n"
    "v_add_f32 v145, v145, v103\n"
    "v_mul_f32 v237, v237, v233\n"
    "v_mfma_f32_16x16x32_bf16 a[68:71], v[40:43], v[182:185], a[68:71]\n"
    "buffer_load_dwordx4 v149, s[36:39], s72 offen offset:0 sc1 lds\n"
    "v_mfma_f32_16x16x32_bf16 a[72:75], v[48:51], v[182:185], a[72:75]\n"
    "v_exp_f32 v104, v104\n"
    "v_mfma_f32_16x16x32_bf16 a[72:75], v[52:55], v[186:189], a[72:75]\n"
    "ds_write_b128 v247, v[230:233]\n"
    "v_add_f32 v146, v146, v104\n"
    "v_mfma_f32_16x16x32_bf16 a[76:79], v[60:63], v[186:189], a[76:79]\n"
    "ds_write_b128 v247, v[234:237] offset:1024\n"
    "v_mfma_f32_16x16x32_bf16 a[76:79], v[56:59], v[182:185], a[76:79]\n"
    "v_exp_f32 v105, v105\n"
    "s_waitcnt lgkmcnt(2)\n"
    "v_mfma_f32_16x16x32_bf16 a[80:83], v[32:35], v[150:153], a[80:83]\n"
    "ds_read_b128 v[206:209], v249 offset:0\n"
    "ds_read_b128 v[210:213], v249 offset:1024\n"
    "v_mfma_f32_16x16x32_bf16 a[80:83], v[36:39], v[154:157], a[80:83]\n"
    "buffer_load_dwordx4 v149, s[36:39], s72 offen offset:1024 sc1 lds\n"
    "v_mfma_f32_16x16x32_bf16 a[84:87], v[44:47], v[154:157], a[84:87]\n"
    "v_exp_f32 v106, v106\n"
    "v_mfma_f32_16x16x32_bf16 a[84:87], v[40:43], v[150:153], a[84:87]\n"
    "v_add_f32 v146, v146, v105\n"
    "v_add_f32 v146, v146, v106\n"
    "v_mfma_f32_16x16x32_bf16 a[88:91], v[48:51], v[150:153], a[88:91]\n"
    "v_mfma_f32_16x16x32_bf16 a[88:91], v[52:55], v[154:157], a[88:91]\n"
    "v_exp_f32 v107, v107\n"
    "v_mfma_f32_16x16x32_bf16 a[92:95], v[60:63], v[154:157], a[92:95]\n"
    "buffer_load_dwordx4 v149, s[36:39], s72 offen offset:2048 sc1 lds\n"
    "v_mfma_f32_16x16x32_bf16 a[92:95], v[56:59], v[150:153], a[92:95]\n"
    "ds_read_b128 v[150:153], v148 offset:32768\n"
    "ds_read_b128 v[154:157], v148 offset:45056\n"
    "v_mfma_f32_16x16x32_bf16 a[96:99], v[32:35], v[158:161], a[96:99]\n"
    "v_exp_f32 v108, v108\n"
    "v_mfma_f32_16x16x32_bf16 a[96:99], v[36:39], v[162:165], a[96:99]\n"
    "v_add_f32 v146, v146, v107\n"
    "v_add_f32 v147, v147, v108\n"
    "v_mfma_f32_16x16x32_bf16 a[100:103], v[44:47], v[162:165], a[100:103]\n"
    "v_mfma_f32_16x16x32_bf16 a[100:103], v[40:43], v[158:161], a[100:103]\n"
    "v_exp_f32 v109, v109\n"
    "v_mfma_f32_16x16x32_bf16 a[104:107], v[48:51], v[158:161], a[104:107]\n"
    "buffer_load_dwordx4 v149, s[36:39], s72 offen offset:3072 sc1 lds\n"
    "v_mfma_f32_16x16x32_bf16 a[104:107], v[52:55], v[162:165], a[104:107]\n"
    "s_add_u32 s72, s72, 16384\n"
    "v_add_f32 v147, v147, v109\n"
    "v_mfma_f32_16x16x32_bf16 a[108:111], v[60:63], v[162:165], a[108:111]\n"
    "v_exp_f32 v110, v110\n"
    "v_mfma_f32_16x16x32_bf16 a[108:111], v[56:59], v[158:161], a[108:111]\n"
    "ds_read_b128 v[158:161], v148 offset:33792\n"
    "ds_read_b128 v[162:165], v148 offset:46080\n"
    "v_mfma_f32_16x16x32_bf16 a[112:115], v[32:35], v[166:169], a[112:115]\n"
    "ds_read_b128 v[174:177], v148 offset:34816\n"
    "ds_read_b128 v[178:181], v148 offset:47104\n"
    "v_mfma_f32_16x16x32_bf16 a[112:115], v[36:39], v[170:173], a[112:115]\n"
    "v_exp_f32 v111, v111\n"
    "v_mfma_f32_16x16x32_bf16 a[116:119], v[44:47], v[170:173], a[116:119]\n"
    "ds_read_b128 v[182:185], v148 offset:35840\n"
    "v_add_f32 v147, v147, v110\n"
    "v_mfma_f32_16x16x32_bf16 a[116:119], v[40:43], v[166:169], a[116:119]\n"
    "ds_read_b128 v[186:189], v148 offset:48128\n"
    "v_add_f32 v147, v147, v111\n"
    "v_mfma_f32_16x16x32_bf16 a[120:123], v[48:51], v[166:169], a[120:123]\n"
    "v_exp_f32 v112, v112\n"
    "v_mfma_f32_16x16x32_bf16 a[120:123], v[52:55], v[170:173], a[120:123]\n"
    "v_add_f32 v144, v144, v112\n"
    "v_mfma_f32_16x16x32_bf16 a[124:127], v[60:63], v[170:173], a[124:127]\n"
    "v_mfma_f32_16x16x32_bf16 a[124:127], v[56:59], v[166:169], a[124:127]\n"
    "v_exp_f32 v113, v113\n"
    "s_waitcnt lgkmcnt(2)\n"
    "v_mfma_f32_16x16x32_bf16 a[128:131], v[32:35], v[150:153], a[128:131]\n"
    "v_add_f32 v144, v144, v113\n"
    "v_mfma_f32_16x16x32_bf16 a[128:131], v[36:39], v[154:157], a[128:131]\n"
    "v_mfma_f32_16x16x32_bf16 a[132:135], v[44:47], v[154:157], a[132:135]\n"
    "v_exp_f32 v114, v114\n"
    "v_mfma_f32_16x16x32_bf16 a[132:135], v[40:43], v[150:153], a[132:135]\n"
    "s_waitcnt vmcnt(13) lgkmcnt(0)\n"
    "s_barrier\n"
    "v_mfma_f32_16x16x32_bf16 a[136:139], v[48:51], v[150:153], a[136:139]\n"
    "v_add_u32 v254, s73, v254\n"
    "v_add_u32 v251, s73, v251\n"
    "v_mfma_f32_16x16x32_bf16 a[136:139], v[52:55], v[154:157], a[136:139]\n"
    "v_exp_f32 v115, v115\n"
    "v_mfma_f32_16x16x32_bf16 a[140:143], v[60:63], v[154:157], a[140:143]\n"
    "buffer_load_dwordx2 v[252:253], v250, s[64:67], s87 offen sc1\n"
    "v_mfma_f32_16x16x32_bf16 a[140:143], v[56:59], v[150:153], a[140:143]\n"
    "ds_read_b128 v[150:153], v254 offset:0\n"
    "v_add_f32 v144, v144, v114\n"
    "v_mfma_f32_16x16x32_bf16 a[144:147], v[32:35], v[158:161], a[144:147]\n"
    "v_exp_f32 v116, v116\n"
    "v_mfma_f32_16x16x32_bf16 a[144:147], v[36:39], v[162:165], a[144:147]\n"
    "ds_read_b128 v[154:157], v251 offset:0\n"
    "v_add_f32 v144, v144, v115\n"
    "v_mfma_f32_16x16x32_bf16 a[148:151], v[44:47], v[162:165], a[148:151]\n"
    "buffer_load_dwordx4 v[214:217], v248, s[40:43], s82 offen offset:256 nt\n"
    "v_mfma_f32_16x16x32_bf16 a[148:151], v[40:43], v[158:161], a[148:151]\n"
    "v_exp_f32 v117, v117\n"
    "v_mfma_f32_16x16x32_bf16 a[152:155], v[48:51], v[158:161], a[152:155]\n"
    "v_add_f32 v145, v145, v116\n"
    "v_add_f32 v145, v145, v117\n"
    "v_mfma_f32_16x16x32_bf16 a[152:155], v[52:55], v[162:165], a[152:155]\n"
    "buffer_load_dwordx4 v[218:221], v248, s[40:43], s82 offen offset:384 nt\n"
    "v_mfma_f32_16x16x32_bf16 a[156:159], v[60:63], v[162:165], a[156:159]\n"
    "v_exp_f32 v118, v118\n"
    "v_mfma_f32_16x16x32_bf16 a[156:159], v[56:59], v[158:161], a[156:159]\n"
    "ds_read_b128 v[158:161], v254 offset:2048\n"
    "ds_read_b128 v[162:165], v251 offset:2048\n"
    "v_mfma_f32_16x16x32_bf16 a[160:163], v[32:35], v[174:177], a[160:163]\n"
    "v_add_u32 v148, s73, v148\n"
    "ds_read_b128 v[166:169], v254 offset:4096\n"
    "v_mfma_f32_16x16x32_bf16 a[160:163], v[36:39], v[178:181], a[160:163]\n"
    "v_exp_f32 v119, v119\n"
    "v_mfma_f32_16x16x32_bf16 a[164:167], v[44:47], v[178:181], a[164:167]\n"
    "ds_read_b128 v[170:173], v251 offset:4096\n"
    "v_add_f32 v145, v145, v118\n"
    "v_mfma_f32_16x16x32_bf16 a[164:167], v[40:43], v[174:177], a[164:167]\n"
    "v_add_f32 v145, v145, v119\n"
    "v_cvt_f32_i32 v190, v190\n"
    "v_mfma_f32_16x16x32_bf16 a[168:171], v[48:51], v[174:177], a[168:171]\n"
    "v_exp_f32 v120, v120\n"
    "v_mfma_f32_16x16x32_bf16 a[168:171], v[52:55], v[178:181], a[168:171]\n"
    "v_add_f32 v146, v146, v120\n"
    "v_cvt_f32_i32 v191, v191\n"
    "v_mfma_f32_16x16x32_bf16 a[172:175], v[60:63], v[178:181], a[172:175]\n"
    "v_cvt_f32_i32 v198, v198\n"
    "v_cvt_f32_i32 v199, v199\n"
    "v_mfma_f32_16x16x32_bf16 a[172:175], v[56:59], v[174:177], a[172:175]\n"
    "v_exp_f32 v121, v121\n"
    "v_mfma_f32_16x16x32_bf16 a[176:179], v[32:35], v[182:185], a[176:179]\n"
    "ds_read_b128 v[230:233], v249 offset:128\n"
    "ds_read_b128 v[234:237], v249 offset:1152\n"
    "v_mfma_f32_16x16x32_bf16 a[176:179], v[36:39], v[186:189], a[176:179]\n"
    "buffer_load_dwordx4 v[222:225], v248, s[40:43], s83 offen offset:256 nt\n"
    "v_mfma_f32_16x16x32_bf16 a[180:183], v[44:47], v[186:189], a[180:183]\n"
    "v_exp_f32 v122, v122\n"
    "v_mfma_f32_16x16x32_bf16 a[180:183], v[40:43], v[182:185], a[180:183]\n"
    "v_cvt_pk_bf16_f32 v32, v64, v65\n"
    "v_cvt_pk_bf16_f32 v33, v66, v67\n"
    "v_add_f32 v146, v146, v122\n"
    "v_mfma_f32_16x16x32_bf16 a[184:187], v[48:51], v[182:185], a[184:187]\n"
    "v_cvt_pk_bf16_f32 v40, v68, v69\n"
    "v_add_f32 v146, v146, v121\n"
    "v_mfma_f32_16x16x32_bf16 a[184:187], v[52:55], v[186:189], a[184:187]\n"
    "v_exp_f32 v123, v123\n"
    "v_mfma_f32_16x16x32_bf16 a[188:191], v[60:63], v[186:189], a[188:191]\n"
    "buffer_load_dwordx4 v[226:229], v248, s[40:43], s83 offen offset:384 nt\n"
    "v_add_f32 v146, v146, v123\n"
    "v_mfma_f32_16x16x32_bf16 a[188:191], v[56:59], v[182:185], a[188:191]\n"
    "ds_read_b128 v[174:177], v254 offset:6144\n"
    "s_waitcnt lgkmcnt(1)\n"
    "v_mfma_f32_16x16x32_bf16 v[64:67], v[150:153], a[192:195], v[128:131]\n"
    "v_exp_f32 v124, v124\n"
    "v_mfma_f32_16x16x32_bf16 v[64:67], v[154:157], a[196:199], v[64:67]\n"
    "ds_read_b128 v[178:181], v251 offset:6144\n"
    "v_cvt_pk_bf16_f32 v41, v70, v71\n"
    "v_mfma_f32_16x16x32_bf16 v[64:67], v[158:161], a[200:203], v[64:67]\n"
    "ds_read_b128 v[182:185], v254 offset:8192\n"
    "ds_read_b128 v[186:189], v251 offset:8192\n"
    "v_mfma_f32_16x16x32_bf16 v[64:67], v[162:165], a[204:207], v[64:67]\n"
    "v_exp_f32 v125, v125\n"
    "v_mfma_f32_16x16x32_bf16 v[64:67], v[166:169], a[208:211], v[64:67]\n"
    "v_cvt_pk_bf16_f32 v48, v72, v73\n"
    "v_add_f32 v147, v147, v124\n"
    "v_mfma_f32_16x16x32_bf16 v[64:67], v[170:173], a[212:215], v[64:67]\n"
    "v_cvt_pk_bf16_f32 v49, v74, v75\n"
    "v_add_f32 v147, v147, v125\n"
    "v_mfma_f32_16x16x32_bf16 v[68:71], v[170:173], a[236:239], v[132:135]\n"
    "v_exp_f32 v126, v126\n"
    "v_mfma_f32_16x16x32_bf16 v[68:71], v[166:169], a[232:235], v[68:71]\n"
    "v_add_f32 v147, v147, v126\n"
    "v_fma_f32 v190, v190, v206, -v210\n"
    "v_mfma_f32_16x16x32_bf16 v[68:71], v[162:165], a[228:231], v[68:71]\n"
    "v_cvt_pk_bf16_f32 v56, v76, v77\n"
    "v_fma_f32 v191, v191, v207, -v211\n"
    "v_mfma_f32_16x16x32_bf16 v[68:71], v[158:161], a[224:227], v[68:71]\n"
    "v_exp_f32 v127, v127\n"
    "v_mfma_f32_16x16x32_bf16 v[68:71], v[154:157], a[220:223], v[68:71]\n"
    "v_add_f32 v147, v147, v127\n"
    "v_fma_f32 v198, v198, v206, -v210\n"
    "v_mfma_f32_16x16x32_bf16 v[68:71], v[150:153], a[216:219], v[68:71]\n"
    "v_cvt_pk_bf16_f32 v57, v78, v79\n"
    "v_mfma_f32_16x16x32_bf16 v[72:75], v[150:153], a[240:243], v[136:139]\n"
    "v_exp_f32 v64, v64\n"
    "v_mfma_f32_16x16x32_bf16 v[72:75], v[154:157], a[244:247], v[72:75]\n"
    "v_add_f32 v144, v144, v64\n"
    "v_fma_f32 v199, v199, v207, -v211\n"
    "v_mfma_f32_16x16x32_bf16 v[72:75], v[158:161], a[248:251], v[72:75]\n"
    "v_cvt_pk_bf16_f32 v34, v80, v81\n"
    "v_cvt_pk_bf16_f32 v238, v190, v191\n"
    "v_mfma_f32_16x16x32_bf16 v[72:75], v[162:165], a[252:255], v[72:75]\n"
    "v_exp_f32 v65, v65\n"
    "v_mfma_f32_16x16x32_bf16 v[72:75], v[166:169], v[0:3], v[72:75]\n"
    "v_add_f32 v144, v144, v65\n"
    "v_cvt_pk_bf16_f32 v242, v198, v199\n"
    "v_mfma_f32_16x16x32_bf16 v[72:75], v[170:173], v[4:7], v[72:75]\n"
    "v_cvt_pk_bf16_f32 v35, v82, v83\n"
    "v_cvt_f32_i32 v192, v192\n"
    "v_mfma_f32_16x16x32_bf16 v[76:79], v[170:173], v[28:31], v[140:143]\n"
    "v_exp_f32 v66, v66\n"
    "v_mfma_f32_16x16x32_bf16 v[76:79], v[166:169], v[24:27], v[76:79]\n"
    "ds_read_b128 v[170:173], v254 offset:10240\n"
    "ds_read_b128 v[166:169], v251 offset:10240\n"
    "v_mfma_f32_16x16x32_bf16 v[76:79], v[162:165], v[20:23], v[76:79]\n"
    "v_cvt_pk_bf16_f32 v42, v84, v85\n"
    "v_add_f32 v144, v144, v66\n"
    "v_mfma_f32_16x16x32_bf16 v[76:79], v[158:161], v[16:19], v[76:79]\n"
    "v_exp_f32 v67, v67\n"
    "v_mfma_f32_16x16x32_bf16 v[76:79], v[154:157], v[12:15], v[76:79]\n"
    "v_add_f32 v144, v144, v67\n"
    "v_cvt_f32_i32 v193, v193\n"
    "v_mfma_f32_16x16x32_bf16 v[76:79], v[150:153], v[8:11], v[76:79]\n"
    "ds_read_b128 v[162:165], v254 offset:12288\n"
    "s_waitcnt lgkmcnt(3)\n"
    "v_mfma_f32_16x16x32_bf16 v[80:83], v[174:177], a[192:195], v[128:131]\n"
    "v_exp_f32 v68, v68\n"
    "v_mfma_f32_16x16x32_bf16 v[80:83], v[178:181], a[196:199], v[80:83]\n"
    "ds_read_b128 v[158:161], v251 offset:12288\n"
    "v_cvt_pk_bf16_f32 v43, v86, v87\n"
    "v_mfma_f32_16x16x32_bf16 v[80:83], v[182:185], a[200:203], v[80:83]\n"
    "ds_read_b128 v[154:157], v254 offset:14336\n"
    "ds_read_b128 v[150:153], v251 offset:14336\n"
    "v_mfma_f32_16x16x32_bf16 v[80:83], v[186:189], a[204:207], v[80:83]\n"
    "v_exp_f32 v69, v69\n"
    "s_waitcnt lgkmcnt(4)\n"
    "v_mfma_f32_16x16x32_bf16 v[80:83], v[170:173], a[208:211], v[80:83]\n"
    "v_cvt_pk_bf16_f32 v50, v88, v89\n"
    "v_add_f32 v145, v145, v68\n"
    "v_mfma_f32_16x16x32_bf16 v[80:83], v[166:169], a[212:215], v[80:83]\n"
    "v_cvt_pk_bf16_f32 v51, v90, v91\n"
    "v_add_f32 v145, v145, v69\n"
    "v_mfma_f32_16x16x32_bf16 v[84:87], v[166:169], a[236:239], v[132:135]\n"
    "v_exp_f32 v70, v70\n"
    "v_mfma_f32_16x16x32_bf16 v[84:87], v[170:173], a[232:235], v[84:87]\n"
    "v_add_f32 v145, v145, v70\n"
    "v_cvt_f32_i32 v200, v200\n"
    "v_mfma_f32_16x16x32_bf16 v[84:87], v[186:189], a[228:231], v[84:87]\n"
    "v_cvt_pk_bf16_f32 v58, v92, v93\n"
    "v_cvt_f32_i32 v201, v201\n"
    "v_mfma_f32_16x16x32_bf16 v[84:87], v[182:185], a[224:227], v[84:87]\n"
    "v_exp_f32 v71, v71\n"
    "v_mfma_f32_16x16x32_bf16 v[84:87], v[178:181], a[220:223], v[84:87]\n"
    "v_add_f32 v145, v145, v71\n"
    "v_mfma_f32_16x16x32_bf16 v[84:87], v[174:177], a[216:219], v[84:87]\n"
    "v_cvt_pk_bf16_f32 v59, v94, v95\n"
    "v_fma_f32 v192, v192, v208, -v212\n"
    "v_mfma_f32_16x16x32_bf16 v[88:91], v[174:177], a[240:243], v[136:139]\n"
    "v_exp_f32 v72, v72\n"
    "v_mfma_f32_16x16x32_bf16 v[88:91], v[178:181], a[244:247], v[88:91]\n"
    "v_add_f32 v146, v146, v72\n"
    "v_fma_f32 v193, v193, v209, -v213\n"
    "v_mfma_f32_16x16x32_bf16 v[88:91], v[182:185], a[248:251], v[88:91]\n"
    "v_cvt_pk_bf16_f32 v36, v96, v97\n"
    "v_fma_f32 v200, v200, v208, -v212\n"
    "v_mfma_f32_16x16x32_bf16 v[88:91], v[186:189], a[252:255], v[88:91]\n"
    "v_exp_f32 v73, v73\n"
    "v_mfma_f32_16x16x32_bf16 v[88:91], v[170:173], v[0:3], v[88:91]\n"
    "v_add_f32 v146, v146, v73\n"
    "v_fma_f32 v201, v201, v209, -v213\n"
    "v_mfma_f32_16x16x32_bf16 v[88:91], v[166:169], v[4:7], v[88:91]\n"
    "v_cvt_pk_bf16_f32 v37, v98, v99\n"
    "v_cvt_pk_bf16_f32 v239, v192, v193\n"
    "v_mfma_f32_16x16x32_bf16 v[92:95], v[166:169], v[28:31], v[140:143]\n"
    "v_exp_f32 v74, v74\n"
    "v_mfma_f32_16x16x32_bf16 v[92:95], v[170:173], v[24:27], v[92:95]\n"
    "ds_read_b128 v[166:169], v254 offset:16384\n"
    "ds_read_b128 v[170:173], v251 offset:16384\n"
    "v_mfma_f32_16x16x32_bf16 v[92:95], v[186:189], v[20:23], v[92:95]\n"
    "v_cvt_pk_bf16_f32 v44, v100, v101\n"
    "v_add_f32 v146, v146, v74\n"
    "v_mfma_f32_16x16x32_bf16 v[92:95], v[182:185], v[16:19], v[92:95]\n"
    "v_exp_f32 v75, v75\n"
    "v_mfma_f32_16x16x32_bf16 v[92:95], v[178:181], v[12:15], v[92:95]\n"
    "v_add_f32 v146, v146, v75\n"
    "v_cvt_pk_bf16_f32 v243, v200, v201\n"
    "v_mfma_f32_16x16x32_bf16 v[92:95], v[174:177], v[8:11], v[92:95]\n"
    "ds_read_b128 v[186:189], v254 offset:18432\n"
    "s_waitcnt lgkmcnt(3)\n"
    "v_mfma_f32_16x16x32_bf16 v[96:99], v[162:165], a[192:195], v[128:131]\n"
    "v_exp_f32 v76, v76\n"
    "v_mfma_f32_16x16x32_bf16 v[96:99], v[158:161], a[196:199], v[96:99]\n"
    "ds_read_b128 v[182:185], v251 offset:18432\n"
    "v_cvt_pk_bf16_f32 v45, v102, v103\n"
    "v_mfma_f32_16x16x32_bf16 v[96:99], v[154:157], a[200:203], v[96:99]\n"
    "ds_read_b128 v[178:181], v254 offset:20480\n"
    "ds_read_b128 v[174:177], v251 offset:20480\n"
    "v_mfma_f32_16x16x32_bf16 v[96:99], v[150:153], a[204:207], v[96:99]\n"
    "v_exp_f32 v77, v77\n"
    "s_waitcnt lgkmcnt(4)\n"
    "v_mfma_f32_16x16x32_bf16 v[96:99], v[166:169], a[208:211], v[96:99]\n"
    "v_cvt_pk_bf16_f32 v52, v104, v105\n"
    "v_add_f32 v147, v147, v76\n"
    "v_mfma_f32_16x16x32_bf16 v[96:99], v[170:173], a[212:215], v[96:99]\n"
    "v_cvt_pk_bf16_f32 v53, v106, v107\n"
    "v_add_f32 v147, v147, v77\n"
    "v_mfma_f32_16x16x32_bf16 v[100:103], v[170:173], a[236:239], v[132:135]\n"
    "v_exp_f32 v78, v78\n"
    "v_mfma_f32_16x16x32_bf16 v[100:103], v[166:169], a[232:235], v[100:103]\n"
    "v_add_f32 v147, v147, v78\n"
    "v_mfma_f32_16x16x32_bf16 v[100:103], v[150:153], a[228:231], v[100:103]\n"
    "s_waitcnt vmcnt(4)\n"
    "v_cmp_ne_u64 vcc, s[80:81], v[252:253]\n"
    "v_mfma_f32_16x16x32_bf16 v[100:103], v[154:157], a[224:227], v[100:103]\n"
    "v_exp_f32 v79, v79\n"
    "v_mfma_f32_16x16x32_bf16 v[100:103], v[158:161], a[220:223], v[100:103]\n"
    "s_nop 0\n"
    "s_cbranch_vccnz SPK%=\n"
    "RTK%=:\n"
    "v_cvt_pk_bf16_f32 v60, v108, v109\n"
    "v_mfma_f32_16x16x32_bf16 v[100:103], v[162:165], a[216:219], v[100:103]\n"
    "s_add_u32 s87, s87, 256\n"
    "v_cvt_pk_bf16_f32 v61, v110, v111\n"
    "v_mfma_f32_16x16x32_bf16 v[104:107], v[162:165], a[240:243], v[136:139]\n"
    "v_exp_f32 v80, v80\n"
    "v_mfma_f32_16x16x32_bf16 v[104:107], v[158:161], a[244:247], v[104:107]\n"
    "v_add_f32 v147, v147, v79\n"
    "v_add_f32 v144, v144, v80\n"
    "v_mfma_f32_16x16x32_bf16 v[104:107], v[154:157], a[248:251], v[104:107]\n"
    "v_cvt_pk_bf16_f32 v38, v112, v113\n"
    "v_cvt_f32_i32 v194, v194\n"
    "v_mfma_f32_16x16x32_bf16 v[104:107], v[150:153], a[252:255], v[104:107]\n"
    "v_exp_f32 v81, v81\n"
    "v_mfma_f32_16x16x32_bf16 v[104:107], v[166:169], v[0:3], v[104:107]\n"
    "v_add_f32 v144, v144, v81\n"
    "v_cvt_f32_i32 v195, v195\n"
    "v_mfma_f32_16x16x32_bf16 v[104:107], v[170:173], v[4:7], v[104:107]\n"
    "s_mov_b32 m0, s74\n"
    "v_cvt_pk_bf16_f32 v39, v114, v115\n"
    "v_mfma_f32_16x16x32_bf16 v[108:111], v[170:173], v[28:31], v[140:143]\n"
    "v_exp_f32 v82, v82\n"
    "v_mfma_f32_16x16x32_bf16 v[108:111], v[166:169], v[24:27], v[108:111]\n"
    "ds_read_b128 v[170:173], v254 offset:22528\n"
    "ds_read_b128 v[166:169], v251 offset:22528\n"
    "v_mfma_f32_16x16x32_bf16 v[108:111], v[150:153], v[20:23], v[108:111]\n"
    "s_add_u32 s74, s74, s73\n"
    "v_cvt_pk_bf16_f32 v46, v116, v117\n"
    "v_mfma_f32_16x16x32_bf16 v[108:111], v[154:157], v[16:19], v[108:111]\n"
    "v_exp_f32 v83, v83\n"
    "v_mfma_f32_16x16x32_bf16 v[108:111], v[158:161], v[12:15], v[108:111]\n"
    "s_cmp_ge_u32 s74, s75\n"
    "s_cselect_b32 s73, 0xfffe8000, s76\n"
    "v_mfma_f32_16x16x32_bf16 v[108:111], v[162:165], v[8:11], v[108:111]\n"
    "ds_read_b128 v[150:153], v148 offset:24576\n"
    "ds_read_b128 v[206:209], v249 offset:256\n"
    "s_waitcnt lgkmcnt(4)\n"
    "v_mfma_f32_16x16x32_bf16 v[112:115], v[186:189], a[192:195], v[128:131]\n"
    "v_exp_f32 v84, v84\n"
    "v_mfma_f32_16x16x32_bf16 v[112:115], v[182:185], a[196:199], v[112:115]\n"
    "ds_read_b128 v[154:157], v148 offset:36864\n"
    "ds_read_b128 v[210:213], v249 offset:1280\n"
    "v_mfma_f32_16x16x32_bf16 v[112:115], v[178:181], a[200:203], v[112:115]\n"
    "buffer_load_dwordx4 v149, s[36:39], s72 offen offset:0 sc1 lds\n"
    "v_mfma_f32_16x16x32_bf16 v[112:115], v[174:177], a[204:207], v[112:115]\n"
    "v_exp_f32 v85, v85\n"
    "s_waitcnt lgkmcnt(4)\n"
    "v_mfma_f32_16x16x32_bf16 v[112:115], v[170:173], a[208:211], v[112:115]\n"
    "v_cvt_pk_bf16_f32 v47, v118, v119\n"
    "v_cvt_pk_bf16_f32 v54, v120, v121\n"
    "v_mfma_f32_16x16x32_bf16 v[112:115], v[166:169], a[212:215], v[112:115]\n"
    "v_cvt_pk_bf16_f32 v55, v122, v123\n"
    "v_add_f32 v144, v144, v82\n"
    "v_mfma_f32_16x16x32_bf16 v[116:119], v[166:169], a[236:239], v[132:135]\n"
    "v_exp_f32 v86, v86\n"
    "v_mfma_f32_16x16x32_bf16 v[116:119], v[170:173], a[232:235], v[116:119]\n"
    "buffer_load_dwordx4 v149, s[36:39], s72 offen offset:1024 sc1 lds\n"
    "v_mfma_f32_16x16x32_bf16 v[116:119], v[174:177], a[228:231], v[116:119]\n"
    "ds_read_b128 v[158:161], v148 offset:25600\n"
    "ds_read_b128 v[162:165], v148 offset:37888\n"
    "v_mfma_f32_16x16x32_bf16 v[116:119], v[178:181], a[224:227], v[116:119]\n"
    "v_exp_f32 v87, v87\n"
    "v_mfma_f32_16x16x32_bf16 v[116:119], v[182:185], a[220:223], v[116:119]\n"
    "v_cvt_pk_bf16_f32 v62, v124, v125\n"
    "v_add_f32 v144, v144, v83\n"
    "v_mfma_f32_16x16x32_bf16 v[116:119], v[186:189], a[216:219], v[116:119]\n"
    "v_cvt_pk_bf16_f32 v63, v126, v127\n"
    "v_add_f32 v145, v145, v84\n"
    "v_mfma_f32_16x16x32_bf16 v[120:123], v[186:189], a[240:243], v[136:139]\n"
    "v_exp_f32 v88, v88\n"
    "v_mfma_f32_16x16x32_bf16 v[120:123], v[182:185], a[244:247], v[120:123]\n"
    "buffer_load_dwordx4 v149, s[36:39], s72 offen offset:2048 sc1 lds\n"
    "v_mfma_f32_16x16x32_bf16 v[120:123], v[178:181], a[248:251], v[120:123]\n"
    "v_add_f32 v145, v145, v85\n"
    "v_add_f32 v145, v145, v86\n"
    "v_mfma_f32_16x16x32_bf16 v[120:123], v[174:177], a[252:255], v[120:123]\n"
    "v_exp_f32 v89, v89\n"
    "v_mfma_f32_16x16x32_bf16 v[120:123], v[170:173], v[0:3], v[120:123]\n"
    "v_add_f32 v145, v145, v87\n"
    "v_add_f32 v146, v146, v88\n"
    "v_mfma_f32_16x16x32_bf16 v[120:123], v[166:169], v[4:7], v[120:123]\n"
    "v_add_f32 v146, v146, v89\n"
    "v_cvt_f32_i32 v202, v202\n"
    "v_mfma_f32_16x16x32_bf16 v[124:127], v[166:169], v[28:31], v[140:143]\n"
    "v_exp_f32 v90, v90\n"
    "v_mfma_f32_16x16x32_bf16 v[124:127], v[170:173], v[24:27], v[124:127]\n"
    "ds_read_b128 v[166:169], v148 offset:26624\n"
    "ds_read_b128 v[170:173], v148 offset:38912\n"
    "v_mfma_f32_16x16x32_bf16 v[124:127], v[174:177], v[20:23], v[124:127]\n"
    "buffer_load_dwordx4 v149, s[36:39], s72 offen offset:3072 sc1 lds\n"
    "v_mfma_f32_16x16x32_bf16 v[124:127], v[178:181], v[16:19], v[124:127]\n"
    "v_exp_f32 v91, v91\n"
    "v_mfma_f32_16x16x32_bf16 v[124:127], v[182:185], v[12:15], v[124:127]\n"
    "s_add_u32 s72, s72, 4096\n"
    "v_add_f32 v146, v146, v90\n"
    "v_mfma_f32_16x16x32_bf16 v[124:127], v[186:189], v[8:11], v[124:127]\n"
    "ds_read_b128 v[174:177], v148 offset:27648\n"
    "s_waitcnt lgkmcnt(3)\n"
    "v_mfma_f32_16x16x32_bf16 a[0:3], v[32:35], v[150:153], a[0:3]\n"
    "v_exp_f32 v92, v92\n"
    "v_mfma_f32_16x16x32_bf16 a[0:3], v[36:39], v[154:157], a[0:3]\n"
    "ds_read_b128 v[178:181], v148 offset:39936\n"
    "s_add_u32 m0, m0, 4096\n"
    "v_mfma_f32_16x16x32_bf16 a[4:7], v[44:47], v[154:157], a[4:7]\n"
    "v_add_f32 v146, v146, v91\n"
    "v_add_f32 v147, v147, v92\n"
    "v_mfma_f32_16x16x32_bf16 a[4:7], v[40:43], v[150:153], a[4:7]\n"
    "v_exp_f32 v93, v93\n"
    "v_mfma_f32_16x16x32_bf16 a[8:11], v[48:51], v[150:153], a[8:11]\n"
    "v_add_f32 v147, v147, v93\n"
    "v_cvt_f32_i32 v203, v203\n"
    "v_mfma_f32_16x16x32_bf16 a[8:11], v[52:55], v[154:157], a[8:11]\n"
    "v_fma_f32 v194, v194, v230, -v234\n"
    "v_mfma_f32_16x16x32_bf16 a[12:15], v[60:63], v[154:157], a[12:15]\n"
    "v_exp_f32 v94, v94\n"
    "v_mfma_f32_16x16x32_bf16 a[12:15], v[56:59], v[150:153], a[12:15]\n"
    "buffer_load_dwordx4 v149, s[36:39], s72 offen offset:0 sc1 lds\n"
    "v_mfma_f32_16x16x32_bf16 a[16:19], v[32:35], v[158:161], a[16:19]\n"
    "ds_read_b128 v[182:185], v148 offset:28672\n"
    "ds_read_b128 v[186:189], v148 offset:40960\n"
    "v_mfma_f32_16x16x32_bf16 a[16:19], v[36:39], v[162:165], a[16:19]\n"
    "v_exp_f32 v95, v95\n"
    "v_mfma_f32_16x16x32_bf16 a[20:23], v[44:47], v[162:165], a[20:23]\n"
    "v_add_f32 v147, v147, v94\n"
    "v_add_f32 v147, v147, v95\n"
    "v_mfma_f32_16x16x32_bf16 a[20:23], v[40:43], v[158:161], a[20:23]\n"
    "v_fma_f32 v195, v195, v231, -v235\n"
    "v_fma_f32 v202, v202, v230, -v234\n"
    "v_mfma_f32_16x16x32_bf16 a[24:27], v[48:51], v[158:161], a[24:27]\n"
    "v_exp_f32 v96, v96\n"
    "v_mfma_f32_16x16x32_bf16 a[24:27], v[52:55], v[162:165], a[24:27]\n"
    "buffer_load_dwordx4 v149, s[36:39], s72 offen offset:1024 sc1 lds\n"
    "v_mfma_f32_16x16x32_bf16 a[28:31], v[60:63], v[162:165], a[28:31]\n"
    "v_add_f32 v144, v144, v96\n"
    "v_fma_f32 v203, v203, v231, -v235\n"
    "v_mfma_f32_16x16x32_bf16 a[28:31], v[56:59], v[158:161], a[28:31]\n"
    "v_exp_f32 v97, v97\n"
    "s_waitcnt lgkmcnt(0)\n"
    "v_mfma_f32_16x16x32_bf16 a[32:35], v[32:35], v[166:169], a[32:35]\n"
    "ds_read_b128 v[150:153], v148 offset:29696\n"
    "v_add_f32 v144, v144, v97\n"
    "v_mfma_f32_16x16x32_bf16 a[32:35], v[36:39], v[170:173], a[32:35]\n"
    "ds_read_b128 v[154:157], v148 offset:41984\n"
    "v_mfma_f32_16x16x32_bf16 a[36:39], v[44:47], v[170:173], a[36:39]\n"
    "v_exp_f32 v98, v98\n"
    "v_mfma_f32_16x16x32_bf16 a[36:39], v[40:43], v[166:169], a[36:39]\n"
    "buffer_load_dwordx4 v149, s[36:39], s72 offen offset:2048 sc1 lds\n"
    "v_mfma_f32_16x16x32_bf16 a[40:43], v[48:51], v[166:169], a[40:43]\n"
    "v_add_f32 v144, v144, v98\n"
    "v_cvt_pk_bf16_f32 v240, v194, v195\n"
    "v_mfma_f32_16x16x32_bf16 a[40:43], v[52:55], v[170:173], a[40:43]\n"
    "v_exp_f32 v99, v99\n"
    "v_mfma_f32_16x16x32_bf16 a[44:47], v[60:63], v[170:173], a[44:47]\n"
    "v_add_f32 v144, v144, v99\n"
    "v_cvt_pk_bf16_f32 v244, v202, v203\n"
    "v_mfma_f32_16x16x32_bf16 a[44:47], v[56:59], v[166:169], a[44:47]\n"
    "ds_read_b128 v[158:161], v148 offset:30720\n"
    "v_cvt_f32_i32 v196, v196\n"
    "v_mfma_f32_16x16x32_bf16 a[48:51], v[32:35], v[174:177], a[48:51]\n"
    "v_exp_f32 v100, v100\n"
    "v_mfma_f32_16x16x32_bf16 a[48:51], v[36:39], v[178:181], a[48:51]\n"
    "ds_read_b128 v[162:165], v148 offset:43008\n"
    "v_add_f32 v145, v145, v100\n"
    "v_mfma_f32_16x16x32_bf16 a[52:55], v[44:47], v[178:181], a[52:55]\n"
    "buffer_load_dwordx4 v149, s[36:39], s72 offen offset:3072 sc1 lds\n"
    "v_mfma_f32_16x16x32_bf16 a[52:55], v[40:43], v[174:177], a[52:55]\n"
    "v_exp_f32 v101, v101\n"
    "v_mfma_f32_16x16x32_bf16 a[56:59], v[48:51], v[174:177], a[56:59]\n"
    "s_add_u32 s72, s72, 4096\n"
    "v_add_f32 v145, v145, v101\n"
    "v_mfma_f32_16x16x32_bf16 a[56:59], v[52:55], v[178:181], a[56:59]\n"
    "s_add_u32 m0, m0, 4096\n"
    "v_cvt_f32_i32 v197, v197\n"
    "v_mfma_f32_16x16x32_bf16 a[60:63], v[60:63], v[178:181], a[60:63]\n"
    "v_exp_f32 v102, v102\n"
    "v_mfma_f32_16x16x32_bf16 a[60:63], v[56:59], v[174:177], a[60:63]\n"
    "v_add_f32 v145, v145, v102\n"
    "v_mfma_f32_16x16x32_bf16 a[64:67], v[32:35], v[182:185], a[64:67]\n"
    "ds_read_b128 v[166:169], v148 offset:31744\n"
    "ds_read_b128 v[170:173], v148 offset:44032\n"
    "v_mfma_f32_16x16x32_bf16 a[64:67], v[36:39], v[186:189], a[64:67]\n"
    "v_exp_f32 v103, v103\n"
    "v_mfma_f32_16x16x32_bf16 a[68:71], v[44:47], v[186:189], a[68:71]\n"
    "v_add_f32 v145, v145, v103\n"
    "v_cvt_f32_i32 v204, v204\n"
    "v_mfma_f32_16x16x32_bf16 a[68:71], v[40:43], v[182:185], a[68:71]\n"
    "buffer_load_dwordx4 v149, s[36:39], s72 offen offset:0 sc1 lds\n"
    "v_mfma_f32_16x16x32_bf16 a[72:75], v[48:51], v[182:185], a[72:75]\n"
    "v_exp_f32 v104, v104\n"
    "v_mfma_f32_16x16x32_bf16 a[72:75], v[52:55], v[186:189], a[72:75]\n"
    "v_add_f32 v146, v146, v104\n"
    "v_cvt_f32_i32 v205, v205\n"
    "v_mfma_f32_16x16x32_bf16 a[76:79], v[60:63], v[186:189], a[76:79]\n"
    "v_fma_f32 v196, v196, v232, -v236\n"
    "v_fma_f32 v197, v197, v233, -v237\n"
    "v_mfma_f32_16x16x32_bf16 a[76:79], v[56:59], v[182:185], a[76:79]\n"
    "v_exp_f32 v105, v105\n"
    "s_waitcnt lgkmcnt(0)\n"
    "v_mfma_f32_16x16x32_bf16 a[80:83], v[32:35], v[150:153], a[80:83]\n"
    "v_add_f32 v146, v146, v105\n"
    "v_mfma_f32_16x16x32_bf16 a[80:83], v[36:39], v[154:157], a[80:83]\n"
    "buffer_load_dwordx4 v149, s[36:39], s72 offen offset:1024 sc1 lds\n"
    "v_mfma_f32_16x16x32_bf16 a[84:87], v[44:47], v[154:157], a[84:87]\n"
    "v_exp_f32 v106, v106\n"
    "v_mfma_f32_16x16x32_bf16 a[84:87], v[40:43], v[150:153], a[84:87]\n"
    "v_add_f32 v146, v146, v106\n"
    "v_fma_f32 v204, v204, v232, -v236\n"
    "v_mfma_f32_16x16x32_bf16 a[88:91], v[48:51], v[150:153], a[88:91]\n"
    "v_fma_f32 v205, v205, v233, -v237\n"
    "v_cvt_pk_bf16_f32 v241, v196, v197\n"
    "v_mfma_f32_16x16x32_bf16 a[88:91], v[52:55], v[154:157], a[88:91]\n"
    "v_exp_f32 v107, v107\n"
    "v_mfma_f32_16x16x32_bf16 a[92:95], v[60:63], v[154:157], a[92:95]\n"
    "buffer_load_dwordx4 v149, s[36:39], s72 offen offset:2048 sc1 lds\n"
    "v_mfma_f32_16x16x32_bf16 a[92:95], v[56:59], v[150:153], a[92:95]\n"
    "ds_read_b128 v[150:153], v148 offset:32768\n"
    "ds_read_b128 v[154:157], v148 offset:45056\n"
    "v_mfma_f32_16x16x32_bf16 a[96:99], v[32:35], v[158:161], a[96:99]\n"
    "v_exp_f32 v108, v108\n"
    "v_mfma_f32_16x16x32_bf16 a[96:99], v[36:39], v[162:165], a[96:99]\n"
    "v_add_f32 v146, v146, v107\n"
    "v_add_f32 v147, v147, v108\n"
    "v_mfma_f32_16x16x32_bf16 a[100:103], v[44:47], v[162:165], a[100:103]\n"
    "v_cvt_pk_bf16_f32 v245, v204, v205\n"
    "v_mfma_f32_16x16x32_bf16 a[100:103], v[40:43], v[158:161], a[100:103]\n"
    "v_exp_f32 v109, v109\n"
    "v_mfma_f32_16x16x32_bf16 a[104:107], v[48:51], v[158:161], a[104:107]\n"
    "buffer_load_dwordx4 v149, s[36:39], s72 offen offset:3072 sc1 lds\n"
    "v_mfma_f32_16x16x32_bf16 a[104:107], v[52:55], v[162:165], a[104:107]\n"
    "s_add_u32 s72, s72, 16384\n"
    "v_add_f32 v147, v147, v109\n"
    "v_mfma_f32_16x16x32_bf16 a[108:111], v[60:63], v[162:165], a[108:111]\n"
    "v_exp_f32 v110, v110\n"
    "v_mfma_f32_16x16x32_bf16 a[108:111], v[56:59], v[158:161], a[108:111]\n"
    "ds_read_b128 v[158:161], v148 offset:33792\n"
    "ds_read_b128 v[162:165], v148 offset:46080\n"
    "v_mfma_f32_16x16x32_bf16 a[112:115], v[32:35], v[166:169], a[112:115]\n"
    "ds_read_b128 v[174:177], v148 offset:34816\n"
    "ds_read_b128 v[178:181], v148 offset:47104\n"
    "v_mfma_f32_16x16x32_bf16 a[112:115], v[36:39], v[170:173], a[112:115]\n"
    "v_exp_f32 v111, v111\n"
    "v_mfma_f32_16x16x32_bf16 a[116:119], v[44:47], v[170:173], a[116:119]\n"
    "ds_read_b128 v[182:185], v148 offset:35840\n"
    "v_add_f32 v147, v147, v110\n"
    "v_mfma_f32_16x16x32_bf16 a[116:119], v[40:43], v[166:169], a[116:119]\n"
    "ds_read_b128 v[186:189], v148 offset:48128\n"
    "v_add_f32 v147, v147, v111\n"
    "v_mfma_f32_16x16x32_bf16 a[120:123], v[48:51], v[166:169], a[120:123]\n"
    "v_exp_f32 v112, v112\n"
    "v_mfma_f32_16x16x32_bf16 a[120:123], v[52:55], v[170:173], a[120:123]\n"
    "buffer_store_dwordx4 v[238:241], v149, s[52:55], s85 offen offset:0 sc1\n"
    "v_mfma_f32_16x16x32_bf16 a[124:127], v[60:63], v[170:173], a[124:127]\n"
    "buffer_store_dwordx4 v[242:245], v149, s[52:55], s85 offen offset:1024 sc1\n"
    "v_mfma_f32_16x16x32_bf16 a[124:127], v[56:59], v[166:169], a[124:127]\n"
    "v_exp_f32 v113, v113\n"
    "s_waitcnt lgkmcnt(2)\n"
    "v_mfma_f32_16x16x32_bf16 a[128:131], v[32:35], v[150:153], a[128:131]\n"
    "v_add_f32 v144, v144, v112\n"
    "v_add_f32 v144, v144, v113\n"
    "v_mfma_f32_16x16x32_bf16 a[128:131], v[36:39], v[154:157], a[128:131]\n"
    "v_mfma_f32_16x16x32_bf16 a[132:135], v[44:47], v[154:157], a[132:135]\n"
    "v_exp_f32 v114, v114\n"
    "v_mfma_f32_16x16x32_bf16 a[132:135], v[40:43], v[150:153], a[132:135]\n"
    "s_waitcnt vmcnt(14) lgkmcnt(0)\n"
    "s_barrier\n"
    "v_mfma_f32_16x16x32_bf16 a[136:139], v[48:51], v[150:153], a[136:139]\n"
    "v_add_u32 v254, s73, v254\n"
    "v_add_u32 v251, s73, v251\n"
    "v_mfma_f32_16x16x32_bf16 a[136:139], v[52:55], v[154:157], a[136:139]\n"
    "v_exp_f32 v115, v115\n"
    "v_mfma_f32_16x16x32_bf16 a[140:143], v[60:63], v[154:157], a[140:143]\n"
    "buffer_load_dwordx4 v[190:193], v248, s[40:43], s82 offen offset:512 nt\n"
    "v_mfma_f32_16x16x32_bf16 a[140:143], v[56:59], v[150:153], a[140:143]\n"
    "ds_read_b128 v[150:153], v254 offset:0\n"
    "v_add_f32 v144, v144, v114\n"
    "v_mfma_f32_16x16x32_bf16 a[144:147], v[32:35], v[158:161], a[144:147]\n"
    "v_exp_f32 v116, v116\n"
    "v_mfma_f32_16x16x32_bf16 a[144:147], v[36:39], v[162:165], a[144:147]\n"
    "ds_read_b128 v[154:157], v251 offset:0\n"
    "v_add_f32 v144, v144, v115\n"
    "v_mfma_f32_16x16x32_bf16 a[148:151], v[44:47], v[162:165], a[148:151]\n"
    "v_add_f32 v145, v145, v116\n"
    "v_cvt_f32_i32 v214, v214\n"
    "v_mfma_f32_16x16x32_bf16 a[148:151], v[40:43], v[158:161], a[148:151]\n"
    "v_exp_f32 v117, v117\n"
    "v_mfma_f32_16x16x32_bf16 a[152:155], v[48:51], v[158:161], a[152:155]\n"
    "v_add_f32 v145, v145, v117\n"
    "v_cvt_f32_i32 v215, v215\n"
    "v_mfma_f32_16x16x32_bf16 a[152:155], v[52:55], v[162:165], a[152:155]\n"
    "buffer_load_dwordx4 v[194:197], v248, s[40:43], s82 offen offset:640 nt\n"
    "v_mfma_f32_16x16x32_bf16 a[156:159], v[60:63], v[162:165], a[156:159]\n"
    "v_exp_f32 v118, v118\n"
    "v_mfma_f32_16x16x32_bf16 a[156:159], v[56:59], v[158:161], a[156:159]\n"
    "ds_read_b128 v[158:161], v254 offset:2048\n"
    "ds_read_b128 v[162:165], v251 offset:2048\n"
    "v_mfma_f32_16x16x32_bf16 a[160:163], v[32:35], v[174:177], a[160:163]\n"
    "v_add_u32 v148, s73, v148\n"
    "ds_read_b128 v[166:169], v254 offset:4096\n"
    "v_mfma_f32_16x16x32_bf16 a[160:163], v[36:39], v[178:181], a[160:163]\n"
    "v_exp_f32 v119, v119\n"
    "v_mfma_f32_16x16x32_bf16 a[164:167], v[44:47], v[178:181], a[164:167]\n"
    "ds_read_b128 v[170:173], v251 offset:4096\n"
    "v_add_f32 v145, v145, v118\n"
    "v_mfma_f32_16x16x32_bf16 a[164:167], v[40:43], v[174:177], a[164:167]\n"
    "v_add_f32 v145, v145, v119\n"
    "v_cvt_f32_i32 v222, v222\n"
    "v_mfma_f32_16x16x32_bf16 a[168:171], v[48:51], v[174:177], a[168:171]\n"
    "v_exp_f32 v120, v120\n"
    "v_mfma_f32_16x16x32_bf16 a[168:171], v[52:55], v[178:181], a[168:171]\n"
    "v_add_f32 v146, v146, v120\n"
    "v_cvt_f32_i32 v223, v223\n"
    "v_mfma_f32_16x16x32_bf16 a[172:175], v[60:63], v[178:181], a[172:175]\n"
    "buffer_load_dwordx4 v[198:201], v248, s[40:43], s83 offen offset:512 nt\n"
    "v_mfma_f32_16x16x32_bf16 a[172:175], v[56:59], v[174:177], a[172:175]\n"
    "v_exp_f32 v121, v121\n"
    "v_mfma_f32_16x16x32_bf16 a[176:179], v[32:35], v[182:185], a[176:179]\n"
    "ds_read_b128 v[230:233], v249 offset:384\n"
    "ds_read_b128 v[234:237], v249 offset:1408\n"
    "v_mfma_f32_16x16x32_bf16 a[176:179], v[36:39], v[186:189], a[176:179]\n"
    "v_cvt_pk_bf16_f32 v32, v64, v65\n"
    "v_cvt_pk_bf16_f32 v33, v66, v67\n"
    "v_mfma_f32_16x16x32_bf16 a[180:183], v[44:47], v[186:189], a[180:183]\n"
    "v_exp_f32 v122, v122\n"
    "v_mfma_f32_16x16x32_bf16 a[180:183], v[40:43], v[182:185], a[180:183]\n"
    "v_add_f32 v146, v146, v121\n"
    "v_add_f32 v146, v146, v122\n"
    "v_mfma_f32_16x16x32_bf16 a[184:187], v[48:51], v[182:185], a[184:187]\n"
    "v_cvt_pk_bf16_f32 v40, v68, v69\n"
    "v_fma_f32 v214, v214, v206, -v210\n"
    "v_mfma_f32_16x16x32_bf16 a[184:187], v[52:55], v[186:189], a[184:187]\n"
    "v_exp_f32 v123, v123\n"
    "v_mfma_f32_16x16x32_bf16 a[188:191], v[60:63], v[186:189], a[188:191]\n"
    "buffer_load_dwordx4 v[202:205], v248, s[40:43], s83 offen offset:640 nt\n"
    "v_add_f32 v146, v146, v123\n"
    "v_mfma_f32_16x16x32_bf16 a[188:191], v[56:59], v[182:185], a[188:191]\n"
    "ds_read_b128 v[174:177], v254 offset:6144\n"
    "s_waitcnt lgkmcnt(1)\n"
    "v_mfma_f32_16x16x32_bf16 v[64:67], v[150:153], a[192:195], v[128:131]\n"
    "v_exp_f32 v124, v124\n"
    "v_mfma_f32_16x16x32_bf16 v[64:67], v[154:157], a[196:199], v[64:67]\n"
    "ds_read_b128 v[178:181], v251 offset:6144\n"
    "s_add_u32 s82, s82, 196608\n"
    "v_mfma_f32_16x16x32_bf16 v[64:67], v[158:161], a[200:203], v[64:67]\n"
    "ds_read_b128 v[182:185], v254 offset:8192\n"
    "ds_read_b128 v[186:189], v251 offset:8192\n"
    "v_mfma_f32_16x16x32_bf16 v[64:67], v[162:165], a[204:207], v[64:67]\n"
    "v_exp_f32 v125, v125\n"
    "v_mfma_f32_16x16x32_bf16 v[64:67], v[166:169], a[208:211], v[64:67]\n"
    "v_cvt_pk_bf16_f32 v41, v70, v71\n"
    "v_cvt_pk_bf16_f32 v48, v72, v73\n"
    "v_mfma_f32_16x16x32_bf16 v[64:67], v[170:173], a[212:215], v[64:67]\n"
    "s_add_u32 s83, s83, 196608\n"
    "v_cvt_pk_bf16_f32 v49, v74, v75\n"
    "v_mfma_f32_16x16x32_bf16 v[68:71], v[170:173], a[236:239], v[132:135]\n"
    "v_exp_f32 v126, v126\n"
    "v_mfma_f32_16x16x32_bf16 v[68:71], v[166:169], a[232:235], v[68:71]\n"
    "v_add_f32 v147, v147, v124\n"
    "v_add_f32 v147, v147, v125\n"
    "v_mfma_f32_16x16x32_bf16 v[68:71], v[162:165], a[228:231], v[68:71]\n"
    "v_cvt_pk_bf16_f32 v56, v76, v77\n"
    "v_add_f32 v147, v147, v126\n"
    "v_mfma_f32_16x16x32_bf16 v[68:71], v[158:161], a[224:227], v[68:71]\n"
    "v_exp_f32 v127, v127\n"
    "v_mfma_f32_16x16x32_bf16 v[68:71], v[154:157], a[220:223], v[68:71]\n"
    "v_add_f32 v147, v147, v127\n"
    "v_fma_f32 v215, v215, v207, -v211\n"
    "v_mfma_f32_16x16x32_bf16 v[68:71], v[150:153], a[216:219], v[68:71]\n"
    "v_cvt_pk_bf16_f32 v57, v78, v79\n"
    "v_fma_f32 v222, v222, v206, -v210\n"
    "v_mfma_f32_16x16x32_bf16 v[72:75], v[150:153], a[240:243], v[136:139]\n"
    "v_exp_f32 v64, v64\n"
    "v_mfma_f32_16x16x32_bf16 v[72:75], v[154:157], a[244:247], v[72:75]\n"
    "v_add_f32 v144, v144, v64\n"
    "v_fma_f32 v223, v223, v207, -v211\n"
    "v_mfma_f32_16x16x32_bf16 v[72:75], v[158:161], a[248:251], v[72:75]\n"
    "s_add_u32 s84, s84, 3072\n"
    "v_cvt_pk_bf16_f32 v34, v80, v81\n"
    "v_mfma_f32_16x16x32_bf16 v[72:75], v[162:165], a[252:255], v[72:75]\n"
    "v_exp_f32 v65, v65\n"
    "v_mfma_f32_16x16x32_bf16 v[72:75], v[166:169], v[0:3], v[72:75]\n"
    "v_add_f32 v144, v144, v65\n"
    "v_cvt_pk_bf16_f32 v238, v214, v215\n"
    "v_mfma_f32_16x16x32_bf16 v[72:75], v[170:173], v[4:7], v[72:75]\n"
    "v_cvt_pk_bf16_f32 v35, v82, v83\n"
    "v_cvt_pk_bf16_f32 v242, v222, v223\n"
    "v_mfma_f32_16x16x32_bf16 v[76:79], v[170:173], v[28:31], v[140:143]\n"
    "v_exp_f32 v66, v66\n"
    "v_mfma_f32_16x16x32_bf16 v[76:79], v[166:169], v[24:27], v[76:79]\n"
    "ds_read_b128 v[170:173], v254 offset:10240\n"
    "ds_read_b128 v[166:169], v251 offset:10240\n"
    "v_mfma_f32_16x16x32_bf16 v[76:79], v[162:165], v[20:23], v[76:79]\n"
    "v_cvt_pk_bf16_f32 v42, v84, v85\n"
    "v_add_f32 v144, v144, v66\n"
    "v_mfma_f32_16x16x32_bf16 v[76:79], v[158:161], v[16:19], v[76:79]\n"
    "v_exp_f32 v67, v67\n"
    "v_mfma_f32_16x16x32_bf16 v[76:79], v[154:157], v[12:15], v[76:79]\n"
    "v_add_f32 v144, v144, v67\n"
    "v_mfma_f32_16x16x32_bf16 v[76:79], v[150:153], v[8:11], v[76:79]\n"
    "ds_read_b128 v[162:165], v254 offset:12288\n"
    "s_waitcnt lgkmcnt(3)\n"
    "v_mfma_f32_16x16x32_bf16 v[80:83], v[174:177], a[192:195], v[128:131]\n"
    "v_exp_f32 v68, v68\n"
    "v_mfma_f32_16x16x32_bf16 v[80:83], v[178:181], a[196:199], v[80:83]\n"
    "ds_read_b128 v[158:161], v251 offset:12288\n"
    "v_cvt_pk_bf16_f32 v43, v86, v87\n"
    "v_mfma_f32_16x16x32_bf16 v[80:83], v[182:185], a[200:203], v[80:83]\n"
    "ds_read_b128 v[154:157], v254 offset:14336\n"
    "ds_read_b128 v[150:153], v251 offset:14336\n"
    "v_mfma_f32_16x16x32_bf16 v[80:83], v[186:189], a[204:207], v[80:83]\n"
    "v_exp_f32 v69, v69\n"
    "s_waitcnt lgkmcnt(4)\n"
    "v_mfma_f32_16x16x32_bf16 v[80:83], v[170:173], a[208:211], v[80:83]\n"
    "v_cvt_pk_bf16_f32 v50, v88, v89\n"
    "v_add_f32 v145, v145, v68\n"
    "v_mfma_f32_16x16x32_bf16 v[80:83], v[166:169], a[212:215], v[80:83]\n"
    "v_cvt_pk_bf16_f32 v51, v90, v91\n"
    "v_add_f32 v145, v145, v69\n"
    "v_mfma_f32_16x16x32_bf16 v[84:87], v[166:169], a[236:239], v[132:135]\n"
    "v_exp_f32 v70, v70\n"
    "v_mfma_f32_16x16x32_bf16 v[84:87], v[170:173], a[232:235], v[84:87]\n"
    "v_add_f32 v145, v145, v70\n"
    "v_cvt_f32_i32 v216, v216\n"
    "v_mfma_f32_16x16x32_bf16 v[84:87], v[186:189], a[228:231], v[84:87]\n"
    "v_cvt_pk_bf16_f32 v58, v92, v93\n"
    "v_cvt_f32_i32 v217, v217\n"
    "v_mfma_f32_16x16x32_bf16 v[84:87], v[182:185], a[224:227], v[84:87]\n"
    "v_exp_f32 v71, v71\n"
    "v_mfma_f32_16x16x32_bf16 v[84:87], v[178:181], a[220:223], v[84:87]\n"
    "v_add_f32 v145, v145, v71\n"
    "v_cvt_f32_i32 v224, v224\n"
    "v_mfma_f32_16x16x32_bf16 v[84:87], v[174:177], a[216:219], v[84:87]\n"
    "v_cvt_pk_bf16_f32 v59, v94, v95\n"
    "v_cvt_f32_i32 v225, v225\n"
    "v_mfma_f32_16x16x32_bf16 v[88:91], v[174:177], a[240:243], v[136:139]\n"
    "v_exp_f32 v72, v72\n"
    "v_mfma_f32_16x16x32_bf16 v[88:91], v[178:181], a[244:247], v[88:91]\n"
    "v_add_f32 v146, v146, v72\n"
    "v_fma_f32 v216, v216, v208, -v212\n"
    "v_mfma_f32_16x16x32_bf16 v[88:91], v[182:185], a[248:251], v[88:91]\n"
    "v_cvt_pk_bf16_f32 v36, v96, v97\n"
    "v_fma_f32 v217, v217, v209, -v213\n"
    "v_mfma_f32_16x16x32_bf16 v[88:91], v[186:189], a[252:255], v[88:91]\n"
    "v_exp_f32 v73, v73\n"
    "v_mfma_f32_16x16x32_bf16 v[88:91], v[170:173], v[0:3], v[88:91]\n"
    "v_add_f32 v146, v146, v73\n"
    "v_fma_f32 v224, v224, v208, -v212\n"
    "v_mfma_f32_16x16x32_bf16 v[88:91], v[166:169], v[4:7], v[88:91]\n"
    "v_cvt_pk_bf16_f32 v37, v98, v99\n"
    "v_fma_f32 v225, v225, v209, -v213\n"
    "v_mfma_f32_16x16x32_bf16 v[92:95], v[166:169], v[28:31], v[140:143]\n"
    "v_exp_f32 v74, v74\n"
    "v_mfma_f32_16x16x32_bf16 v[92:95], v[170:173], v[24:27], v[92:95]\n"
    "ds_read_b128 v[166:169], v254 offset:16384\n"
    "ds_read_b128 v[170:173], v251 offset:16384\n"
    "v_mfma_f32_16x16x32_bf16 v[92:95], v[186:189], v[20:23], v[92:95]\n"
    "v_cvt_pk_bf16_f32 v44, v100, v101\n"
    "v_add_f32 v146, v146, v74\n"
    "v_mfma_f32_16x16x32_bf16 v[92:95], v[182:185], v[16:19], v[92:95]\n"
    "v_exp_f32 v75, v75\n"
    "v_mfma_f32_16x16x32_bf16 v[92:95], v[178:181], v[12:15], v[92:95]\n"
    "v_add_f32 v146, v146, v75\n"
    "v_cvt_pk_bf16_f32 v239, v216, v217\n"
    "v_mfma_f32_16x16x32_bf16 v[92:95], v[174:177], v[8:11], v[92:95]\n"
    "ds_read_b128 v[186:189], v254 offset:18432\n"
    "s_waitcnt lgkmcnt(3)\n"
    "v_mfma_f32_16x16x32_bf16 v[96:99], v[162:165], a[192:195], v[128:131]\n"
    "v_exp_f32 v76, v76\n"
    "v_mfma_f32_16x16x32_bf16 v[96:99], v[158:161], a[196:199], v[96:99]\n"
    "ds_read_b128 v[182:185], v251 offset:18432\n"
    "v_cvt_pk_bf16_f32 v45, v102, v103\n"
    "v_mfma_f32_16x16x32_bf16 v[96:99], v[154:157], a[200:203], v[96:99]\n"
    "ds_read_b128 v[178:181], v254 offset:20480\n"
    "ds_read_b128 v[174:177], v251 offset:20480\n"
    "v_mfma_f32_16x16x32_bf16 v[96:99], v[150:153], a[204:207], v[96:99]\n"
    "v_exp_f32 v77, v77\n"
    "s_waitcnt lgkmcnt(4)\n"
    "v_mfma_f32_16x16x32_bf16 v[96:99], v[166:169], a[208:211], v[96:99]\n"
    "v_cvt_pk_bf16_f32 v52, v104, v105\n"
    "v_add_f32 v147, v147, v76\n"
    "v_mfma_f32_16x16x32_bf16 v[96:99], v[170:173], a[212:215], v[96:99]\n"
    "v_cvt_pk_bf16_f32 v53, v106, v107\n"
    "v_add_f32 v147, v147, v77\n"
    "v_mfma_f32_16x16x32_bf16 v[100:103], v[170:173], a[236:239], v[132:135]\n"
    "v_exp_f32 v78, v78\n"
    "v_mfma_f32_16x16x32_bf16 v[100:103], v[166:169], a[232:235], v[100:103]\n"
    "v_add_f32 v147, v147, v78\n"
    "v_cvt_pk_bf16_f32 v243, v224, v225\n"
    "v_mfma_f32_16x16x32_bf16 v[100:103], v[150:153], a[228:231], v[100:103]\n"
    "v_cvt_pk_bf16_f32 v60, v108, v109\n"
    "v_mfma_f32_16x16x32_bf16 v[100:103], v[154:157], a[224:227], v[100:103]\n"
    "v_exp_f32 v79, v79\n"
    "v_mfma_f32_16x16x32_bf16 v[100:103], v[158:161], a[220:223], v[100:103]\n"
    "v_add_f32 v147, v147, v79\n"
    "v_cvt_f32_i32 v218, v218\n"
    "v_mfma_f32_16x16x32_bf16 v[100:103], v[162:165], a[216:219], v[100:103]\n"
    "v_cvt_pk_bf16_f32 v61, v110, v111\n"
    "v_cvt_f32_i32 v219, v219\n"
    "v_mfma_f32_16x16x32_bf16 v[104:107], v[162:165], a[240:243], v[136:139]\n"
    "v_exp_f32 v80, v80\n"
    "v_mfma_f32_16x16x32_bf16 v[104:107], v[158:161], a[244:247], v[104:107]\n"
    "v_add_f32 v144, v144, v80\n"
    "v_cvt_f32_i32 v226, v226\n"
    "v_mfma_f32_16x16x32_bf16 v[104:107], v[154:157], a[248:251], v[104:107]\n"
    "v_cvt_pk_bf16_f32 v38, v112, v113\n"
    "v_mfma_f32_16x16x32_bf16 v[104:107], v[150:153], a[252:255], v[104:107]\n"
    "v_exp_f32 v81, v81\n"
    "v_mfma_f32_16x16x32_bf16 v[104:107], v[166:169], v[0:3], v[104:107]\n"
    "v_add_f32 v144, v144, v81\n"
    "v_cvt_f32_i32 v227, v227\n"
    "v_mfma_f32_16x16x32_bf16 v[104:107], v[170:173], v[4:7], v[104:107]\n"
    "s_mov_b32 m0, s74\n"
    "v_cvt_pk_bf16_f32 v39, v114, v115\n"
    "v_mfma_f32_16x16x32_bf16 v[108:111], v[170:173], v[28:31], v[140:143]\n"
    "v_exp_f32 v82, v82\n"
    "v_mfma_f32_16x16x32_bf16 v[108:111], v[166:169], v[24:27], v[108:111]\n"
    "ds_read_b128 v[170:173], v254 offset:22528\n"
    "ds_read_b128 v[166:169], v251 offset:22528\n"
    "v_mfma_f32_16x16x32_bf16 v[108:111], v[150:153], v[20:23], v[108:111]\n"
    "s_add_u32 s74, s74, s73\n"
    "v_cvt_pk_bf16_f32 v46, v116, v117\n"
    "v_mfma_f32_16x16x32_bf16 v[108:111], v[154:157], v[16:19], v[108:111]\n"
    "v_exp_f32 v83, v83\n"
    "v_mfma_f32_16x16x32_bf16 v[108:111], v[158:161], v[12:15], v[108:111]\n"
    "s_cmp_ge_u32 s74, s75\n"
    "s_cselect_b32 s73, 0xfffe8000, s76\n"
    "v_mfma_f32_16x16x32_bf16 v[108:111], v[162:165], v[8:11], v[108:111]\n"
    "ds_read_b128 v[150:153], v148 offset:24576\n"
    "ds_read_b128 v[206:209], v249 offset:512\n"
    "s_waitcnt lgkmcnt(4)\n"
    "v_mfma_f32_16x16x32_bf16 v[112:115], v[186:189], a[192:195], v[128:131]\n"
    "v_exp_f32 v84, v84\n"
    "v_mfma_f32_16x16x32_bf16 v[112:115], v[182:185], a[196:199], v[112:115]\n"
    "ds_read_b128 v[154:157], v148 offset:36864\n"
    "ds_read_b128 v[210:213], v249 offset:1536\n"
    "v_mfma_f32_16x16x32_bf16 v[112:115], v[178:181], a[200:203], v[112:115]\n"
    "buffer_load_dwordx4 v149, s[36:39], s72 offen offset:0 sc1 lds\n"
    "v_mfma_f32_16x16x32_bf16 v[112:115], v[174:177], a[204:207], v[112:115]\n"
    "v_exp_f32 v85, v85\n"
    "s_waitcnt lgkmcnt(4)\n"
    "v_mfma_f32_16x16x32_bf16 v[112:115], v[170:173], a[208:211], v[112:115]\n"
    "v_cvt_pk_bf16_f32 v47, v118, v119\n"
    "v_cvt_pk_bf16_f32 v54, v120, v121\n"
    "v_mfma_f32_16x16x32_bf16 v[112:115], v[166:169], a[212:215], v[112:115]\n"
    "v_cvt_pk_bf16_f32 v55, v122, v123\n"
    "v_add_f32 v144, v144, v82\n"
    "v_mfma_f32_16x16x32_bf16 v[116:119], v[166:169], a[236:239], v[132:135]\n"
    "v_exp_f32 v86, v86\n"
    "v_mfma_f32_16x16x32_bf16 v[116:119], v[170:173], a[232:235], v[116:119]\n"
    "buffer_load_dwordx4 v149, s[36:39], s72 offen offset:1024 sc1 lds\n"
    "v_mfma_f32_16x16x32_bf16 v[116:119], v[174:177], a[228:231], v[116:119]\n"
    "ds_read_b128 v[158:161], v148 offset:25600\n"
    "ds_read_b128 v[162:165], v148 offset:37888\n"
    "v_mfma_f32_16x16x32_bf16 v[116:119], v[178:181], a[224:227], v[116:119]\n"
    "v_exp_f32 v87, v87\n"
    "v_mfma_f32_16x16x32_bf16 v[116:119], v[182:185], a[220:223], v[116:119]\n"
    "v_cvt_pk_bf16_f32 v62, v124, v125\n"
    "v_add_f32 v144, v144, v83\n"
    "v_mfma_f32_16x16x32_bf16 v[116:119], v[186:189], a[216:219], v[116:119]\n"
    "v_cvt_pk_bf16_f32 v63, v126, v127\n"
    "v_add_f32 v145, v145, v84\n"
    "v_mfma_f32_16x16x32_bf16 v[120:123], v[186:189], a[240:243], v[136:139]\n"
    "v_exp_f32 v88, v88\n"
    "v_mfma_f32_16x16x32_bf16 v[120:123], v[182:185], a[244:247], v[120:123]\n"
    "buffer_load_dwordx4 v149, s[36:39], s72 offen offset:2048 sc1 lds\n"
    "v_mfma_f32_16x16x32_bf16 v[120:123], v[178:181], a[248:251], v[120:123]\n"
    "v_add_f32 v145, v145, v85\n"
    "v_add_f32 v145, v145, v86\n"
    "v_mfma_f32_16x16x32_bf16 v[120:123], v[174:177], a[252:255], v[120:123]\n"
    "v_exp_f32 v89, v89\n"
    "v_mfma_f32_16x16x32_bf16 v[120:123], v[170:173], v[0:3], v[120:123]\n"
    "v_add_f32 v145, v145, v87\n"
    "v_add_f32 v146, v146, v88\n"
    "v_mfma_f32_16x16x32_bf16 v[120:123], v[166:169], v[4:7], v[120:123]\n"
    "v_add_f32 v146, v146, v89\n"
    "v_fma_f32 v218, v218, v230, -v234\n"
    "v_mfma_f32_16x16x32_bf16 v[124:127], v[166:169], v[28:31], v[140:143]\n"
    "v_exp_f32 v90, v90\n"
    "v_mfma_f32_16x16x32_bf16 v[124:127], v[170:173], v[24:27], v[124:127]\n"
    "ds_read_b128 v[166:169], v148 offset:26624\n"
    "ds_read_b128 v[170:173], v148 offset:38912\n"
    "v_mfma_f32_16x16x32_bf16 v[124:127], v[174:177], v[20:23], v[124:127]\n"
    "buffer_load_dwordx4 v149, s[36:39], s72 offen offset:3072 sc1 lds\n"
    "v_mfma_f32_16x16x32_bf16 v[124:127], v[178:181], v[16:19], v[124:127]\n"
    "v_exp_f32 v91, v91\n"
    "v_mfma_f32_16x16x32_bf16 v[124:127], v[182:185], v[12:15], v[124:127]\n"
    "s_add_u32 s72, s72, 4096\n"
    "v_add_f32 v146, v146, v90\n"
    "v_mfma_f32_16x16x32_bf16 v[124:127], v[186:189], v[8:11], v[124:127]\n"
    "ds_read_b128 v[174:177], v148 offset:27648\n"
    "s_waitcnt lgkmcnt(3)\n"
    "v_mfma_f32_16x16x32_bf16 a[0:3], v[32:35], v[150:153], a[0:3]\n"
    "v_exp_f32 v92, v92\n"
    "v_mfma_f32_16x16x32_bf16 a[0:3], v[36:39], v[154:157], a[0:3]\n"
    "ds_read_b128 v[178:181], v148 offset:39936\n"
    "s_add_u32 m0, m0, 4096\n"
    "v_mfma_f32_16x16x32_bf16 a[4:7], v[44:47], v[154:157], a[4:7]\n"
    "v_add_f32 v146, v146, v91\n"
    "v_add_f32 v147, v147, v92\n"
    "v_mfma_f32_16x16x32_bf16 a[4:7], v[40:43], v[150:153], a[4:7]\n"
    "v_exp_f32 v93, v93\n"
    "v_mfma_f32_16x16x32_bf16 a[8:11], v[48:51], v[150:153], a[8:11]\n"
    "v_add_f32 v147, v147, v93\n"
    "v_mfma_f32_16x16x32_bf16 a[8:11], v[52:55], v[154:157], a[8:11]\n"
    "v_fma_f32 v219, v219, v231, -v235\n"
    "v_fma_f32 v226, v226, v230, -v234\n"
    "v_mfma_f32_16x16x32_bf16 a[12:15], v[60:63], v[154:157], a[12:15]\n"
    "v_exp_f32 v94, v94\n"
    "v_mfma_f32_16x16x32_bf16 a[12:15], v[56:59], v[150:153], a[12:15]\n"
    "buffer_load_dwordx4 v149, s[36:39], s72 offen offset:0 sc1 lds\n"
    "v_mfma_f32_16x16x32_bf16 a[16:19], v[32:35], v[158:161], a[16:19]\n"
    "ds_read_b128 v[182:185], v148 offset:28672\n"
    "ds_read_b128 v[186:189], v148 offset:40960\n"
    "v_mfma_f32_16x16x32_bf16 a[16:19], v[36:39], v[162:165], a[16:19]\n"
    "v_exp_f32 v95, v95\n"
    "v_mfma_f32_16x16x32_bf16 a[20:23], v[44:47], v[162:165], a[20:23]\n"
    "v_add_f32 v147, v147, v94\n"
    "v_add_f32 v147, v147, v95\n"
    "v_mfma_f32_16x16x32_bf16 a[20:23], v[40:43], v[158:161], a[20:23]\n"
    "v_fma_f32 v227, v227, v231, -v235\n"
    "v_mfma_f32_16x16x32_bf16 a[24:27], v[48:51], v[158:161], a[24:27]\n"
    "v_exp_f32 v96, v96\n"
    "v_mfma_f32_16x16x32_bf16 a[24:27], v[52:55], v[162:165], a[24:27]\n"
    "buffer_load_dwordx4 v149, s[36:39], s72 offen offset:1024 sc1 lds\n"
    "v_mfma_f32_16x16x32_bf16 a[28:31], v[60:63], v[162:165], a[28:31]\n"
    "v_add_f32 v144, v144, v96\n"
    "v_cvt_pk_bf16_f32 v240, v218, v219\n"
    "v_mfma_f32_16x16x32_bf16 a[28:31], v[56:59], v[158:161], a[28:31]\n"
    "v_exp_f32 v97, v97\n"
    "s_waitcnt lgkmcnt(0)\n"
    "v_mfma_f32_16x16x32_bf16 a[32:35], v[32:35], v[166:169], a[32:35]\n"
    "ds_read_b128 v[150:153], v148 offset:29696\n"
    "v_add_f32 v144, v144, v97\n"
    "v_mfma_f32_16x16x32_bf16 a[32:35], v[36:39], v[170:173], a[32:35]\n"
    "ds_read_b128 v[154:157], v148 offset:41984\n"
    "v_cvt_pk_bf16_f32 v244, v226, v227\n"
    "v_mfma_f32_16x16x32_bf16 a[36:39], v[44:47], v[170:173], a[36:39]\n"
    "v_exp_f32 v98, v98\n"
    "v_mfma_f32_16x16x32_bf16 a[36:39], v[40:43], v[166:169], a[36:39]\n"
    "buffer_load_dwordx4 v149, s[36:39], s72 offen offset:2048 sc1 lds\n"
    "v_mfma_f32_16x16x32_bf16 a[40:43], v[48:51], v[166:169], a[40:43]\n"
    "v_add_f32 v144, v144, v98\n"
    "v_mfma_f32_16x16x32_bf16 a[40:43], v[52:55], v[170:173], a[40:43]\n"
    "v_exp_f32 v99, v99\n"
    "v_mfma_f32_16x16x32_bf16 a[44:47], v[60:63], v[170:173], a[44:47]\n"
    "v_add_f32 v144, v144, v99\n"
    "v_cvt_f32_i32 v220, v220\n"
    "v_mfma_f32_16x16x32_bf16 a[44:47], v[56:59], v[166:169], a[44:47]\n"
    "ds_read_b128 v[158:161], v148 offset:30720\n"
    "v_cvt_f32_i32 v221, v221\n"
    "v_mfma_f32_16x16x32_bf16 a[48:51], v[32:35], v[174:177], a[48:51]\n"
    "v_exp_f32 v100, v100\n"
    "v_mfma_f32_16x16x32_bf16 a[48:51], v[36:39], v[178:181], a[48:51]\n"
    "ds_read_b128 v[162:165], v148 offset:43008\n"
    "v_add_f32 v145, v145, v100\n"
    "v_mfma_f32_16x16x32_bf16 a[52:55], v[44:47], v[178:181], a[52:55]\n"
    "buffer_load_dwordx4 v149, s[36:39], s72 offen offset:3072 sc1 lds\n"
    "v_mfma_f32_16x16x32_bf16 a[52:55], v[40:43], v[174:177], a[52:55]\n"
    "v_exp_f32 v101, v101\n"
    "v_mfma_f32_16x16x32_bf16 a[56:59], v[48:51], v[174:177], a[56:59]\n"
    "s_add_u32 s72, s72, 4096\n"
    "v_add_f32 v145, v145, v101\n"
    "v_mfma_f32_16x16x32_bf16 a[56:59], v[52:55], v[178:181], a[56:59]\n"
    "s_add_u32 m0, m0, 4096\n"
    "v_cvt_f32_i32 v228, v228\n"
    "v_mfma_f32_16x16x32_bf16 a[60:63], v[60:63], v[178:181], a[60:63]\n"
    "v_exp_f32 v102, v102\n"
    "v_mfma_f32_16x16x32_bf16 a[60:63], v[56:59], v[174:177], a[60:63]\n"
    "v_add_f32 v145, v145, v102\n"
    "v_mfma_f32_16x16x32_bf16 a[64:67], v[32:35], v[182:185], a[64:67]\n"
    "ds_read_b128 v[166:169], v148 offset:31744\n"
    "ds_read_b128 v[170:173], v148 offset:44032\n"
    "v_mfma_f32_16x16x32_bf16 a[64:67], v[36:39], v[186:189], a[64:67]\n"
    "v_exp_f32 v103, v103\n"
    "v_mfma_f32_16x16x32_bf16 a[68:71], v[44:47], v[186:189], a[68:71]\n"
    "v_add_f32 v145, v145, v103\n"
    "v_cvt_f32_i32 v229, v229\n"
    "v_mfma_f32_16x16x32_bf16 a[68:71], v[40:43], v[182:185], a[68:71]\n"
    "buffer_load_dwordx4 v149, s[36:39], s72 offen offset:0 sc1 lds\n"
    "v_mfma_f32_16x16x32_bf16 a[72:75], v[48:51], v[182:185], a[72:75]\n"
    "v_exp_f32 v104, v104\n"
    "v_mfma_f32_16x16x32_bf16 a[72:75], v[52:55], v[186:189], a[72:75]\n"
    "v_add_f32 v146, v146, v104\n"
    "v_fma_f32 v220, v220, v232, -v236\n"
    "v_mfma_f32_16x16x32_bf16 a[76:79], v[60:63], v[186:189], a[76:79]\n"
    "v_fma_f32 v221, v221, v233, -v237\n"
    "v_mfma_f32_16x16x32_bf16 a[76:79], v[56:59], v[182:185], a[76:79]\n"
    "v_exp_f32 v105, v105\n"
    "s_waitcnt lgkmcnt(0)\n"
    "v_mfma_f32_16x16x32_bf16 a[80:83], v[32:35], v[150:153], a[80:83]\n"
    "v_add_f32 v146, v146, v105\n"
    "v_fma_f32 v228, v228, v232, -v236\n"
    "v_mfma_f32_16x16x32_bf16 a[80:83], v[36:39], v[154:157], a[80:83]\n"
    "buffer_load_dwordx4 v149, s[36:39], s72 offen offset:1024 sc1 lds\n"
    "v_mfma_f32_16x16x32_bf16 a[84:87], v[44:47], v[154:157], a[84:87]\n"
    "v_exp_f32 v106, v106\n"
    "v_mfma_f32_16x16x32_bf16 a[84:87], v[40:43], v[150:153], a[84:87]\n"
    "v_add_f32 v146, v146, v106\n"
    "v_fma_f32 v229, v229, v233, -v237\n"
    "v_mfma_f32_16x16x32_bf16 a[88:91], v[48:51], v[150:153], a[88:91]\n"
    "v_cvt_pk_bf16_f32 v241, v220, v221\n"
    "v_mfma_f32_16x16x32_bf16 a[88:91], v[52:55], v[154:157], a[88:91]\n"
    "v_exp_f32 v107, v107\n"
    "v_mfma_f32_16x16x32_bf16 a[92:95], v[60:63], v[154:157], a[92:95]\n"
    "buffer_load_dwordx4 v149, s[36:39], s72 offen offset:2048 sc1 lds\n"
    "v_mfma_f32_16x16x32_bf16 a[92:95], v[56:59], v[150:153], a[92:95]\n"
    "ds_read_b128 v[150:153], v148 offset:32768\n"
    "ds_read_b128 v[154:157], v148 offset:45056\n"
    "v_mfma_f32_16x16x32_bf16 a[96:99], v[32:35], v[158:161], a[96:99]\n"
    "v_exp_f32 v108, v108\n"
    "v_mfma_f32_16x16x32_bf16 a[96:99], v[36:39], v[162:165], a[96:99]\n"
    "v_add_f32 v146, v146, v107\n"
    "v_add_f32 v147, v147, v108\n"
    "v_mfma_f32_16x16x32_bf16 a[100:103], v[44:47], v[162:165], a[100:103]\n"
    "v_cvt_pk_bf16_f32 v245, v228, v229\n"
    "v_mfma_f32_16x16x32_bf16 a[100:103], v[40:43], v[158:161], a[100:103]\n"
    "v_exp_f32 v109, v109\n"
    "v_mfma_f32_16x16x32_bf16 a[104:107], v[48:51], v[158:161], a[104:107]\n"
    "buffer_load_dwordx4 v149, s[36:39], s72 offen offset:3072 sc1 lds\n"
    "v_mfma_f32_16x16x32_bf16 a[104:107], v[52:55], v[162:165], a[104:107]\n"
    "s_add_u32 s72, s72, 16384\n"
    "v_add_f32 v147, v147, v109\n"
    "v_mfma_f32_16x16x32_bf16 a[108:111], v[60:63], v[162:165], a[108:111]\n"
    "v_exp_f32 v110, v110\n"
    "v_mfma_f32_16x16x32_bf16 a[108:111], v[56:59], v[158:161], a[108:111]\n"
    "ds_read_b128 v[158:161], v148 offset:33792\n"
    "ds_read_b128 v[162:165], v148 offset:46080\n"
    "v_mfma_f32_16x16x32_bf16 a[112:115], v[32:35], v[166:169], a[112:115]\n"
    "ds_read_b128 v[174:177], v148 offset:34816\n"
    "ds_read_b128 v[178:181], v148 offset:47104\n"
    "v_mfma_f32_16x16x32_bf16 a[112:115], v[36:39], v[170:173], a[112:115]\n"
    "v_exp_f32 v111, v111\n"
    "v_mfma_f32_16x16x32_bf16 a[116:119], v[44:47], v[170:173], a[116:119]\n"
    "ds_read_b128 v[182:185], v148 offset:35840\n"
    "v_add_f32 v147, v147, v110\n"
    "v_mfma_f32_16x16x32_bf16 a[116:119], v[40:43], v[166:169], a[116:119]\n"
    "ds_read_b128 v[186:189], v148 offset:48128\n"
    "v_add_f32 v147, v147, v111\n"
    "v_mfma_f32_16x16x32_bf16 a[120:123], v[48:51], v[166:169], a[120:123]\n"
    "v_exp_f32 v112, v112\n"
    "v_mfma_f32_16x16x32_bf16 a[120:123], v[52:55], v[170:173], a[120:123]\n"
    "buffer_store_dwordx4 v[238:241], v149, s[52:55], s85 offen offset:2048 sc1\n"
    "v_mfma_f32_16x16x32_bf16 a[124:127], v[60:63], v[170:173], a[124:127]\n"
    "buffer_store_dwordx4 v[242:245], v149, s[52:55], s85 offen offset:3072 sc1\n"
    "v_mfma_f32_16x16x32_bf16 a[124:127], v[56:59], v[166:169], a[124:127]\n"
    "v_exp_f32 v113, v113\n"
    "s_waitcnt lgkmcnt(2)\n"
    "v_mfma_f32_16x16x32_bf16 a[128:131], v[32:35], v[150:153], a[128:131]\n"
    "v_add_f32 v144, v144, v112\n"
    "v_add_f32 v144, v144, v113\n"
    "v_mfma_f32_16x16x32_bf16 a[128:131], v[36:39], v[154:157], a[128:131]\n"
    "v_mfma_f32_16x16x32_bf16 a[132:135], v[44:47], v[154:157], a[132:135]\n"
    "v_exp_f32 v114, v114\n"
    "v_mfma_f32_16x16x32_bf16 a[132:135], v[40:43], v[150:153], a[132:135]\n"
    "s_waitcnt vmcnt(14) lgkmcnt(0)\n"
    "s_barrier\n"
    "v_mfma_f32_16x16x32_bf16 a[136:139], v[48:51], v[150:153], a[136:139]\n"
    "v_add_u32 v254, s73, v254\n"
    "v_add_u32 v251, s73, v251\n"
    "v_mfma_f32_16x16x32_bf16 a[136:139], v[52:55], v[154:157], a[136:139]\n"
    "v_exp_f32 v115, v115\n"
    "v_mfma_f32_16x16x32_bf16 a[140:143], v[60:63], v[154:157], a[140:143]\n"
    "v_add_f32 v144, v144, v114\n"
    "v_add_f32 v144, v144, v115\n"
    "v_mfma_f32_16x16x32_bf16 a[140:143], v[56:59], v[150:153], a[140:143]\n"
    "ds_read_b128 v[150:153], v254 offset:0\n"
    "v_cvt_f32_i32 v190, v190\n"
    "v_mfma_f32_16x16x32_bf16 a[144:147], v[32:35], v[158:161], a[144:147]\n"
    "v_exp_f32 v116, v116\n"
    "v_mfma_f32_16x16x32_bf16 a[144:147], v[36:39], v[162:165], a[144:147]\n"
    "ds_read_b128 v[154:157], v251 offset:0\n"
    "v_add_f32 v145, v145, v116\n"
    "v_mfma_f32_16x16x32_bf16 a[148:151], v[44:47], v[162:165], a[148:151]\n"
    "v_cvt_f32_i32 v191, v191\n"
    "v_mfma_f32_16x16x32_bf16 a[148:151], v[40:43], v[158:161], a[148:151]\n"
    "v_exp_f32 v117, v117\n"
    "v_mfma_f32_16x16x32_bf16 a[152:155], v[48:51], v[158:161], a[152:155]\n"
    "v_add_f32 v145, v145, v117\n"
    "v_cvt_f32_i32 v198, v198\n"
    "v_mfma_f32_16x16x32_bf16 a[152:155], v[52:55], v[162:165], a[152:155]\n"
    "v_cvt_f32_i32 v199, v199\n"
    "v_mfma_f32_16x16x32_bf16 a[156:159], v[60:63], v[162:165], a[156:159]\n"
    "v_exp_f32 v118, v118\n"
    "v_mfma_f32_16x16x32_bf16 a[156:159], v[56:59], v[158:161], a[156:159]\n"
    "ds_read_b128 v[158:161], v254 offset:2048\n"
    "ds_read_b128 v[162:165], v251 offset:2048\n"
    "v_mfma_f32_16x16x32_bf16 a[160:163], v[32:35], v[174:177], a[160:163]\n"
    "v_add_u32 v148, s73, v148\n"
    "ds_read_b128 v[166:169], v254 offset:4096\n"
    "v_mfma_f32_16x16x32_bf16 a[160:163], v[36:39], v[178:181], a[160:163]\n"
    "v_exp_f32 v119, v119\n"
    "v_mfma_f32_16x16x32_bf16 a[164:167], v[44:47], v[178:181], a[164:167]\n"
    "ds_read_b128 v[170:173], v251 offset:4096\n"
    "v_add_f32 v145, v145, v118\n"
    "v_mfma_f32_16x16x32_bf16 a[164:167], v[40:43], v[174:177], a[164:167]\n"
    "v_add_f32 v145, v145, v119\n"
    "v_fma_f32 v190, v190, v206, -v210\n"
    "v_mfma_f32_16x16x32_bf16 a[168:171], v[48:51], v[174:177], a[168:171]\n"
    "v_exp_f32 v120, v120\n"
    "v_mfma_f32_16x16x32_bf16 a[168:171], v[52:55], v[178:181], a[168:171]\n"
    "v_add_f32 v146, v146, v120\n"
    "v_mfma_f32_16x16x32_bf16 a[172:175], v[60:63], v[178:181], a[172:175]\n"
    "v_fma_f32 v191, v191, v207, -v211\n"
    "v_fma_f32 v198, v198, v206, -v210\n"
    "v_mfma_f32_16x16x32_bf16 a[172:175], v[56:59], v[174:177], a[172:175]\n"
    "v_exp_f32 v121, v121\n"
    "v_mfma_f32_16x16x32_bf16 a[176:179], v[32:35], v[182:185], a[176:179]\n"
    "ds_read_b128 v[230:233], v249 offset:640\n"
    "ds_read_b128 v[234:237], v249 offset:1664\n"
    "v_mfma_f32_16x16x32_bf16 a[176:179], v[36:39], v[186:189], a[176:179]\n"
    "v_cvt_pk_bf16_f32 v32, v64, v65\n"
    "v_cvt_pk_bf16_f32 v33, v66, v67\n"
    "v_mfma_f32_16x16x32_bf16 a[180:183], v[44:47], v[186:189], a[180:183]\n"
    "v_exp_f32 v122, v122\n"
    "v_mfma_f32_16x16x32_bf16 a[180:183], v[40:43], v[182:185], a[180:183]\n"
    "v_add_f32 v146, v146, v121\n"
    "v_add_f32 v146, v146, v122\n"
    "v_mfma_f32_16x16x32_bf16 a[184:187], v[48:51], v[182:185], a[184:187]\n"
    "v_cvt_pk_bf16_f32 v40, v68, v69\n"
    "v_mfma_f32_16x16x32_bf16 a[184:187], v[52:55], v[186:189], a[184:187]\n"
    "v_exp_f32 v123, v123\n"
    "v_mfma_f32_16x16x32_bf16 a[188:191], v[60:63], v[186:189], a[188:191]\n"
    "v_add_f32 v146, v146, v123\n"
    "v_fma_f32 v199, v199, v207, -v211\n"
    "v_mfma_f32_16x16x32_bf16 a[188:191], v[56:59], v[182:185], a[188:191]\n"
    "ds_read_b128 v[174:177], v254 offset:6144\n"
    "s_waitcnt lgkmcnt(1)\n"
    "v_mfma_f32_16x16x32_bf16 v[64:67], v[150:153], a[192:195], v[128:131]\n"
    "v_exp_f32 v124, v124\n"
    "v_mfma_f32_16x16x32_bf16 v[64:67], v[154:157], a[196:199], v[64:67]\n"
    "ds_read_b128 v[178:181], v251 offset:6144\n"
    "v_cvt_pk_bf16_f32 v41, v70, v71\n"
    "v_mfma_f32_16x16x32_bf16 v[64:67], v[158:161], a[200:203], v[64:67]\n"
    "ds_read_b128 v[182:185], v254 offset:8192\n"
    "ds_read_b128 v[186:189], v251 offset:8192\n"
    "v_mfma_f32_16x16x32_bf16 v[64:67], v[162:165], a[204:207], v[64:67]\n"
    "v_exp_f32 v125, v125\n"
    "v_mfma_f32_16x16x32_bf16 v[64:67], v[166:169], a[208:211], v[64:67]\n"
    "v_cvt_pk_bf16_f32 v48, v72, v73\n"
    "v_add_f32 v147, v147, v124\n"
    "v_mfma_f32_16x16x32_bf16 v[64:67], v[170:173], a[212:215], v[64:67]\n"
    "v_cvt_pk_bf16_f32 v49, v74, v75\n"
    "v_add_f32 v147, v147, v125\n"
    "v_mfma_f32_16x16x32_bf16 v[68:71], v[170:173], a[236:239], v[132:135]\n"
    "v_exp_f32 v126, v126\n"
    "v_mfma_f32_16x16x32_bf16 v[68:71], v[166:169], a[232:235], v[68:71]\n"
    "v_add_f32 v147, v147, v126\n"
    "v_cvt_pk_bf16_f32 v238, v190, v191\n"
    "v_mfma_f32_16x16x32_bf16 v[68:71], v[162:165], a[228:231], v[68:71]\n"
    "v_cvt_pk_bf16_f32 v56, v76, v77\n"
    "v_mfma_f32_16x16x32_bf16 v[68:71], v[158:161], a[224:227], v[68:71]\n"
    "v_exp_f32 v127, v127\n"
    "v_mfma_f32_16x16x32_bf16 v[68:71], v[154:157], a[220:223], v[68:71]\n"
    "v_add_f32 v147, v147, v127\n"
    "v_cvt_pk_bf16_f32 v242, v198, v199\n"
    "v_mfma_f32_16x16x32_bf16 v[68:71], v[150:153], a[216:219], v[68:71]\n"
    "v_cvt_pk_bf16_f32 v57, v78, v79\n"
    "v_mfma_f32_16x16x32_bf16 v[72:75], v[150:153], a[240:243], v[136:139]\n"
    "v_exp_f32 v64, v64\n"
    "v_mfma_f32_16x16x32_bf16 v[72:75], v[154:157], a[244:247], v[72:75]\n"
    "v_add_f32 v144, v144, v64\n"
    "v_cvt_f32_i32 v192, v192\n"
    "v_mfma_f32_16x16x32_bf16 v[72:75], v[158:161], a[248:251], v[72:75]\n"
    "v_cvt_pk_bf16_f32 v34, v80, v81\n"
    "v_cvt_f32_i32 v193, v193\n"
    "v_mfma_f32_16x16x32_bf16 v[72:75], v[162:165], a[252:255], v[72:75]\n"
    "v_exp_f32 v65, v65\n"
    "v_mfma_f32_16x16x32_bf16 v[72:75], v[166:169], v[0:3], v[72:75]\n"
    "v_add_f32 v144, v144, v65\n"
    "v_mfma_f32_16x16x32_bf16 v[72:75], v[170:173], v[4:7], v[72:75]\n"
    "v_cvt_pk_bf16_f32 v35, v82, v83\n"
    "v_cvt_f32_i32 v200, v200\n"
    "v_mfma_f32_16x16x32_bf16 v[76:79], v[170:173], v[28:31], v[140:143]\n"
    "v_exp_f32 v66, v66\n"
    "v_mfma_f32_16x16x32_bf16 v[76:79], v[166:169], v[24:27], v[76:79]\n"
    "ds_read_b128 v[170:173], v254 offset:10240\n"
    "ds_read_b128 v[166:169], v251 offset:10240\n"
    "v_mfma_f32_16x16x32_bf16 v[76:79], v[162:165], v[20:23], v[76:79]\n"
    "v_cvt_pk_bf16_f32 v42, v84, v85\n"
    "v_add_f32 v144, v144, v66\n"
    "v_mfma_f32_16x16x32_bf16 v[76:79], v[158:161], v[16:19], v[76:79]\n"
    "v_exp_f32 v67, v67\n"
    "v_mfma_f32_16x16x32_bf16 v[76:79], v[154:157], v[12:15], v[76:79]\n"
    "v_add_f32 v144, v144, v67\n"
    "v_cvt_f32_i32 v201, v201\n"
    "v_mfma_f32_16x16x32_bf16 v[76:79], v[150:153], v[8:11], v[76:79]\n"
    "ds_read_b128 v[162:165], v254 offset:12288\n"
    "s_waitcnt lgkmcnt(3)\n"
    "v_mfma_f32_16x16x32_bf16 v[80:83], v[174:177], a[192:195], v[128:131]\n"
    "v_exp_f32 v68, v68\n"
    "v_mfma_f32_16x16x32_bf16 v[80:83], v[178:181], a[196:199], v[80:83]\n"
    "ds_read_b128 v[158:161], v251 offset:12288\n"
    "v_cvt_pk_bf16_f32 v43, v86, v87\n"
    "v_mfma_f32_16x16x32_bf16 v[80:83], v[182:185], a[200:203], v[80:83]\n"
    "ds_read_b128 v[154:157], v254 offset:14336\n"
    "ds_read_b128 v[150:153], v251 offset:14336\n"
    "v_mfma_f32_16x16x32_bf16 v[80:83], v[186:189], a[204:207], v[80:83]\n"
    "v_exp_f32 v69, v69\n"
    "s_waitcnt lgkmcnt(4)\n"
    "v_mfma_f32_16x16x32_bf16 v[80:83], v[170:173], a[208:211], v[80:83]\n"
    "v_cvt_pk_bf16_f32 v50, v88, v89\n"
    "v_add_f32 v145, v145, v68\n"
    "v_mfma_f32_16x16x32_bf16 v[80:83], v[166:169], a[212:215], v[80:83]\n"
    "v_cvt_pk_bf16_f32 v51, v90, v91\n"
    "v_add_f32 v145, v145, v69\n"
    "v_mfma_f32_16x16x32_bf16 v[84:87], v[166:169], a[236:239], v[132:135]\n"
    "v_exp_f32 v70, v70\n"
    "v_mfma_f32_16x16x32_bf16 v[84:87], v[170:173], a[232:235], v[84:87]\n"
    "v_add_f32 v145, v145, v70\n"
    "v_mfma_f32_16x16x32_bf16 v[84:87], v[186:189], a[228:231], v[84:87]\n"
    "v_cvt_pk_bf16_f32 v58, v92, v93\n"
    "v_fma_f32 v192, v192, v208, -v212\n"
    "v_mfma_f32_16x16x32_bf16 v[84:87], v[182:185], a[224:227], v[84:87]\n"
    "v_exp_f32 v71, v71\n"
    "v_mfma_f32_16x16x32_bf16 v[84:87], v[178:181], a[220:223], v[84:87]\n"
    "v_add_f32 v145, v145, v71\n"
    "v_mfma_f32_16x16x32_bf16 v[84:87], v[174:177], a[216:219], v[84:87]\n"
    "v_cvt_pk_bf16_f32 v59, v94, v95\n"
    "v_fma_f32 v193, v193, v209, -v213\n"
    "v_mfma_f32_16x16x32_bf16 v[88:91], v[174:177], a[240:243], v[136:139]\n"
    "v_exp_f32 v72, v72\n"
    "v_mfma_f32_16x16x32_bf16 v[88:91], v[178:181], a[244:247], v[88:91]\n"
    "v_add_f32 v146, v146, v72\n"
    "v_fma_f32 v200, v200, v208, -v212\n"
    "v_mfma_f32_16x16x32_bf16 v[88:91], v[182:185], a[248:251], v[88:91]\n"
    "v_cvt_pk_bf16_f32 v36, v96, v97\n"
    "v_mfma_f32_16x16x32_bf16 v[88:91], v[186:189], a[252:255], v[88:91]\n"
    "v_exp_f32 v73, v73\n"
    "v_mfma_f32_16x16x32_bf16 v[88:91], v[170:173], v[0:3], v[88:91]\n"
    "v_add_f32 v146, v146, v73\n"
    "v_fma_f32 v201, v201, v209, -v213\n"
    "v_mfma_f32_16x16x32_bf16 v[88:91], v[166:169], v[4:7], v[88:91]\n"
    "v_cvt_pk_bf16_f32 v37, v98, v99\n"
    "v_cvt_pk_bf16_f32 v239, v192, v193\n"
    "v_mfma_f32_16x16x32_bf16 v[92:95], v[166:169], v[28:31], v[140:143]\n"
    "v_exp_f32 v74, v74\n"
    "v_mfma_f32_16x16x32_bf16 v[92:95], v[170:173], v[24:27], v[92:95]\n"
    "ds_read_b128 v[166:169], v254 offset:16384\n"
    "ds_read_b128 v[170:173], v251 offset:16384\n"
    "v_mfma_f32_16x16x32_bf16 v[92:95], v[186:189], v[20:23], v[92:95]\n"
    "v_cvt_pk_bf16_f32 v44, v100, v101\n"
    "v_add_f32 v146, v146, v74\n"
    "v_mfma_f32_16x16x32_bf16 v[92:95], v[182:185], v[16:19], v[92:95]\n"
    "v_exp_f32 v75, v75\n"
    "v_mfma_f32_16x16x32_bf16 v[92:95], v[178:181], v[12:15], v[92:95]\n"
    "v_add_f32 v146, v146, v75\n"
    "v_mfma_f32_16x16x32_bf16 v[92:95], v[174:177], v[8:11], v[92:95]\n"
    "ds_read_b128 v[186:189], v254 offset:18432\n"
    "s_waitcnt lgkmcnt(3)\n"
    "v_mfma_f32_16x16x32_bf16 v[96:99], v[162:165], a[192:195], v[128:131]\n"
    "v_exp_f32 v76, v76\n"
    "v_mfma_f32_16x16x32_bf16 v[96:99], v[158:161], a[196:199], v[96:99]\n"
    "ds_read_b128 v[182:185], v251 offset:18432\n"
    "v_cvt_pk_bf16_f32 v45, v102, v103\n"
    "v_mfma_f32_16x16x32_bf16 v[96:99], v[154:157], a[200:203], v[96:99]\n"
    "ds_read_b128 v[178:181], v254 offset:20480\n"
    "ds_read_b128 v[174:177], v251 offset:20480\n"
    "v_mfma_f32_16x16x32_bf16 v[96:99], v[150:153], a[204:207], v[96:99]\n"
    "v_exp_f32 v77, v77\n"
    "s_waitcnt lgkmcnt(4)\n"
    "v_mfma_f32_16x16x32_bf16 v[96:99], v[166:169], a[208:211], v[96:99]\n"
    "v_cvt_pk_bf16_f32 v52, v104, v105\n"
    "v_add_f32 v147, v147, v76\n"
    "v_mfma_f32_16x16x32_bf16 v[96:99], v[170:173], a[212:215], v[96:99]\n"
    "v_cvt_pk_bf16_f32 v53, v106, v107\n"
    "v_add_f32 v147, v147, v77\n"
    "v_mfma_f32_16x16x32_bf16 v[100:103], v[170:173], a[236:239], v[132:135]\n"
    "v_exp_f32 v78, v78\n"
    "v_mfma_f32_16x16x32_bf16 v[100:103], v[166:169], a[232:235], v[100:103]\n"
    "v_add_f32 v147, v147, v78\n"
    "v_cvt_pk_bf16_f32 v243, v200, v201\n"
    "v_mfma_f32_16x16x32_bf16 v[100:103], v[150:153], a[228:231], v[100:103]\n"
    "v_cvt_pk_bf16_f32 v60, v108, v109\n"
    "v_mfma_f32_16x16x32_bf16 v[100:103], v[154:157], a[224:227], v[100:103]\n"
    "v_exp_f32 v79, v79\n"
    "v_mfma_f32_16x16x32_bf16 v[100:103], v[158:161], a[220:223], v[100:103]\n"
    "v_add_f32 v147, v147, v79\n"
    "v_cvt_f32_i32 v194, v194\n"
    "v_mfma_f32_16x16x32_bf16 v[100:103], v[162:165], a[216:219], v[100:103]\n"
    "v_cvt_pk_bf16_f32 v61, v110, v111\n"
    "v_cvt_f32_i32 v195, v195\n"
    "v_mfma_f32_16x16x32_bf16 v[104:107], v[162:165], a[240:243], v[136:139]\n"
    "v_exp_f32 v80, v80\n"
    "v_mfma_f32_16x16x32_bf16 v[104:107], v[158:161], a[244:247], v[104:107]\n"
    "v_add_f32 v144, v144, v80\n"
    "v_cvt_f32_i32 v202, v202\n"
    "v_mfma_f32_16x16x32_bf16 v[104:107], v[154:157], a[248:251], v[104:107]\n"
    "v_cvt_pk_bf16_f32 v38, v112, v113\n"
    "v_mfma_f32_16x16x32_bf16 v[104:107], v[150:153], a[252:255], v[104:107]\n"
    "v_exp_f32 v81, v81\n"
    "v_mfma_f32_16x16x32_bf16 v[104:107], v[166:169], v[0:3], v[104:107]\n"
    "v_add_f32 v144, v144, v81\n"
    "v_cvt_f32_i32 v203, v203\n"
    "v_mfma_f32_16x16x32_bf16 v[104:107], v[170:173], v[4:7], v[104:107]\n"
    "s_mov_b32 m0, s74\n"
    "v_cvt_pk_bf16_f32 v39, v114, v115\n"
    "v_mfma_f32_16x16x32_bf16 v[108:111], v[170:173], v[28:31], v[140:143]\n"
    "v_exp_f32 v82, v82\n"
    "v_mfma_f32_16x16x32_bf16 v[108:111], v[166:169], v[24:27], v[108:111]\n"
    "ds_read_b128 v[170:173], v254 offset:22528\n"
    "ds_read_b128 v[166:169], v251 offset:22528\n"
    "v_mfma_f32_16x16x32_bf16 v[108:111], v[150:153], v[20:23], v[108:111]\n"
    "s_add_u32 s74, s74, s73\n"
    "v_cvt_pk_bf16_f32 v46, v116, v117\n"
    "v_mfma_f32_16x16x32_bf16 v[108:111], v[154:157], v[16:19], v[108:111]\n"
    "v_exp_f32 v83, v83\n"
    "v_mfma_f32_16x16x32_bf16 v[108:111], v[158:161], v[12:15], v[108:111]\n"
    "s_cmp_ge_u32 s74, s75\n"
    "s_cselect_b32 s73, 0xfffe8000, s76\n"
    "v_mfma_f32_16x16x32_bf16 v[108:111], v[162:165], v[8:11], v[108:111]\n"
    "ds_read_b128 v[150:153], v148 offset:24576\n"
    "s_waitcnt lgkmcnt(3)\n"
    "v_mfma_f32_16x16x32_bf16 v[112:115], v[186:189], a[192:195], v[128:131]\n"
    "v_exp_f32 v84, v84\n"
    "v_mfma_f32_16x16x32_bf16 v[112:115], v[182:185], a[196:199], v[112:115]\n"
    "ds_read_b128 v[154:157], v148 offset:36864\n"
    "v_cvt_pk_bf16_f32 v47, v118, v119\n"
    "v_mfma_f32_16x16x32_bf16 v[112:115], v[178:181], a[200:203], v[112:115]\n"
    "buffer_load_dwordx4 v149, s[36:39], s72 offen offset:0 sc1 lds\n"
    "v_mfma_f32_16x16x32_bf16 v[112:115], v[174:177], a[204:207], v[112:115]\n"
    "v_exp_f32 v85, v85\n"
    "s_waitcnt lgkmcnt(2)\n"
    "v_mfma_f32_16x16x32_bf16 v[112:115], v[170:173], a[208:211], v[112:115]\n"
    "v_cvt_pk_bf16_f32 v54, v120, v121\n"
    "v_add_f32 v144, v144, v82\n"
    "v_mfma_f32_16x16x32_bf16 v[112:115], v[166:169], a[212:215], v[112:115]\n"
    "v_cvt_pk_bf16_f32 v55, v122, v123\n"
    "v_add_f32 v144, v144, v83\n"
    "v_mfma_f32_16x16x32_bf16 v[116:119], v[166:169], a[236:239], v[132:135]\n"
    "v_exp_f32 v86, v86\n"
    "v_mfma_f32_16x16x32_bf16 v[116:119], v[170:173], a[232:235], v[116:119]\n"
    "buffer_load_dwordx4 v149, s[36:39], s72 offen offset:1024 sc1 lds\n"
    "v_mfma_f32_16x16x32_bf16 v[116:119], v[174:177], a[228:231], v[116:119]\n"
    "ds_read_b128 v[158:161], v148 offset:25600\n"
    "ds_read_b128 v[162:165], v148 offset:37888\n"
    "v_mfma_f32_16x16x32_bf16 v[116:119], v[178:181], a[224:227], v[116:119]\n"
    "v_exp_f32 v87, v87\n"
    "v_mfma_f32_16x16x32_bf16 v[116:119], v[182:185], a[220:223], v[116:119]\n"
    "v_cvt_pk_bf16_f32 v62, v124, v125\n"
    "v_add_f32 v145, v145, v84\n"
    "v_mfma_f32_16x16x32_bf16 v[116:119], v[186:189], a[216:219], v[116:119]\n"
    "v_cvt_pk_bf16_f32 v63, v126, v127\n"
    "v_add_f32 v145, v145, v85\n"
    "v_mfma_f32_16x16x32_bf16 v[120:123], v[186:189], a[240:243], v[136:139]\n"
    "v_exp_f32 v88, v88\n"
    "v_mfma_f32_16x16x32_bf16 v[120:123], v[182:185], a[244:247], v[120:123]\n"
    "buffer_load_dwordx4 v149, s[36:39], s72 offen offset:2048 sc1 lds\n"
    "v_mfma_f32_16x16x32_bf16 v[120:123], v[178:181], a[248:251], v[120:123]\n"
    "v_add_f32 v145, v145, v86\n"
    "v_add_f32 v145, v145, v87\n"
    "v_mfma_f32_16x16x32_bf16 v[120:123], v[174:177], a[252:255], v[120:123]\n"
    "v_exp_f32 v89, v89\n"
    "v_mfma_f32_16x16x32_bf16 v[120:123], v[170:173], v[0:3], v[120:123]\n"
    "v_add_f32 v146, v146, v88\n"
    "v_add_f32 v146, v146, v89\n"
    "v_mfma_f32_16x16x32_bf16 v[120:123], v[166:169], v[4:7], v[120:123]\n"
    "v_fma_f32 v194, v194, v230, -v234\n"
    "v_mfma_f32_16x16x32_bf16 v[124:127], v[166:169], v[28:31], v[140:143]\n"
    "v_exp_f32 v90, v90\n"
    "v_mfma_f32_16x16x32_bf16 v[124:127], v[170:173], v[24:27], v[124:127]\n"
    "ds_read_b128 v[166:169], v148 offset:26624\n"
    "ds_read_b128 v[170:173], v148 offset:38912\n"
    "v_mfma_f32_16x16x32_bf16 v[124:127], v[174:177], v[20:23], v[124:127]\n"
    "buffer_load_dwordx4 v149, s[36:39], s72 offen offset:3072 sc1 lds\n"
    "v_mfma_f32_16x16x32_bf16 v[124:127], v[178:181], v[16:19], v[124:127]\n"
    "v_exp_f32 v91, v91\n"
    "v_mfma_f32_16x16x32_bf16 v[124:127], v[182:185], v[12:15], v[124:127]\n"
    "s_add_u32 s72, s72, 4096\n"
    "v_add_f32 v146, v146, v90\n"
    "v_mfma_f32_16x16x32_bf16 v[124:127], v[186:189], v[8:11], v[124:127]\n"
    "ds_read_b128 v[174:177], v148 offset:27648\n"
    "s_waitcnt lgkmcnt(3)\n"
    "v_mfma_f32_16x16x32_bf16 a[0:3], v[32:35], v[150:153], a[0:3]\n"
    "v_exp_f32 v92, v92\n"
    "v_mfma_f32_16x16x32_bf16 a[0:3], v[36:39], v[154:157], a[0:3]\n"
    "ds_read_b128 v[178:181], v148 offset:39936\n"
    "s_add_u32 m0, m0, 4096\n"
    "v_mfma_f32_16x16x32_bf16 a[4:7], v[44:47], v[154:157], a[4:7]\n"
    "v_add_f32 v146, v146, v91\n"
    "v_add_f32 v147, v147, v92\n"
    "v_mfma_f32_16x16x32_bf16 a[4:7], v[40:43], v[150:153], a[4:7]\n"
    "v_exp_f32 v93, v93\n"
    "v_mfma_f32_16x16x32_bf16 a[8:11], v[48:51], v[150:153], a[8:11]\n"
    "v_add_f32 v147, v147, v93\n"
    "v_fma_f32 v195, v195, v231, -v235\n"
    "v_mfma_f32_16x16x32_bf16 a[8:11], v[52:55], v[154:157], a[8:11]\n"
    "v_fma_f32 v202, v202, v230, -v234\n"
    "v_mfma_f32_16x16x32_bf16 a[12:15], v[60:63], v[154:157], a[12:15]\n"
    "v_exp_f32 v94, v94\n"
    "v_mfma_f32_16x16x32_bf16 a[12:15], v[56:59], v[150:153], a[12:15]\n"
    "buffer_load_dwordx4 v149, s[36:39], s72 offen offset:0 sc1 lds\n"
    "v_mfma_f32_16x16x32_bf16 a[16:19], v[32:35], v[158:161], a[16:19]\n"
    "ds_read_b128 v[182:185], v148 offset:28672\n"
    "ds_read_b128 v[186:189], v148 offset:40960\n"
    "v_mfma_f32_16x16x32_bf16 a[16:19], v[36:39], v[162:165], a[16:19]\n"
    "v_exp_f32 v95, v95\n"
    "v_mfma_f32_16x16x32_bf16 a[20:23], v[44:47], v[162:165], a[20:23]\n"
    "v_add_f32 v147, v147, v94\n"
    "v_add_f32 v147, v147, v95\n"
    "v_mfma_f32_16x16x32_bf16 a[20:23], v[40:43], v[158:161], a[20:23]\n"
    "v_fma_f32 v203, v203, v231, -v235\n"
    "v_cvt_pk_bf16_f32 v240, v194, v195\n"
    "v_mfma_f32_16x16x32_bf16 a[24:27], v[48:51], v[158:161], a[24:27]\n"
    "v_exp_f32 v96, v96\n"
    "v_mfma_f32_16x16x32_bf16 a[24:27], v[52:55], v[162:165], a[24:27]\n"
    "buffer_load_dwordx4 v149, s[36:39], s72 offen offset:1024 sc1 lds\n"
    "v_mfma_f32_16x16x32_bf16 a[28:31], v[60:63], v[162:165], a[28:31]\n"
    "v_add_f32 v144, v144, v96\n"
    "v_mfma_f32_16x16x32_bf16 a[28:31], v[56:59], v[158:161], a[28:31]\n"
    "v_exp_f32 v97, v97\n"
    "s_waitcnt lgkmcnt(0)\n"
    "v_mfma_f32_16x16x32_bf16 a[32:35], v[32:35], v[166:169], a[32:35]\n"
    "ds_read_b128 v[150:153], v148 offset:29696\n"
    "v_add_f32 v144, v144, v97\n"
    "v_mfma_f32_16x16x32_bf16 a[32:35], v[36:39], v[170:173], a[32:35]\n"
    "ds_read_b128 v[154:157], v148 offset:41984\n"
    "v_cvt_pk_bf16_f32 v244, v202, v203\n"
    "v_mfma_f32_16x16x32_bf16 a[36:39], v[44:47], v[170:173], a[36:39]\n"
    "v_exp_f32 v98, v98\n"
    "v_mfma_f32_16x16x32_bf16 a[36:39], v[40:43], v[166:169], a[36:39]\n"
    "buffer_load_dwordx4 v149, s[36:39], s72 offen offset:2048 sc1 lds\n"
    "v_mfma_f32_16x16x32_bf16 a[40:43], v[48:51], v[166:169], a[40:43]\n"
    "v_add_f32 v144, v144, v98\n"
    "v_cvt_f32_i32 v196, v196\n"
    "v_mfma_f32_16x16x32_bf16 a[40:43], v[52:55], v[170:173], a[40:43]\n"
    "v_exp_f32 v99, v99\n"
    "v_mfma_f32_16x16x32_bf16 a[44:47], v[60:63], v[170:173], a[44:47]\n"
    "v_add_f32 v144, v144, v99\n"
    "v_cvt_f32_i32 v197, v197\n"
    "v_mfma_f32_16x16x32_bf16 a[44:47], v[56:59], v[166:169], a[44:47]\n"
    "ds_read_b128 v[158:161], v148 offset:30720\n"
    "v_mfma_f32_16x16x32_bf16 a[48:51], v[32:35], v[174:177], a[48:51]\n"
    "v_exp_f32 v100, v100\n"
    "v_mfma_f32_16x16x32_bf16 a[48:51], v[36:39], v[178:181], a[48:51]\n"
    "ds_read_b128 v[162:165], v148 offset:43008\n"
    "v_add_f32 v145, v145, v100\n"
    "v_mfma_f32_16x16x32_bf16 a[52:55], v[44:47], v[178:181], a[52:55]\n"
    "buffer_load_dwordx4 v149, s[36:39], s72 offen offset:3072 sc1 lds\n"
    "v_mfma_f32_16x16x32_bf16 a[52:55], v[40:43], v[174:177], a[52:55]\n"
    "v_exp_f32 v101, v101\n"
    "v_mfma_f32_16x16x32_bf16 a[56:59], v[48:51], v[174:177], a[56:59]\n"
    "s_add_u32 s72, s72, 4096\n"
    "v_add_f32 v145, v145, v101\n"
    "v_mfma_f32_16x16x32_bf16 a[56:59], v[52:55], v[178:181], a[56:59]\n"
    "s_add_u32 m0, m0, 4096\n"
    "v_cvt_f32_i32 v204, v204\n"
    "v_mfma_f32_16x16x32_bf16 a[60:63], v[60:63], v[178:181], a[60:63]\n"
    "v_exp_f32 v102, v102\n"
    "v_mfma_f32_16x16x32_bf16 a[60:63], v[56:59], v[174:177], a[60:63]\n"
    "v_add_f32 v145, v145, v102\n"
    "v_cvt_f32_i32 v205, v205\n"
    "v_mfma_f32_16x16x32_bf16 a[64:67], v[32:35], v[182:185], a[64:67]\n"
    "ds_read_b128 v[166:169], v148 offset:31744\n"
    "ds_read_b128 v[170:173], v148 offset:44032\n"
    "v_mfma_f32_16x16x32_bf16 a[64:67], v[36:39], v[186:189], a[64:67]\n"
    "v_exp_f32 v103, v103\n"
    "v_mfma_f32_16x16x32_bf16 a[68:71], v[44:47], v[186:189], a[68:71]\n"
    "v_add_f32 v145, v145, v103\n"
    "v_mfma_f32_16x16x32_bf16 a[68:71], v[40:43], v[182:185], a[68:71]\n"
    "buffer_load_dwordx4 v149, s[36:39], s72 offen offset:0 sc1 lds\n"
    "v_mfma_f32_16x16x32_bf16 a[72:75], v[48:51], v[182:185], a[72:75]\n"
    "v_exp_f32 v104, v104\n"
    "v_mfma_f32_16x16x32_bf16 a[72:75], v[52:55], v[186:189], a[72:75]\n"
    "v_add_f32 v146, v146, v104\n"
    "v_fma_f32 v196, v196, v232, -v236\n"
    "v_mfma_f32_16x16x32_bf16 a[76:79], v[60:63], v[186:189], a[76:79]\n"
    "v_fma_f32 v197, v197, v233, -v237\n"
    "v_mfma_f32_16x16x32_bf16 a[76:79], v[56:59], v[182:185], a[76:79]\n"
    "v_exp_f32 v105, v105\n"
    "s_waitcnt lgkmcnt(0)\n"
    "v_mfma_f32_16x16x32_bf16 a[80:83], v[32:35], v[150:153], a[80:83]\n"
    "v_add_f32 v146, v146, v105\n"
    "v_fma_f32 v204, v204, v232, -v236\n"
    "v_mfma_f32_16x16x32_bf16 a[80:83], v[36:39], v[154:157], a[80:83]\n"
    "buffer_load_dwordx4 v149, s[36:39], s72 offen offset:1024 sc1 lds\n"
    "v_mfma_f32_16x16x32_bf16 a[84:87], v[44:47], v[154:157], a[84:87]\n"
    "v_exp_f32 v106, v106\n"
    "v_mfma_f32_16x16x32_bf16 a[84:87], v[40:43], v[150:153], a[84:87]\n"
    "v_add_f32 v146, v146, v106\n"
    "v_fma_f32 v205, v205, v233, -v237\n"
    "v_mfma_f32_16x16x32_bf16 a[88:91], v[48:51], v[150:153], a[88:91]\n"
    "v_cvt_pk_bf16_f32 v241, v196, v197\n"
    "v_mfma_f32_16x16x32_bf16 a[88:91], v[52:55], v[154:157], a[88:91]\n"
    "v_exp_f32 v107, v107\n"
    "v_mfma_f32_16x16x32_bf16 a[92:95], v[60:63], v[154:157], a[92:95]\n"
    "buffer_load_dwordx4 v149, s[36:39], s72 offen offset:2048 sc1 lds\n"
    "v_mfma_f32_16x16x32_bf16 a[92:95], v[56:59], v[150:153], a[92:95]\n"
    "ds_read_b128 v[150:153], v148 offset:32768\n"
    "ds_read_b128 v[154:157], v148 offset:45056\n"
    "v_mfma_f32_16x16x32_bf16 a[96:99], v[32:35], v[158:161], a[96:99]\n"
    "v_exp_f32 v108, v108\n"
    "v_mfma_f32_16x16x32_bf16 a[96:99], v[36:39], v[162:165], a[96:99]\n"
    "v_add_f32 v146, v146, v107\n"
    "v_add_f32 v147, v147, v108\n"
    "v_mfma_f32_16x16x32_bf16 a[100:103], v[44:47], v[162:165], a[100:103]\n"
    "v_cvt_pk_bf16_f32 v245, v204, v205\n"
    "v_mfma_f32_16x16x32_bf16 a[100:103], v[40:43], v[158:161], a[100:103]\n"
    "v_exp_f32 v109, v109\n"
    "v_mfma_f32_16x16x32_bf16 a[104:107], v[48:51], v[158:161], a[104:107]\n"
    "buffer_load_dwordx4 v149, s[36:39], s72 offen offset:3072 sc1 lds\n"
    "v_mfma_f32_16x16x32_bf16 a[104:107], v[52:55], v[162:165], a[104:107]\n"
    "s_add_u32 s72, s72, 16384\n"
    "v_add_f32 v147, v147, v109\n"
    "v_mfma_f32_16x16x32_bf16 a[108:111], v[60:63], v[162:165], a[108:111]\n"
    "v_exp_f32 v110, v110\n"
    "v_mfma_f32_16x16x32_bf16 a[108:111], v[56:59], v[158:161], a[108:111]\n"
    "ds_read_b128 v[158:161], v148 offset:33792\n"
    "ds_read_b128 v[162:165], v148 offset:46080\n"
    "v_mfma_f32_16x16x32_bf16 a[112:115], v[32:35], v[166:169], a[112:115]\n"
    "ds_read_b128 v[174:177], v148 offset:34816\n"
    "ds_read_b128 v[178:181], v148 offset:47104\n"
    "v_mfma_f32_16x16x32_bf16 a[112:115], v[36:39], v[170:173], a[112:115]\n"
    "v_exp_f32 v111, v111\n"
    "v_mfma_f32_16x16x32_bf16 a[116:119], v[44:47], v[170:173], a[116:119]\n"
    "ds_read_b128 v[182:185], v148 offset:35840\n"
    "v_add_f32 v147, v147, v110\n"
    "v_mfma_f32_16x16x32_bf16 a[116:119], v[40:43], v[166:169], a[116:119]\n"
    "ds_read_b128 v[186:189], v148 offset:48128\n"
    "v_add_f32 v147, v147, v111\n"
    "v_mfma_f32_16x16x32_bf16 a[120:123], v[48:51], v[166:169], a[120:123]\n"
    "v_exp_f32 v112, v112\n"
    "v_mfma_f32_16x16x32_bf16 a[120:123], v[52:55], v[170:173], a[120:123]\n"
    "s_add_u32 s89, s85, 4096\n"
    "v_add_f32 v144, v144, v112\n"
    "v_mfma_f32_16x16x32_bf16 a[124:127], v[60:63], v[170:173], a[124:127]\n"
    "buffer_store_dwordx4 v[238:241], v149, s[52:55], s89 offen offset:0 sc1\n"
    "v_mfma_f32_16x16x32_bf16 a[124:127], v[56:59], v[166:169], a[124:127]\n"
    "v_exp_f32 v113, v113\n"
    "s_waitcnt lgkmcnt(2)\n"
    "v_mfma_f32_16x16x32_bf16 a[128:131], v[32:35], v[150:153], a[128:131]\n"
    "buffer_store_dwordx4 v[242:245], v149, s[52:55], s89 offen offset:1024 sc1\n"
    "v_mfma_f32_16x16x32_bf16 a[128:131], v[36:39], v[154:157], a[128:131]\n"
    "s_add_u32 s85, s85, 98304\n"
    "v_add_f32 v144, v144, v113\n"
    "v_mfma_f32_16x16x32_bf16 a[132:135], v[44:47], v[154:157], a[132:135]\n"
    "v_exp_f32 v114, v114\n"
    "v_mfma_f32_16x16x32_bf16 a[132:135], v[40:43], v[150:153], a[132:135]\n"
    "s_waitcnt vmcnt(14) lgkmcnt(0)\n"
    "s_barrier\n"
    "v_mfma_f32_16x16x32_bf16 a[136:139], v[48:51], v[150:153], a[136:139]\n"
    "v_add_u32 v254, s73, v254\n"
    "v_add_u32 v251, s73, v251\n"
    "v_mfma_f32_16x16x32_bf16 a[136:139], v[52:55], v[154:157], a[136:139]\n"
    "v_exp_f32 v115, v115\n"
    "v_mfma_f32_16x16x32_bf16 a[140:143], v[60:63], v[154:157], a[140:143]\n"
    "buffer_load_dwordx2 v[206:207], v246, s[44:47], s84 offen nt\n"
    "v_mfma_f32_16x16x32_bf16 a[140:143], v[56:59], v[150:153], a[140:143]\n"
    "ds_read_b128 v[150:153], v254 offset:0\n"
    "v_add_f32 v144, v144, v114\n"
    "v_mfma_f32_16x16x32_bf16 a[144:147], v[32:35], v[158:161], a[144:147]\n"
    "v_exp_f32 v116, v116\n"
    "v_mfma_f32_16x16x32_bf16 a[144:147], v[36:39], v[162:165], a[144:147]\n"
    "ds_read_b128 v[154:157], v251 offset:0\n"
    "v_add_f32 v144, v144, v115\n"
    "v_mfma_f32_16x16x32_bf16 a[148:151], v[44:47], v[162:165], a[148:151]\n"
    "buffer_load_dwordx2 v[208:209], v246, s[48:51], s84 offen nt\n"
    "v_mfma_f32_16x16x32_bf16 a[148:151], v[40:43], v[158:161], a[148:151]\n"
    "v_exp_f32 v117, v117\n"
    "v_mfma_f32_16x16x32_bf16 a[152:155], v[48:51], v[158:161], a[152:155]\n"
    "buffer_load_dwordx4 v[190:193], v248, s[40:43], s82 offen offset:0 nt\n"
    "v_mfma_f32_16x16x32_bf16 a[152:155], v[52:55], v[162:165], a[152:155]\n"
    "v_add_f32 v145, v145, v116\n"
    "v_add_f32 v145, v145, v117\n"
    "v_mfma_f32_16x16x32_bf16 a[156:159], v[60:63], v[162:165], a[156:159]\n"
    "v_exp_f32 v118, v118\n"
    "v_mfma_f32_16x16x32_bf16 a[156:159], v[56:59], v[158:161], a[156:159]\n"
    "ds_read_b128 v[158:161], v254 offset:2048\n"
    "ds_read_b128 v[162:165], v251 offset:2048\n"
    "v_mfma_f32_16x16x32_bf16 a[160:163], v[32:35], v[174:177], a[160:163]\n"
    "v_add_u32 v148, s73, v148\n"
    "ds_read_b128 v[166:169], v254 offset:4096\n"
    "v_mfma_f32_16x16x32_bf16 a[160:163], v[36:39], v[178:181], a[160:163]\n"
    "v_exp_f32 v119, v119\n"
    "v_mfma_f32_16x16x32_bf16 a[164:167], v[44:47], v[178:181], a[164:167]\n"
    "ds_read_b128 v[170:173], v251 offset:4096\n"
    "v_add_f32 v145, v145, v118\n"
    "v_mfma_f32_16x16x32_bf16 a[164:167], v[40:43], v[174:177], a[164:167]\n"
    "buffer_load_dwordx4 v[194:197], v248, s[40:43], s82 offen offset:128 nt\n"
    "v_mfma_f32_16x16x32_bf16 a[168:171], v[48:51], v[174:177], a[168:171]\n"
    "v_exp_f32 v120, v120\n"
    "v_mfma_f32_16x16x32_bf16 a[168:171], v[52:55], v[178:181], a[168:171]\n"
    "v_add_f32 v145, v145, v119\n"
    "v_add_f32 v146, v146, v120\n"
    "v_mfma_f32_16x16x32_bf16 a[172:175], v[60:63], v[178:181], a[172:175]\n"
    "v_mfma_f32_16x16x32_bf16 a[172:175], v[56:59], v[174:177], a[172:175]\n"
    "v_exp_f32 v121, v121\n"
    "v_mfma_f32_16x16x32_bf16 a[176:179], v[32:35], v[182:185], a[176:179]\n"
    "v_cvt_pk_bf16_f32 v32, v64, v65\n"
    "v_add_f32 v146, v146, v121\n"
    "v_mfma_f32_16x16x32_bf16 a[176:179], v[36:39], v[186:189], a[176:179]\n"
    "buffer_load_dwordx4 v[198:201], v248, s[40:43], s83 offen offset:0 nt\n"
    "v_mfma_f32_16x16x32_bf16 a[180:183], v[44:47], v[186:189], a[180:183]\n"
    "v_exp_f32 v122, v122\n"
    "v_mfma_f32_16x16x32_bf16 a[180:183], v[40:43], v[182:185], a[180:183]\n"
    "v_cvt_pk_bf16_f32 v33, v66, v67\n"
    "v_add_f32 v146, v146, v122\n"
    "v_mfma_f32_16x16x32_bf16 a[184:187], v[48:51], v[182:185], a[184:187]\n"
    "buffer_load_dwordx4 v[202:205], v248, s[40:43], s83 offen offset:128 nt\n"
    "v_mfma_f32_16x16x32_bf16 a[184:187], v[52:55], v[186:189], a[184:187]\n"
    "v_exp_f32 v123, v123\n"
    "v_mfma_f32_16x16x32_bf16 a[188:191], v[60:63], v[186:189], a[188:191]\n"
    "s_cmp_lt_u32 s72, 663552\n"
    "v_cvt_pk_bf16_f32 v40, v68, v69\n"
    "v_add_f32 v146, v146, v123\n"
    "v_mfma_f32_16x16x32_bf16 a[188:191], v[56:59], v[182:185], a[188:191]\n"
    "ds_read_b128 v[174:177], v254 offset:6144\n"
    "s_cbranch_scc1 SUPK%=\n"
    "s_branch PLAIN%=\n"
    "PROV%=:\n"
    "s_mul_i32 s91, s97, 384\n"
    "s_add_u32 s82, s82, s91\n"
    "s_add_u32 s83, s82, 3072\n"
    "s_lshl_b32 s84, s84, 6\n"
    "v_mov_b32 v248, v180\n"
    "v_mov_b32 v249, v181\n"
    "s_mov_b64 s[40:41], s[28:29]\n"
    "s_mov_b64 s[42:43], s[30:31]\n"
    "s_mov_b64 s[44:45], s[36:37]\n"
    "s_mov_b64 s[46:47], s[38:39]\n"
    "s_mov_b64 s[48:49], s[52:53]\n"
    "s_mov_b64 s[50:51], s[54:55]\n"
    "s_mov_b64 s[52:53], s[60:61]\n"
    "s_mov_b64 s[54:55], s[62:63]\n"
    "s_mov_b64 s[36:37], s[88:89]\n"
    "s_mov_b32 s38, s90\n"
    "s_mov_b32 s39, 0x20000\n"
    "buffer_load_dwordx4 v[206:209], v249, s[44:47], s84 offen nt\n"
    "buffer_load_dwordx4 v[210:213], v249, s[48:51], s84 offen nt\n"
    "buffer_load_dwordx2 v[190:191], v248, s[40:43], s82 offen offset:0 nt\n"
    "buffer_load_dwordx2 v[192:193], v248, s[40:43], s82 offen offset:768 nt\n"
    "buffer_load_dwordx2 v[194:195], v248, s[40:43], s82 offen offset:1536 nt\n"
    "buffer_load_dwordx2 v[196:197], v248, s[40:43], s82 offen offset:2304 nt\n"
    "buffer_load_dwordx2 v[198:199], v248, s[40:43], s83 offen offset:0 nt\n"
    "buffer_load_dwordx2 v[200:201], v248, s[40:43], s83 offen offset:768 nt\n"
    "buffer_load_dwordx2 v[202:203], v248, s[40:43], s83 offen offset:1536 nt\n"
    "buffer_load_dwordx2 v[204:205], v248, s[40:43], s83 offen offset:2304 nt\n"
    "s_waitcnt vmcnt(26)\n"
    "ds_read_b64 v[32:33], v162 offset:18432\n"
    "ds_read_b64 v[34:35], v163 offset:18432\n"
    "ds_read_b64 v[36:37], v164 offset:18432\n"
    "ds_read_b64 v[38:39], v165 offset:18432\n"
    "ds_read_b64 v[40:41], v162 offset:18560\n"
    "ds_read_b64 v[42:43], v163 offset:18560\n"
    "ds_read_b64 v[44:45], v164 offset:18560\n"
    "ds_read_b64 v[46:47], v165 offset:18560\n"
    "ds_read_b64 v[48:49], v162 offset:18688\n"
    "ds_read_b64 v[50:51], v163 offset:18688\n"
    "ds_read_b64 v[52:53], v164 offset:18688\n"
    "ds_read_b64 v[54:55], v165 offset:18688\n"
    "s_waitcnt lgkmcnt(11)\n"
    "v_lshlrev_b32 v170, 16, v32\n"
    "v_and_b32 v171, s78, v32\n"
    "v_lshlrev_b32 v172, 16, v33\n"
    "v_and_b32 v173, s78, v33\n"
    "s_waitcnt lgkmcnt(10)\n"
    "v_lshlrev_b32 v174, 16, v34\n"
    "v_and_b32 v175, s78, v34\n"
    "v_lshlrev_b32 v176, 16, v35\n"
    "v_and_b32 v177, s78, v35\n"
    "v_pk_mul_f32 v[170:171], v[170:171], s[92:93] op_sel:[0,1] op_sel_hi:[1,1]\n"
    "v_pk_mul_f32 v[172:173], v[172:173], s[92:93] op_sel:[0,1] op_sel_hi:[1,1]\n"
    "v_pk_mul_f32 v[174:175], v[174:175], s[92:93] op_sel:[0,1] op_sel_hi:[1,1]\n"
    "v_pk_mul_f32 v[176:177], v[176:177], s[92:93] op_sel:[0,1] op_sel_hi:[1,1]\n"
    "v_cvt_pk_bf16_f32 v8, v170, v171\n"
    "v_cvt_pk_bf16_f32 v9, v172, v173\n"
    "v_cvt_pk_bf16_f32 v10, v174, v175\n"
    "v_cvt_pk_bf16_f32 v11, v176, v177\n"
    "s_waitcnt lgkmcnt(9)\n"
    "v_lshlrev_b32 v170, 16, v36\n"
    "v_and_b32 v171, s78, v36\n"
    "v_lshlrev_b32 v172, 16, v37\n"
    "v_and_b32 v173, s78, v37\n"
    "s_waitcnt lgkmcnt(8)\n"
    "v_lshlrev_b32 v174, 16, v38\n"
    "v_and_b32 v175, s78, v38\n"
    "v_lshlrev_b32 v176, 16, v39\n"
    "v_and_b32 v177, s78, v39\n"
    "v_pk_mul_f32 v[170:171], v[170:171], s[92:93] op_sel:[0,1] op_sel_hi:[1,1]\n"
    "v_pk_mul_f32 v[172:173], v[172:173], s[92:93] op_sel:[0,1] op_sel_hi:[1,1]\n"
    "v_pk_mul_f32 v[174:175], v[174:175], s[92:93] op_sel:[0,1] op_sel_hi:[1,1]\n"
    "v_pk_mul_f32 v[176:177], v[176:177], s[92:93] op_sel:[0,1] op_sel_hi:[1,1]\n"
    "v_cvt_pk_bf16_f32 v12, v170, v171\n"
    "v_cvt_pk_bf16_f32 v13, v172, v173\n"
    "v_cvt_pk_bf16_f32 v14, v174, v175\n"
    "v_cvt_pk_bf16_f32 v15, v176, v177\n"
    "s_waitcnt lgkmcnt(7)\n"
    "v_lshlrev_b32 v170, 16, v40\n"
    "v_and_b32 v171, s78, v40\n"
    "v_lshlrev_b32 v172, 16, v41\n"
    "v_and_b32 v173, s78, v41\n"
    "s_waitcnt lgkmcnt(6)\n"
    "v_lshlrev_b32 v174, 16, v42\n"
    "v_and_b32 v175, s78, v42\n"
    "v_lshlrev_b32 v176, 16, v43\n"
    "v_and_b32 v177, s78, v43\n"
    "v_pk_mul_f32 v[170:171], v[170:171], s[92:93] op_sel:[0,1] op_sel_hi:[1,1]\n"
    "v_pk_mul_f32 v[172:173], v[172:173], s[92:93] op_sel:[0,1] op_sel_hi:[1,1]\n"
    "v_pk_mul_f32 v[174:175], v[174:175], s[92:93] op_sel:[0,1] op_sel_hi:[1,1]\n"
    "v_pk_mul_f32 v[176:177], v[176:177], s[92:93] op_sel:[0,1] op_sel_hi:[1,1]\n"
    "v_cvt_pk_bf16_f32 v16, v170, v171\n"
    "v_cvt_pk_bf16_f32 v17, v172, v173\n"
    "v_cvt_pk_bf16_f32 v18, v174, v175\n"
    "v_cvt_pk_bf16_f32 v19, v176, v177\n"
    "s_waitcnt lgkmcnt(5)\n"
    "v_lshlrev_b32 v170, 16, v44\n"
    "v_and_b32 v171, s78, v44\n"
    "v_lshlrev_b32 v172, 16, v45\n"
    "v_and_b32 v173, s78, v45\n"
    "s_waitcnt lgkmcnt(4)\n"
    "v_lshlrev_b32 v174, 16, v46\n"
    "v_and_b32 v175, s78, v46\n"
    "v_lshlrev_b32 v176, 16, v47\n"
    "v_and_b32 v177, s78, v47\n"
    "v_pk_mul_f32 v[170:171], v[170:171], s[92:93] op_sel:[0,1] op_sel_hi:[1,1]\n"
    "v_pk_mul_f32 v[172:173], v[172:173], s[92:93] op_sel:[0,1] op_sel_hi:[1,1]\n"
    "v_pk_mul_f32 v[174:175], v[174:175], s[92:93] op_sel:[0,1] op_sel_hi:[1,1]\n"
    "v_pk_mul_f32 v[176:177], v[176:177], s[92:93] op_sel:[0,1] op_sel_hi:[1,1]\n"
    "v_cvt_pk_bf16_f32 v20, v170, v171\n"
    "v_cvt_pk_bf16_f32 v21, v172, v173\n"
    "v_cvt_pk_bf16_f32 v22, v174, v175\n"
    "v_cvt_pk_bf16_f32 v23, v176, v177\n"
    "s_waitcnt lgkmcnt(3)\n"
    "v_lshlrev_b32 v170, 16, v48\n"
    "v_and_b32 v171, s78, v48\n"
    "v_lshlrev_b32 v172, 16, v49\n"
    "v_and_b32 v173, s78, v49\n"
    "s_waitcnt lgkmcnt(2)\n"
    "v_lshlrev_b32 v174, 16, v50\n"
    "v_and_b32 v175, s78, v50\n"
    "v_lshlrev_b32 v176, 16, v51\n"
    "v_and_b32 v177, s78, v51\n"
    "v_pk_mul_f32 v[170:171], v[170:171], s[92:93] op_sel:[0,1] op_sel_hi:[1,1]\n"
    "v_pk_mul_f32 v[172:173], v[172:173], s[92:93] op_sel:[0,1] op_sel_hi:[1,1]\n"
    "v_pk_mul_f32 v[174:175], v[174:175], s[92:93] op_sel:[0,1] op_sel_hi:[1,1]\n"
    "v_pk_mul_f32 v[176:177], v[176:177], s[92:93] op_sel:[0,1] op_sel_hi:[1,1]\n"
    "v_cvt_pk_bf16_f32 v24, v170, v171\n"
    "v_cvt_pk_bf16_f32 v25, v172, v173\n"
    "v_cvt_pk_bf16_f32 v26, v174, v175\n"
    "v_cvt_pk_bf16_f32 v27, v176, v177\n"
    "s_waitcnt lgkmcnt(1)\n"
    "v_lshlrev_b32 v170, 16, v52\n"
    "v_and_b32 v171, s78, v52\n"
    "v_lshlrev_b32 v172, 16, v53\n"
    "v_and_b32 v173, s78, v53\n"
    "s_waitcnt lgkmcnt(0)\n"
    "v_lshlrev_b32 v174, 16, v54\n"
    "v_and_b32 v175, s78, v54\n"
    "v_lshlrev_b32 v176, 16, v55\n"
    "v_and_b32 v177, s78, v55\n"
    "v_pk_mul_f32 v[170:171], v[170:171], s[92:93] op_sel:[0,1] op_sel_hi:[1,1]\n"
    "v_pk_mul_f32 v[172:173], v[172:173], s[92:93] op_sel:[0,1] op_sel_hi:[1,1]\n"
    "v_pk_mul_f32 v[174:175], v[174:175], s[92:93] op_sel:[0,1] op_sel_hi:[1,1]\n"
    "v_pk_mul_f32 v[176:177], v[176:177], s[92:93] op_sel:[0,1] op_sel_hi:[1,1]\n"
    "v_cvt_pk_bf16_f32 v28, v170, v171\n"
    "v_cvt_pk_bf16_f32 v29, v172, v173\n"
    "v_cvt_pk_bf16_f32 v30, v174, v175\n"
    "v_cvt_pk_bf16_f32 v31, v176, v177\n"
    "buffer_load_dwordx2 v[214:215], v248, s[40:43], s82 offen offset:128 nt\n"
    "buffer_load_dwordx2 v[216:217], v248, s[40:43], s82 offen offset:896 nt\n"
    "buffer_load_dwordx2 v[218:219], v248, s[40:43], s82 offen offset:1664 nt\n"
    "buffer_load_dwordx2 v[220:221], v248, s[40:43], s82 offen offset:2432 nt\n"
    "buffer_load_dwordx2 v[222:223], v248, s[40:43], s83 offen offset:128 nt\n"
    "buffer_load_dwordx2 v[224:225], v248, s[40:43], s83 offen offset:896 nt\n"
    "buffer_load_dwordx2 v[226:227], v248, s[40:43], s83 offen offset:1664 nt\n"
    "buffer_load_dwordx2 v[228:229], v248, s[40:43], s83 offen offset:2432 nt\n"
    "s_waitcnt vmcnt(20)\n"
    "s_waitcnt lgkmcnt(0)\n"
    "s_barrier\n"
    "ds_read_b128 v[150:153], v254 offset:0\n"
    "ds_read_b128 v[154:157], v251 offset:0\n"
    "ds_read_b128 v[158:161], v254 offset:2048\n"
    "ds_read_b128 v[162:165], v251 offset:2048\n"
    "ds_read_b128 v[166:169], v254 offset:4096\n"
    "ds_read_b128 v[170:173], v251 offset:4096\n"
    "ds_read_b128 v[174:177], v254 offset:6144\n"
    "s_waitcnt lgkmcnt(6)\n"
    "v_mfma_f32_16x16x32_bf16 v[64:67], v[150:153], a[192:195], 0\n"
    "s_waitcnt lgkmcnt(5)\n"
    "v_mfma_f32_16x16x32_bf16 v[64:67], v[154:157], a[196:199], v[64:67]\n"
    "s_waitcnt lgkmcnt(4)\n"
    "v_mfma_f32_16x16x32_bf16 v[64:67], v[158:161], a[200:203], v[64:67]\n"
    "s_waitcnt lgkmcnt(3)\n"
    "v_mfma_f32_16x16x32_bf16 v[64:67], v[162:165], a[204:207], v[64:67]\n"
    "s_waitcnt lgkmcnt(2)\n"
    "v_mfma_f32_16x16x32_bf16 v[64:67], v[166:169], a[208:211], v[64:67]\n"
    "s_waitcnt lgkmcnt(1)\n"
    "v_mfma_f32_16x16x32_bf16 v[64:67], v[170:173], a[212:215], v[64:67]\n"
    "v_mfma_f32_16x16x32_bf16 v[68:71], v[150:153], a[216:219], 0\n"
    "v_mfma_f32_16x16x32_bf16 v[68:71], v[154:157], a[220:223], v[68:71]\n"
    "v_mfma_f32_16x16x32_bf16 v[68:71], v[158:161], a[224:227], v[68:71]\n"
    "v_mfma_f32_16x16x32_bf16 v[68:71], v[162:165], a[228:231], v[68:71]\n"
    "v_mfma_f32_16x16x32_bf16 v[68:71], v[166:169], a[232:235], v[68:71]\n"
    "v_mfma_f32_16x16x32_bf16 v[68:71], v[170:173], a[236:239], v[68:71]\n"
    "v_mfma_f32_16x16x32_bf16 v[72:75], v[150:153], a[240:243], 0\n"
    "v_mfma_f32_16x16x32_bf16 v[72:75], v[154:157], a[244:247], v[72:75]\n"
    "v_mfma_f32_16x16x32_bf16 v[72:75], v[158:161], a[248:251], v[72:75]\n"
    "v_mfma_f32_16x16x32_bf16 v[72:75], v[162:165], a[252:255], v[72:75]\n"
    "v_mfma_f32_16x16x32_bf16 v[72:75], v[166:169], v[0:3], v[72:75]\n"
    "v_mfma_f32_16x16x32_bf16 v[72:75], v[170:173], v[4:7], v[72:75]\n"
    "v_mfma_f32_16x16x32_bf16 v[76:79], v[150:153], v[8:11], 0\n"
    "v_mfma_f32_16x16x32_bf16 v[76:79], v[154:157], v[12:15], v[76:79]\n"
    "v_mfma_f32_16x16x32_bf16 v[76:79], v[158:161], v[16:19], v[76:79]\n"
    "v_mfma_f32_16x16x32_bf16 v[76:79], v[162:165], v[20:23], v[76:79]\n"
    "v_mfma_f32_16x16x32_bf16 v[76:79], v[166:169], v[24:27], v[76:79]\n"
    "v_mfma_f32_16x16x32_bf16 v[76:79], v[170:173], v[28:31], v[76:79]\n"
    "v_max3_f32 v56, v64, v65, v66\n"
    "v_max_f32 v56, v56, v67\n"
    "v_max3_f32 v58, v68, v69, v70\n"
    "v_max_f32 v58, v58, v71\n"
    "v_max3_f32 v60, v72, v73, v74\n"
    "v_max_f32 v60, v60, v75\n"
    "s_nop 1\n"
    "v_max3_f32 v62, v76, v77, v78\n"
    "v_max_f32 v62, v62, v79\n"
    "v_mov_b32 v57, v56\n"
    "v_mov_b32 v59, v58\n"
    "v_mov_b32 v61, v60\n"
    "v_mov_b32 v63, v62\n"
    "v_permlane16_swap_b32 v56, v57\n"
    "v_permlane16_swap_b32 v58, v59\n"
    "v_permlane16_swap_b32 v60, v61\n"
    "v_permlane16_swap_b32 v62, v63\n"
    "v_max_f32 v56, v56, v57\n"
    "v_max_f32 v58, v58, v59\n"
    "v_max_f32 v60, v60, v61\n"
    "v_max_f32 v62, v62, v63\n"
    "v_mov_b32 v57, v56\n"
    "v_mov_b32 v59, v58\n"
    "v_mov_b32 v61, v60\n"
    "v_mov_b32 v63, v62\n"
    "v_permlane32_swap_b32 v56, v57\n"
    "v_permlane32_swap_b32 v58, v59\n"
    "v_permlane32_swap_b32 v60, v61\n"
    "v_permlane32_swap_b32 v62, v63\n"
    "v_max_f32 v56, v56, v57\n"
    "v_max_f32 v58, v58, v59\n"
    "v_max_f32 v60, v60, v61\n"
    "v_max_f32 v62, v62, v63\n"
    "v_sub_f32 v128, 0, v56\n"
    "v_sub_f32 v129, 0, v56\n"
    "v_sub_f32 v130, 0, v56\n"
    "v_sub_f32 v131, 0, v56\n"
    "v_sub_f32 v132, 0, v58\n"
    "v_sub_f32 v133, 0, v58\n"
    "v_sub_f32 v134, 0, v58\n"
    "v_sub_f32 v135, 0, v58\n"
    "v_sub_f32 v136, 0, v60\n"
    "v_sub_f32 v137, 0, v60\n"
    "v_sub_f32 v138, 0, v60\n"
    "v_sub_f32 v139, 0, v60\n"
    "v_sub_f32 v140, 0, v62\n"
    "v_sub_f32 v141, 0, v62\n"
    "v_sub_f32 v142, 0, v62\n"
    "v_sub_f32 v143, 0, v62\n"
    "v_mfma_f32_16x16x32_bf16 v[64:67], v[150:153], a[192:195], v[128:131]\n"
    "v_mfma_f32_16x16x32_bf16 v[64:67], v[154:157], a[196:199], v[64:67]\n"
    "ds_read_b128 v[178:181], v251 offset:6144\n"
    "v_mfma_f32_16x16x32_bf16 v[64:67], v[158:161], a[200:203], v[64:67]\n"
    "ds_read_b128 v[182:185], v254 offset:8192\n"
    "ds_read_b128 v[186:189], v251 offset:8192\n"
    "v_mfma_f32_16x16x32_bf16 v[64:67], v[162:165], a[204:207], v[64:67]\n"
    "v_mfma_f32_16x16x32_bf16 v[64:67], v[166:169], a[208:211], v[64:67]\n"
    "v_mfma_f32_16x16x32_bf16 v[64:67], v[170:173], a[212:215], v[64:67]\n"
    "v_mfma_f32_16x16x32_bf16 v[68:71], v[170:173], a[236:239], v[132:135]\n"
    "s_waitcnt vmcnt(18)\n"
    "v_cmp_ne_u64 vcc, s[80:81], v[252:253]\n"
    "s_nop 3\n"
    "s_cbranch_vccnz SPBV%=\n"
    "RTBV%=:\n"
    "s_mov_b32 s87, 512\n"
    "s_mul_i32 s89, %11, 6144\n"
    "s_add_u32 s88, s89, 0\n"
    "s_add_u32 m0, s89, 73728\n"
    "s_nop 0\n"
    "buffer_load_dwordx4 v149, s[60:63], s88 offen offset:0 sc1 lds\n"
    "v_mfma_f32_16x16x32_bf16 v[68:71], v[166:169], a[232:235], v[68:71]\n"
    "v_mfma_f32_16x16x32_bf16 v[68:71], v[162:165], a[228:231], v[68:71]\n"
    "buffer_load_dwordx4 v149, s[60:63], s88 offen offset:1024 sc1 lds\n"
    "v_mfma_f32_16x16x32_bf16 v[68:71], v[158:161], a[224:227], v[68:71]\n"
    "v_mfma_f32_16x16x32_bf16 v[68:71], v[154:157], a[220:223], v[68:71]\n"
    "buffer_load_dwordx4 v149, s[60:63], s88 offen offset:2048 sc1 lds\n"
    "v_mfma_f32_16x16x32_bf16 v[68:71], v[150:153], a[216:219], v[68:71]\n"
    "v_mfma_f32_16x16x32_bf16 v[72:75], v[150:153], a[240:243], v[136:139]\n"
    "v_exp_f32 v64, v64\n"
    "buffer_load_dwordx4 v149, s[60:63], s88 offen offset:3072 sc1 lds\n"
    "v_mfma_f32_16x16x32_bf16 v[72:75], v[154:157], a[244:247], v[72:75]\n"
    "v_add_f32 v144, v144, v64\n"
    "v_mfma_f32_16x16x32_bf16 v[72:75], v[158:161], a[248:251], v[72:75]\n"
    "s_add_u32 m0, m0, 4096\n"
    "s_add_u32 s88, s88, 4096\n"
    "buffer_load_dwordx4 v149, s[60:63], s88 offen offset:0 sc1 lds\n"
    "v_mfma_f32_16x16x32_bf16 v[72:75], v[162:165], a[252:255], v[72:75]\n"
    "v_exp_f32 v65, v65\n"
    "v_mfma_f32_16x16x32_bf16 v[72:75], v[166:169], v[0:3], v[72:75]\n"
    "v_add_f32 v144, v144, v65\n"
    "buffer_load_dwordx4 v149, s[60:63], s88 offen offset:1024 sc1 lds\n"
    "v_mfma_f32_16x16x32_bf16 v[72:75], v[170:173], v[4:7], v[72:75]\n"
    "v_mfma_f32_16x16x32_bf16 v[76:79], v[170:173], v[28:31], v[140:143]\n"
    "v_exp_f32 v66, v66\n"
    "buffer_load_dwordx2 v[252:253], v250, s[64:67], s87 offen sc1\n"
    "v_mfma_f32_16x16x32_bf16 v[76:79], v[166:169], v[24:27], v[76:79]\n"
    "ds_read_b128 v[170:173], v254 offset:10240\n"
    "ds_read_b128 v[166:169], v251 offset:10240\n"
    "v_mfma_f32_16x16x32_bf16 v[76:79], v[162:165], v[20:23], v[76:79]\n"
    "v_add_f32 v144, v144, v66\n"
    "buffer_load_dwordx2 v[32:33], v248, s[40:43], s82 offen offset:256 nt\n"
    "v_mfma_f32_16x16x32_bf16 v[76:79], v[158:161], v[16:19], v[76:79]\n"
    "v_exp_f32 v67, v67\n"
    "v_mfma_f32_16x16x32_bf16 v[76:79], v[154:157], v[12:15], v[76:79]\n"
    "v_add_f32 v144, v144, v67\n"
    "buffer_load_dwordx2 v[34:35], v248, s[40:43], s82 offen offset:1024 nt\n"
    "v_mfma_f32_16x16x32_bf16 v[76:79], v[150:153], v[8:11], v[76:79]\n"
    "ds_read_b128 v[162:165], v254 offset:12288\n"
    "s_waitcnt lgkmcnt(6)\n"
    "v_mfma_f32_16x16x32_bf16 v[80:83], v[174:177], a[192:195], v[128:131]\n"
    "v_exp_f32 v68, v68\n"
    "buffer_load_dwordx2 v[36:37], v248, s[40:43], s82 offen offset:1792 nt\n"
    "s_waitcnt lgkmcnt(5)\n"
    "v_mfma_f32_16x16x32_bf16 v[80:83], v[178:181], a[196:199], v[80:83]\n"
    "ds_read_b128 v[158:161], v251 offset:12288\n"
    "s_waitcnt lgkmcnt(5)\n"
    "v_mfma_f32_16x16x32_bf16 v[80:83], v[182:185], a[200:203], v[80:83]\n"
    "ds_read_b128 v[154:157], v254 offset:14336\n"
    "ds_read_b128 v[150:153], v251 offset:14336\n"
    "buffer_load_dwordx2 v[38:39], v248, s[40:43], s82 offen offset:2560 nt\n"
    "s_waitcnt lgkmcnt(6)\n"
    "v_mfma_f32_16x16x32_bf16 v[80:83], v[186:189], a[204:207], v[80:83]\n"
    "v_exp_f32 v69, v69\n"
    "s_waitcnt lgkmcnt(5)\n"
    "v_mfma_f32_16x16x32_bf16 v[80:83], v[170:173], a[208:211], v[80:83]\n"
    "v_add_f32 v145, v145, v68\n"
    "buffer_load_dwordx2 v[40:41], v248, s[40:43], s83 offen offset:256 nt\n"
    "s_waitcnt lgkmcnt(4)\n"
    "v_mfma_f32_16x16x32_bf16 v[80:83], v[166:169], a[212:215], v[80:83]\n"
    "v_add_f32 v145, v145, v69\n"
    "v_mfma_f32_16x16x32_bf16 v[84:87], v[166:169], a[236:239], v[132:135]\n"
    "v_exp_f32 v70, v70\n"
    "buffer_load_dwordx2 v[42:43], v248, s[40:43], s83 offen offset:1024 nt\n"
    "v_mfma_f32_16x16x32_bf16 v[84:87], v[170:173], a[232:235], v[84:87]\n"
    "v_add_f32 v145, v145, v70\n"
    "v_mfma_f32_16x16x32_bf16 v[84:87], v[186:189], a[228:231], v[84:87]\n"
    "buffer_load_dwordx2 v[44:45], v248, s[40:43], s83 offen offset:1792 nt\n"
    "v_mfma_f32_16x16x32_bf16 v[84:87], v[182:185], a[224:227], v[84:87]\n"
    "v_exp_f32 v71, v71\n"
    "v_mfma_f32_16x16x32_bf16 v[84:87], v[178:181], a[220:223], v[84:87]\n"
    "v_add_f32 v145, v145, v71\n"
    "buffer_load_dwordx2 v[46:47], v248, s[40:43], s83 offen offset:2560 nt\n"
    "v_mfma_f32_16x16x32_bf16 v[84:87], v[174:177], a[216:219], v[84:87]\n"
    "v_mfma_f32_16x16x32_bf16 v[88:91], v[174:177], a[240:243], v[136:139]\n"
    "v_exp_f32 v72, v72\n"
    "s_add_u32 s82, s82, 196608\n"
    "s_add_u32 s83, s83, 196608\n"
    "s_add_u32 s84, s84, 512\n"
    "v_mfma_f32_16x16x32_bf16 v[88:91], v[178:181], a[244:247], v[88:91]\n"
    "v_add_f32 v146, v146, v72\n"
    "v_mfma_f32_16x16x32_bf16 v[88:91], v[182:185], a[248:251], v[88:91]\n"
    "v_mfma_f32_16x16x32_bf16 v[88:91], v[186:189], a[252:255], v[88:91]\n"
    "v_exp_f32 v73, v73\n"
    "v_mfma_f32_16x16x32_bf16 v[88:91], v[170:173], v[0:3], v[88:91]\n"
    "v_add_f32 v146, v146, v73\n"
    "v_mfma_f32_16x16x32_bf16 v[88:91], v[166:169], v[4:7], v[88:91]\n"
    "v_mfma_f32_16x16x32_bf16 v[92:95], v[166:169], v[28:31], v[140:143]\n"
    "v_exp_f32 v74, v74\n"
    "v_mfma_f32_16x16x32_bf16 v[92:95], v[170:173], v[24:27], v[92:95]\n"
    "ds_read_b128 v[166:169], v254 offset:16384\n"
    "ds_read_b128 v[170:173], v251 offset:16384\n"
    "v_mfma_f32_16x16x32_bf16 v[92:95], v[186:189], v[20:23], v[92:95]\n"
    "v_add_f32 v146, v146, v74\n"
    "v_mfma_f32_16x16x32_bf16 v[92:95], v[182:185], v[16:19], v[92:95]\n"
    "v_exp_f32 v75, v75\n"
    "v_mfma_f32_16x16x32_bf16 v[92:95], v[178:181], v[12:15], v[92:95]\n"
    "v_add_f32 v146, v146, v75\n"
    "v_mfma_f32_16x16x32_bf16 v[92:95], v[174:177], v[8:11], v[92:95]\n"
    "ds_read_b128 v[186:189], v254 offset:18432\n"
    "s_waitcnt lgkmcnt(6)\n"
    "v_mfma_f32_16x16x32_bf16 v[96:99], v[162:165], a[192:195], v[128:131]\n"
    "v_exp_f32 v76, v76\n"
    "s_waitcnt lgkmcnt(5)\n"
    "v_mfma_f32_16x16x32_bf16 v[96:99], v[158:161], a[196:199], v[96:99]\n"
    "ds_read_b128 v[182:185], v251 offset:18432\n"
    "s_waitcnt lgkmcnt(5)\n"
    "v_mfma_f32_16x16x32_bf16 v[96:99], v[154:157], a[200:203], v[96:99]\n"
    "ds_read_b128 v[178:181], v254 offset:20480\n"
    "ds_read_b128 v[174:177], v251 offset:20480\n"
    "s_waitcnt lgkmcnt(6)\n"
    "v_mfma_f32_16x16x32_bf16 v[96:99], v[150:153], a[204:207], v[96:99]\n"
    "v_exp_f32 v77, v77\n"
    "s_waitcnt lgkmcnt(5)\n"
    "v_mfma_f32_16x16x32_bf16 v[96:99], v[166:169], a[208:211], v[96:99]\n"
    "v_add_f32 v147, v147, v76\n"
    "s_waitcnt lgkmcnt(4)\n"
    "v_mfma_f32_16x16x32_bf16 v[96:99], v[170:173], a[212:215], v[96:99]\n"
    "v_add_f32 v147, v147, v77\n"
    "v_mfma_f32_16x16x32_bf16 v[100:103], v[170:173], a[236:239], v[132:135]\n"
    "v_exp_f32 v78, v78\n"
    "v_mfma_f32_16x16x32_bf16 v[100:103], v[166:169], a[232:235], v[100:103]\n"
    "v_add_f32 v147, v147, v78\n"
    "v_mfma_f32_16x16x32_bf16 v[100:103], v[150:153], a[228:231], v[100:103]\n"
    "v_mfma_f32_16x16x32_bf16 v[100:103], v[154:157], a[224:227], v[100:103]\n"
    "v_exp_f32 v79, v79\n"
    "v_mfma_f32_16x16x32_bf16 v[100:103], v[158:161], a[220:223], v[100:103]\n"
    "v_add_f32 v147, v147, v79\n"
    "v_mfma_f32_16x16x32_bf16 v[100:103], v[162:165], a[216:219], v[100:103]\n"
    "v_mfma_f32_16x16x32_bf16 v[104:107], v[162:165], a[240:243], v[136:139]\n"
    "v_exp_f32 v80, v80\n"
    "v_mfma_f32_16x16x32_bf16 v[104:107], v[158:161], a[244:247], v[104:107]\n"
    "v_add_f32 v144, v144, v80\n"
    "v_mfma_f32_16x16x32_bf16 v[104:107], v[154:157], a[248:251], v[104:107]\n"
    "v_mfma_f32_16x16x32_bf16 v[104:107], v[150:153], a[252:255], v[104:107]\n"
    "v_exp_f32 v81, v81\n"
    "v_mfma_f32_16x16x32_bf16 v[104:107], v[166:169], v[0:3], v[104:107]\n"
    "v_add_f32 v144, v144, v81\n"
    "v_mfma_f32_16x16x32_bf16 v[104:107], v[170:173], v[4:7], v[104:107]\n"
    "v_mfma_f32_16x16x32_bf16 v[108:111], v[170:173], v[28:31], v[140:143]\n"
    "v_exp_f32 v82, v82\n"
    "v_mfma_f32_16x16x32_bf16 v[108:111], v[166:169], v[24:27], v[108:111]\n"
    "ds_read_b128 v[170:173], v254 offset:22528\n"
    "ds_read_b128 v[166:169], v251 offset:22528\n"
    "v_mfma_f32_16x16x32_bf16 v[108:111], v[150:153], v[20:23], v[108:111]\n"
    "v_mfma_f32_16x16x32_bf16 v[108:111], v[154:157], v[16:19], v[108:111]\n"
    "v_exp_f32 v83, v83\n"
    "v_mfma_f32_16x16x32_bf16 v[108:111], v[158:161], v[12:15], v[108:111]\n"
    "v_mfma_f32_16x16x32_bf16 v[108:111], v[162:165], v[8:11], v[108:111]\n"
    "s_waitcnt lgkmcnt(5)\n"
    "v_mfma_f32_16x16x32_bf16 v[112:115], v[186:189], a[192:195], v[128:131]\n"
    "v_exp_f32 v84, v84\n"
    "s_waitcnt lgkmcnt(4)\n"
    "v_mfma_f32_16x16x32_bf16 v[112:115], v[182:185], a[196:199], v[112:115]\n"
    "s_waitcnt lgkmcnt(3)\n"
    "v_mfma_f32_16x16x32_bf16 v[112:115], v[178:181], a[200:203], v[112:115]\n"
    "s_waitcnt lgkmcnt(2)\n"
    "v_mfma_f32_16x16x32_bf16 v[112:115], v[174:177], a[204:207], v[112:115]\n"
    "v_exp_f32 v85, v85\n"
    "s_waitcnt lgkmcnt(1)\n"
    "v_mfma_f32_16x16x32_bf16 v[112:115], v[170:173], a[208:211], v[112:115]\n"
    "v_add_f32 v144, v144, v82\n"
    "s_waitcnt lgkmcnt(0)\n"
    "v_mfma_f32_16x16x32_bf16 v[112:115], v[166:169], a[212:215], v[112:115]\n"
    "v_add_f32 v144, v144, v83\n"
    "v_mfma_f32_16x16x32_bf16 v[116:119], v[166:169], a[236:239], v[132:135]\n"
    "v_exp_f32 v86, v86\n"
    "v_mfma_f32_16x16x32_bf16 v[116:119], v[170:173], a[232:235], v[116:119]\n"
    "v_mfma_f32_16x16x32_bf16 v[116:119], v[174:177], a[228:231], v[116:119]\n"
    "v_mfma_f32_16x16x32_bf16 v[116:119], v[178:181], a[224:227], v[116:119]\n"
    "v_exp_f32 v87, v87\n"
    "v_mfma_f32_16x16x32_bf16 v[116:119], v[182:185], a[220:223], v[116:119]\n"
    "v_add_f32 v145, v145, v84\n"
    "v_mfma_f32_16x16x32_bf16 v[116:119], v[186:189], a[216:219], v[116:119]\n"
    "v_add_f32 v145, v145, v85\n"
    "v_mfma_f32_16x16x32_bf16 v[120:123], v[186:189], a[240:243], v[136:139]\n"
    "v_exp_f32 v88, v88\n"
    "v_mfma_f32_16x16x32_bf16 v[120:123], v[182:185], a[244:247], v[120:123]\n"
    "v_mfma_f32_16x16x32_bf16 v[120:123], v[178:181], a[248:251], v[120:123]\n"
    "v_add_f32 v145, v145, v86\n"
    "v_add_f32 v145, v145, v87\n"
    "v_mfma_f32_16x16x32_bf16 v[120:123], v[174:177], a[252:255], v[120:123]\n"
    "v_exp_f32 v89, v89\n"
    "v_mfma_f32_16x16x32_bf16 v[120:123], v[170:173], v[0:3], v[120:123]\n"
    "v_add_f32 v146, v146, v88\n"
    "v_add_f32 v146, v146, v89\n"
    "v_mfma_f32_16x16x32_bf16 v[120:123], v[166:169], v[4:7], v[120:123]\n"
    "v_mfma_f32_16x16x32_bf16 v[124:127], v[166:169], v[28:31], v[140:143]\n"
    "v_exp_f32 v90, v90\n"
    "v_mfma_f32_16x16x32_bf16 v[124:127], v[170:173], v[24:27], v[124:127]\n"
    "v_mfma_f32_16x16x32_bf16 v[124:127], v[174:177], v[20:23], v[124:127]\n"
    "v_mfma_f32_16x16x32_bf16 v[124:127], v[178:181], v[16:19], v[124:127]\n"
    "v_exp_f32 v91, v91\n"
    "v_mfma_f32_16x16x32_bf16 v[124:127], v[182:185], v[12:15], v[124:127]\n"
    "v_add_f32 v146, v146, v90\n"
    "v_mfma_f32_16x16x32_bf16 v[124:127], v[186:189], v[8:11], v[124:127]\n"
    "v_exp_f32 v92, v92\n"
    "v_add_f32 v146, v146, v91\n"
    "v_add_f32 v147, v147, v92\n"
    "v_exp_f32 v93, v93\n"
    "s_nop 0\n"
    "v_add_f32 v147, v147, v93\n"
    "v_exp_f32 v94, v94\n"
    "v_exp_f32 v95, v95\n"
    "v_add_f32 v147, v147, v94\n"
    "v_add_f32 v147, v147, v95\n"
    "v_exp_f32 v96, v96\n"
    "s_nop 0\n"
    "v_add_f32 v144, v144, v96\n"
    "v_exp_f32 v97, v97\n"
    "s_nop 0\n"
    "v_add_f32 v144, v144, v97\n"
    "v_exp_f32 v98, v98\n"
    "s_nop 0\n"
    "v_add_f32 v144, v144, v98\n"
    "v_exp_f32 v99, v99\n"
    "s_nop 0\n"
    "v_add_f32 v144, v144, v99\n"
    "v_exp_f32 v100, v100\n"
    "s_nop 0\n"
    "v_add_f32 v145, v145, v100\n"
    "v_exp_f32 v101, v101\n"
    "s_nop 0\n"
    "v_add_f32 v145, v145, v101\n"
    "v_exp_f32 v102, v102\n"
    "s_nop 0\n"
    "v_add_f32 v145, v145, v102\n"
    "v_exp_f32 v103, v103\n"
    "s_nop 0\n"
    "v_add_f32 v145, v145, v103\n"
    "v_exp_f32 v104, v104\n"
    "s_nop 0\n"
    "v_add_f32 v146, v146, v104\n"
    "v_exp_f32 v105, v105\n"
    "s_nop 0\n"
    "v_add_f32 v146, v146, v105\n"
    "v_exp_f32 v106, v106\n"
    "s_nop 0\n"
    "v_add_f32 v146, v146, v106\n"
    "v_exp_f32 v107, v107\n"
    "v_exp_f32 v108, v108\n"
    "v_add_f32 v146, v146, v107\n"
    "v_add_f32 v147, v147, v108\n"
    "v_exp_f32 v109, v109\n"
    "s_nop 0\n"
    "v_add_f32 v147, v147, v109\n"
    "v_exp_f32 v110, v110\n"
    "v_exp_f32 v111, v111\n"
    "v_add_f32 v147, v147, v110\n"
    "v_add_f32 v147, v147, v111\n"
    "v_exp_f32 v112, v112\n"
    "s_nop 0\n"
    "v_add_f32 v144, v144, v112\n"
    "v_exp_f32 v113, v113\n"
    "s_nop 0\n"
    "v_add_f32 v144, v144, v113\n"
    "v_exp_f32 v114, v114\n"
    "s_waitcnt vmcnt(23)\n"
    "s_mov_b32 m0, s74\n"
    "s_add_u32 s74, s74, s73\n"
    "s_cmp_ge_u32 s74, s75\n"
    "s_cselect_b32 s73, 0xfffe8000, s76\n"
    "buffer_load_dwordx4 v149, s[36:39], s72 offen offset:0 sc1 lds\n"
    "buffer_load_dwordx4 v149, s[36:39], s72 offen offset:1024 sc1 lds\n"
    "buffer_load_dwordx4 v149, s[36:39], s72 offen offset:2048 sc1 lds\n"
    "buffer_load_dwordx4 v149, s[36:39], s72 offen offset:3072 sc1 lds\n"
    "s_add_u32 s72, s72, 4096\n"
    "s_add_u32 m0, m0, 4096\n"
    "s_nop 0\n"
    "buffer_load_dwordx4 v149, s[36:39], s72 offen offset:0 sc1 lds\n"
    "buffer_load_dwordx4 v149, s[36:39], s72 offen offset:1024 sc1 lds\n"
    "buffer_load_dwordx4 v149, s[36:39], s72 offen offset:2048 sc1 lds\n"
    "buffer_load_dwordx4 v149, s[36:39], s72 offen offset:3072 sc1 lds\n"
    "s_add_u32 s72, s72, 4096\n"
    "s_add_u32 m0, m0, 4096\n"
    "s_nop 0\n"
    "buffer_load_dwordx4 v149, s[36:39], s72 offen offset:0 sc1 lds\n"
    "buffer_load_dwordx4 v149, s[36:39], s72 offen offset:1024 sc1 lds\n"
    "buffer_load_dwordx4 v149, s[36:39], s72 offen offset:2048 sc1 lds\n"
    "buffer_load_dwordx4 v149, s[36:39], s72 offen offset:3072 sc1 lds\n"
    "s_add_u32 s72, s72, 16384\n"
    "v_lshlrev_b32 v230, 16, v210\n"
    "v_and_b32 v231, s78, v210\n"
    "v_lshlrev_b32 v232, 16, v211\n"
    "v_and_b32 v233, s78, v211\n"
    "v_lshlrev_b32 v234, 16, v212\n"
    "v_and_b32 v235, s78, v212\n"
    "v_lshlrev_b32 v236, 16, v213\n"
    "v_and_b32 v237, s78, v213\n"
    "v_and_b32 v210, s78, v206\n"
    "v_lshlrev_b32 v206, 16, v206\n"
    "v_and_b32 v211, s78, v207\n"
    "v_lshlrev_b32 v207, 16, v207\n"
    "v_and_b32 v212, s78, v208\n"
    "v_lshlrev_b32 v208, 16, v208\n"
    "v_and_b32 v213, s78, v209\n"
    "v_lshlrev_b32 v209, 16, v209\n"
    "v_mul_f32 v206, v206, v230\n"
    "v_mul_f32 v210, v210, v231\n"
    "v_mul_f32 v207, v207, v232\n"
    "v_mul_f32 v211, v211, v233\n"
    "v_mul_f32 v208, v208, v234\n"
    "v_mul_f32 v212, v212, v235\n"
    "v_mul_f32 v209, v209, v236\n"
    "v_mul_f32 v213, v213, v237\n"
    "v_cvt_f32_i32 v190, v190\n"
    "v_cvt_f32_i32 v192, v192\n"
    "v_cvt_f32_i32 v191, v191\n"
    "v_cvt_f32_i32 v193, v193\n"
    "v_fma_f32 v190, v190, v230, -v206\n"
    "v_fma_f32 v192, v192, v231, -v210\n"
    "v_fma_f32 v191, v191, v230, -v206\n"
    "v_fma_f32 v193, v193, v231, -v210\n"
    "v_cvt_pk_bf16_f32 v238, v190, v192\n"
    "v_cvt_pk_bf16_f32 v242, v191, v193\n"
    "v_cvt_f32_i32 v194, v194\n"
    "v_cvt_f32_i32 v196, v196\n"
    "v_cvt_f32_i32 v195, v195\n"
    "v_cvt_f32_i32 v197, v197\n"
    "v_fma_f32 v194, v194, v232, -v207\n"
    "v_fma_f32 v196, v196, v233, -v211\n"
    "v_fma_f32 v195, v195, v232, -v207\n"
    "v_fma_f32 v197, v197, v233, -v211\n"
    "v_cvt_pk_bf16_f32 v239, v194, v196\n"
    "v_cvt_pk_bf16_f32 v243, v195, v197\n"
    "v_cvt_f32_i32 v198, v198\n"
    "v_cvt_f32_i32 v200, v200\n"
    "v_cvt_f32_i32 v199, v199\n"
    "v_cvt_f32_i32 v201, v201\n"
    "v_fma_f32 v198, v198, v234, -v208\n"
    "v_fma_f32 v200, v200, v235, -v212\n"
    "v_fma_f32 v199, v199, v234, -v208\n"
    "v_fma_f32 v201, v201, v235, -v212\n"
    "v_cvt_pk_bf16_f32 v240, v198, v200\n"
    "v_cvt_pk_bf16_f32 v244, v199, v201\n"
    "v_cvt_f32_i32 v202, v202\n"
    "v_cvt_f32_i32 v204, v204\n"
    "v_cvt_f32_i32 v203, v203\n"
    "v_cvt_f32_i32 v205, v205\n"
    "v_fma_f32 v202, v202, v236, -v209\n"
    "v_fma_f32 v204, v204, v237, -v213\n"
    "v_fma_f32 v203, v203, v236, -v209\n"
    "v_fma_f32 v205, v205, v237, -v213\n"
    "v_cvt_pk_bf16_f32 v241, v202, v204\n"
    "v_cvt_pk_bf16_f32 v245, v203, v205\n"
    "buffer_store_dwordx4 v[238:241], v149, s[52:55], s85 offen offset:0 sc1\n"
    "buffer_store_dwordx4 v[242:245], v149, s[52:55], s85 offen offset:1024 sc1\n"
    "s_waitcnt vmcnt(29)\n"
    "v_cvt_f32_i32 v214, v214\n"
    "v_cvt_f32_i32 v216, v216\n"
    "v_cvt_f32_i32 v215, v215\n"
    "v_cvt_f32_i32 v217, v217\n"
    "v_fma_f32 v214, v214, v230, -v206\n"
    "v_fma_f32 v216, v216, v231, -v210\n"
    "v_fma_f32 v215, v215, v230, -v206\n"
    "v_fma_f32 v217, v217, v231, -v210\n"
    "v_cvt_pk_bf16_f32 v190, v214, v216\n"
    "v_cvt_pk_bf16_f32 v194, v215, v217\n"
    "v_cvt_f32_i32 v218, v218\n"
    "v_cvt_f32_i32 v220, v220\n"
    "v_cvt_f32_i32 v219, v219\n"
    "v_cvt_f32_i32 v221, v221\n"
    "v_fma_f32 v218, v218, v232, -v207\n"
    "v_fma_f32 v220, v220, v233, -v211\n"
    "v_fma_f32 v219, v219, v232, -v207\n"
    "v_fma_f32 v221, v221, v233, -v211\n"
    "v_cvt_pk_bf16_f32 v191, v218, v220\n"
    "v_cvt_pk_bf16_f32 v195, v219, v221\n"
    "v_cvt_f32_i32 v222, v222\n"
    "v_cvt_f32_i32 v224, v224\n"
    "v_cvt_f32_i32 v223, v223\n"
    "v_cvt_f32_i32 v225, v225\n"
    "v_fma_f32 v222, v222, v234, -v208\n"
    "v_fma_f32 v224, v224, v235, -v212\n"
    "v_fma_f32 v223, v223, v234, -v208\n"
    "v_fma_f32 v225, v225, v235, -v212\n"
    "v_cvt_pk_bf16_f32 v192, v222, v224\n"
    "v_cvt_pk_bf16_f32 v196, v223, v225\n"
    "v_cvt_f32_i32 v226, v226\n"
    "v_cvt_f32_i32 v228, v228\n"
    "v_cvt_f32_i32 v227, v227\n"
    "v_cvt_f32_i32 v229, v229\n"
    "v_fma_f32 v226, v226, v236, -v209\n"
    "v_fma_f32 v228, v228, v237, -v213\n"
    "v_fma_f32 v227, v227, v236, -v209\n"
    "v_fma_f32 v229, v229, v237, -v213\n"
    "v_cvt_pk_bf16_f32 v193, v226, v228\n"
    "v_cvt_pk_bf16_f32 v197, v227, v229\n"
    "buffer_store_dwordx4 v[190:193], v149, s[52:55], s85 offen offset:2048 sc1\n"
    "buffer_store_dwordx4 v[194:197], v149, s[52:55], s85 offen offset:3072 sc1\n"
    "s_waitcnt vmcnt(16)\n"
    "v_cvt_f32_i32 v32, v32\n"
    "v_cvt_f32_i32 v34, v34\n"
    "v_cvt_f32_i32 v33, v33\n"
    "v_cvt_f32_i32 v35, v35\n"
    "v_fma_f32 v32, v32, v230, -v206\n"
    "v_fma_f32 v34, v34, v231, -v210\n"
    "v_fma_f32 v33, v33, v230, -v206\n"
    "v_fma_f32 v35, v35, v231, -v210\n"
    "v_cvt_pk_bf16_f32 v198, v32, v34\n"
    "v_cvt_pk_bf16_f32 v202, v33, v35\n"
    "v_cvt_f32_i32 v36, v36\n"
    "v_cvt_f32_i32 v38, v38\n"
    "v_cvt_f32_i32 v37, v37\n"
    "v_cvt_f32_i32 v39, v39\n"
    "v_fma_f32 v36, v36, v232, -v207\n"
    "v_fma_f32 v38, v38, v233, -v211\n"
    "v_fma_f32 v37, v37, v232, -v207\n"
    "v_fma_f32 v39, v39, v233, -v211\n"
    "v_cvt_pk_bf16_f32 v199, v36, v38\n"
    "v_cvt_pk_bf16_f32 v203, v37, v39\n"
    "v_cvt_f32_i32 v40, v40\n"
    "v_cvt_f32_i32 v42, v42\n"
    "v_cvt_f32_i32 v41, v41\n"
    "v_cvt_f32_i32 v43, v43\n"
    "v_fma_f32 v40, v40, v234, -v208\n"
    "v_fma_f32 v42, v42, v235, -v212\n"
    "v_fma_f32 v41, v41, v234, -v208\n"
    "v_fma_f32 v43, v43, v235, -v212\n"
    "v_cvt_pk_bf16_f32 v200, v40, v42\n"
    "v_cvt_pk_bf16_f32 v204, v41, v43\n"
    "v_cvt_f32_i32 v44, v44\n"
    "v_cvt_f32_i32 v46, v46\n"
    "v_cvt_f32_i32 v45, v45\n"
    "v_cvt_f32_i32 v47, v47\n"
    "v_fma_f32 v44, v44, v236, -v209\n"
    "v_fma_f32 v46, v46, v237, -v213\n"
    "v_fma_f32 v45, v45, v236, -v209\n"
    "v_fma_f32 v47, v47, v237, -v213\n"
    "v_cvt_pk_bf16_f32 v201, v44, v46\n"
    "v_cvt_pk_bf16_f32 v205, v45, v47\n"
    "s_add_u32 s89, s85, 4096\n"
    "buffer_store_dwordx4 v[198:201], v149, s[52:55], s89 offen offset:0 sc1\n"
    "buffer_store_dwordx4 v[202:205], v149, s[52:55], s89 offen offset:1024 sc1\n"
    "s_add_u32 s85, s85, 98304\n"
    "v_cmp_ne_u64 vcc, s[80:81], v[252:253]\n"
    "s_nop 3\n"
    "s_cbranch_vccnz SPCV%=\n"
    "RTCV%=:\n"
    "s_mov_b32 s87, 768\n"
    "s_waitcnt vmcnt(27) lgkmcnt(0)\n"
    "s_barrier\n"
    "v_add_u32 v254, s73, v254\n"
    "v_add_u32 v251, s73, v251\n"
    "v_exp_f32 v115, v115\n"
    "buffer_load_dwordx4 v[206:209], v249, s[44:47], s84 offen nt\n"
    "ds_read_b128 v[150:153], v254 offset:0\n"
    "v_add_f32 v144, v144, v114\n"
    "v_exp_f32 v116, v116\n"
    "ds_read_b128 v[154:157], v251 offset:0\n"
    "v_add_f32 v144, v144, v115\n"
    "buffer_load_dwordx4 v[210:213], v249, s[48:51], s84 offen nt\n"
    "v_exp_f32 v117, v117\n"
    "buffer_load_dwordx2 v[190:191], v248, s[40:43], s82 offen offset:0 nt\n"
    "buffer_load_dwordx2 v[192:193], v248, s[40:43], s82 offen offset:768 nt\n"
    "v_exp_f32 v118, v118\n"
    "ds_read_b128 v[158:161], v254 offset:2048\n"
    "ds_read_b128 v[162:165], v251 offset:2048\n"
    "v_add_u32 v148, s73, v148\n"
    "ds_read_b128 v[166:169], v254 offset:4096\n"
    "v_exp_f32 v119, v119\n"
    "ds_read_b128 v[170:173], v251 offset:4096\n"
    "v_add_f32 v145, v145, v116\n"
    "buffer_load_dwordx2 v[194:195], v248, s[40:43], s82 offen offset:1536 nt\n"
    "v_exp_f32 v120, v120\n"
    "buffer_load_dwordx2 v[196:197], v248, s[40:43], s82 offen offset:2304 nt\n"
    "v_add_f32 v145, v145, v117\n"
    "v_add_f32 v145, v145, v118\n"
    "v_exp_f32 v121, v121\n"
    "v_cvt_pk_bf16_f32 v32, v64, v65\n"
    "v_add_f32 v145, v145, v119\n"
    "v_add_f32 v146, v146, v121\n"
    "buffer_load_dwordx2 v[198:199], v248, s[40:43], s83 offen offset:0 nt\n"
    "v_exp_f32 v122, v122\n"
    "v_cvt_pk_bf16_f32 v33, v66, v67\n"
    "v_add_f32 v146, v146, v120\n"
    "v_add_f32 v146, v146, v122\n"
    "buffer_load_dwordx2 v[200:201], v248, s[40:43], s83 offen offset:768 nt\n"
    "v_exp_f32 v123, v123\n"
    "v_cvt_pk_bf16_f32 v40, v68, v69\n"
    "v_add_f32 v146, v146, v123\n"
    "ds_read_b128 v[174:177], v254 offset:6144\n"
    ".p2align 6\n"
    "SUPV%=:\n"
    "s_waitcnt lgkmcnt(1)\n"
    "v_mfma_f32_16x16x32_bf16 v[64:67], v[150:153], a[192:195], v[128:131]\n"
    "v_exp_f32 v124, v124\n"
    "v_mfma_f32_16x16x32_bf16 v[64:67], v[154:157], a[196:199], v[64:67]\n"
    "ds_read_b128 v[178:181], v251 offset:6144\n"
    "v_cvt_pk_bf16_f32 v41, v70, v71\n"
    "v_mfma_f32_16x16x32_bf16 v[64:67], v[158:161], a[200:203], v[64:67]\n"
    "ds_read_b128 v[182:185], v254 offset:8192\n"
    "ds_read_b128 v[186:189], v251 offset:8192\n"
    "v_mfma_f32_16x16x32_bf16 v[64:67], v[162:165], a[204:207], v[64:67]\n"
    "v_exp_f32 v125, v125\n"
    "v_mfma_f32_16x16x32_bf16 v[64:67], v[166:169], a[208:211], v[64:67]\n"
    "buffer_load_dwordx2 v[202:203], v248, s[40:43], s83 offen offset:1536 nt\n"
    "v_mfma_f32_16x16x32_bf16 v[64:67], v[170:173], a[212:215], v[64:67]\n"
    "buffer_load_dwordx2 v[204:205], v248, s[40:43], s83 offen offset:2304 nt\n"
    "v_mfma_f32_16x16x32_bf16 v[68:71], v[170:173], a[236:239], v[132:135]\n"
    "v_exp_f32 v126, v126\n"
    "v_mfma_f32_16x16x32_bf16 v[68:71], v[166:169], a[232:235], v[68:71]\n"
    "v_cvt_pk_bf16_f32 v48, v72, v73\n"
    "v_cvt_pk_bf16_f32 v49, v74, v75\n"
    "v_mfma_f32_16x16x32_bf16 v[68:71], v[162:165], a[228:231], v[68:71]\n"
    "v_cvt_pk_bf16_f32 v56, v76, v77\n"
    "v_add_f32 v147, v147, v124\n"
    "v_mfma_f32_16x16x32_bf16 v[68:71], v[158:161], a[224:227], v[68:71]\n"
    "v_exp_f32 v127, v127\n"
    "v_mfma_f32_16x16x32_bf16 v[68:71], v[154:157], a[220:223], v[68:71]\n"
    "v_add_f32 v147, v147, v125\n"
    "v_add_f32 v147, v147, v126\n"
    "v_mfma_f32_16x16x32_bf16 v[68:71], v[150:153], a[216:219], v[68:71]\n"
    "v_cvt_pk_bf16_f32 v57, v78, v79\n"
    "v_add_f32 v147, v147, v127\n"
    "v_mfma_f32_16x16x32_bf16 v[72:75], v[150:153], a[240:243], v[136:139]\n"
    "v_exp_f32 v64, v64\n"
    "v_mfma_f32_16x16x32_bf16 v[72:75], v[154:157], a[244:247], v[72:75]\n"
    "v_add_f32 v144, v144, v64\n"
    "v_mfma_f32_16x16x32_bf16 v[72:75], v[158:161], a[248:251], v[72:75]\n"
    "v_cvt_pk_bf16_f32 v34, v80, v81\n"
    "v_mfma_f32_16x16x32_bf16 v[72:75], v[162:165], a[252:255], v[72:75]\n"
    "v_exp_f32 v65, v65\n"
    "v_mfma_f32_16x16x32_bf16 v[72:75], v[166:169], v[0:3], v[72:75]\n"
    "v_add_f32 v144, v144, v65\n"
    "v_mfma_f32_16x16x32_bf16 v[72:75], v[170:173], v[4:7], v[72:75]\n"
    "v_cvt_pk_bf16_f32 v35, v82, v83\n"
    "v_mfma_f32_16x16x32_bf16 v[76:79], v[170:173], v[28:31], v[140:143]\n"
    "v_exp_f32 v66, v66\n"
    "v_mfma_f32_16x16x32_bf16 v[76:79], v[166:169], v[24:27], v[76:79]\n"
    "ds_read_b128 v[170:173], v254 offset:10240\n"
    "ds_read_b128 v[166:169], v251 offset:10240\n"
    "v_mfma_f32_16x16x32_bf16 v[76:79], v[162:165], v[20:23], v[76:79]\n"
    "v_add_f32 v144, v144, v66\n"
    "v_cvt_pk_bf16_f32 v42, v84, v85\n"
    "v_mfma_f32_16x16x32_bf16 v[76:79], v[158:161], v[16:19], v[76:79]\n"
    "v_exp_f32 v67, v67\n"
    "v_mfma_f32_16x16x32_bf16 v[76:79], v[154:157], v[12:15], v[76:79]\n"
    "v_add_f32 v144, v144, v67\n"
    "v_cvt_pk_bf16_f32 v43, v86, v87\n"
    "v_mfma_f32_16x16x32_bf16 v[76:79], v[150:153], v[8:11], v[76:79]\n"
    "ds_read_b128 v[162:165], v254 offset:12288\n"
    "s_waitcnt lgkmcnt(3)\n"
    "v_mfma_f32_16x16x32_bf16 v[80:83], v[174:177], a[192:195], v[128:131]\n"
    "v_exp_f32 v68, v68\n"
    "v_mfma_f32_16x16x32_bf16 v[80:83], v[178:181], a[196:199], v[80:83]\n"
    "ds_read_b128 v[158:161], v251 offset:12288\n"
    "v_add_f32 v145, v145, v68\n"
    "v_mfma_f32_16x16x32_bf16 v[80:83], v[182:185], a[200:203], v[80:83]\n"
    "ds_read_b128 v[154:157], v254 offset:14336\n"
    "ds_read_b128 v[150:153], v251 offset:14336\n"
    "v_mfma_f32_16x16x32_bf16 v[80:83], v[186:189], a[204:207], v[80:83]\n"
    "v_exp_f32 v69, v69\n"
    "s_waitcnt lgkmcnt(4)\n"
    "v_mfma_f32_16x16x32_bf16 v[80:83], v[170:173], a[208:211], v[80:83]\n"
    "v_add_f32 v145, v145, v69\n"
    "v_cvt_pk_bf16_f32 v50, v88, v89\n"
    "v_mfma_f32_16x16x32_bf16 v[80:83], v[166:169], a[212:215], v[80:83]\n"
    "v_cvt_pk_bf16_f32 v51, v90, v91\n"
    "v_mfma_f32_16x16x32_bf16 v[84:87], v[166:169], a[236:239], v[132:135]\n"
    "v_exp_f32 v70, v70\n"
    "v_mfma_f32_16x16x32_bf16 v[84:87], v[170:173], a[232:235], v[84:87]\n"
    "v_add_f32 v145, v145, v70\n"
    "v_mfma_f32_16x16x32_bf16 v[84:87], v[186:189], a[228:231], v[84:87]\n"
    "v_cvt_pk_bf16_f32 v58, v92, v93\n"
    "v_mfma_f32_16x16x32_bf16 v[84:87], v[182:185], a[224:227], v[84:87]\n"
    "v_exp_f32 v71, v71\n"
    "v_mfma_f32_16x16x32_bf16 v[84:87], v[178:181], a[220:223], v[84:87]\n"
    "v_add_f32 v145, v145, v71\n"
    "v_mfma_f32_16x16x32_bf16 v[84:87], v[174:177], a[216:219], v[84:87]\n"
    "v_cvt_pk_bf16_f32 v59, v94, v95\n"
    "v_mfma_f32_16x16x32_bf16 v[88:91], v[174:177], a[240:243], v[136:139]\n"
    "v_exp_f32 v72, v72\n"
    "v_mfma_f32_16x16x32_bf16 v[88:91], v[178:181], a[244:247], v[88:91]\n"
    "v_add_f32 v146, v146, v72\n"
    "v_mfma_f32_16x16x32_bf16 v[88:91], v[182:185], a[248:251], v[88:91]\n"
    "v_cvt_pk_bf16_f32 v36, v96, v97\n"
    "v_mfma_f32_16x16x32_bf16 v[88:91], v[186:189], a[252:255], v[88:91]\n"
    "v_exp_f32 v73, v73\n"
    "v_mfma_f32_16x16x32_bf16 v[88:91], v[170:173], v[0:3], v[88:91]\n"
    "v_add_f32 v146, v146, v73\n"
    "v_mfma_f32_16x16x32_bf16 v[88:91], v[166:169], v[4:7], v[88:91]\n"
    "v_cvt_pk_bf16_f32 v37, v98, v99\n"
    "v_mfma_f32_16x16x32_bf16 v[92:95], v[166:169], v[28:31], v[140:143]\n"
    "v_exp_f32 v74, v74\n"
    "v_mfma_f32_16x16x32_bf16 v[92:95], v[170:173], v[24:27], v[92:95]\n"
    "ds_read_b128 v[166:169], v254 offset:16384\n"
    "ds_read_b128 v[170:173], v251 offset:16384\n"
    "v_mfma_f32_16x16x32_bf16 v[92:95], v[186:189], v[20:23], v[92:95]\n"
    "v_add_f32 v146, v146, v74\n"
    "v_cvt_pk_bf16_f32 v44, v100, v101\n"
    "v_mfma_f32_16x16x32_bf16 v[92:95], v[182:185], v[16:19], v[92:95]\n"
    "v_exp_f32 v75, v75\n"
    "v_mfma_f32_16x16x32_bf16 v[92:95], v[178:181], v[12:15], v[92:95]\n"
    "v_add_f32 v146, v146, v75\n"
    "v_cvt_pk_bf16_f32 v45, v102, v103\n"
    "v_mfma_f32_16x16x32_bf16 v[92:95], v[174:177], v[8:11], v[92:95]\n"
    "ds_read_b128 v[186:189], v254 offset:18432\n"
    "s_waitcnt lgkmcnt(3)\n"
    "v_mfma_f32_16x16x32_bf16 v[96:99], v[162:165], a[192:195], v[128:131]\n"
    "v_exp_f32 v76, v76\n"
    "v_mfma_f32_16x16x32_bf16 v[96:99], v[158:161], a[196:199], v[96:99]\n"
    "ds_read_b128 v[182:185], v251 offset:18432\n"
    "s_waitcnt vmcnt(10)\n"
    "v_mfma_f32_16x16x32_bf16 v[96:99], v[154:157], a[200:203], v[96:99]\n"
    "ds_read_b128 v[178:181], v254 offset:20480\n"
    "ds_read_b128 v[174:177], v251 offset:20480\n"
    "v_mfma_f32_16x16x32_bf16 v[96:99], v[150:153], a[204:207], v[96:99]\n"
    "v_exp_f32 v77, v77\n"
    "s_waitcnt lgkmcnt(4)\n"
    "v_mfma_f32_16x16x32_bf16 v[96:99], v[166:169], a[208:211], v[96:99]\n"
    "buffer_store_dwordx2 v[252:253], off, s[64:67], s86 sc1\n"
    "v_mfma_f32_16x16x32_bf16 v[96:99], v[170:173], a[212:215], v[96:99]\n"
    "s_add_u32 s86, s86, 256\n"
    "v_add_f32 v147, v147, v76\n"
    "v_mfma_f32_16x16x32_bf16 v[100:103], v[170:173], a[236:239], v[132:135]\n"
    "v_exp_f32 v78, v78\n"
    "v_mfma_f32_16x16x32_bf16 v[100:103], v[166:169], a[232:235], v[100:103]\n"
    "v_add_f32 v147, v147, v77\n"
    "v_add_f32 v147, v147, v78\n"
    "v_mfma_f32_16x16x32_bf16 v[100:103], v[150:153], a[228:231], v[100:103]\n"
    "v_cvt_pk_bf16_f32 v52, v104, v105\n"
    "v_cvt_pk_bf16_f32 v53, v106, v107\n"
    "v_mfma_f32_16x16x32_bf16 v[100:103], v[154:157], a[224:227], v[100:103]\n"
    "v_exp_f32 v79, v79\n"
    "v_mfma_f32_16x16x32_bf16 v[100:103], v[158:161], a[220:223], v[100:103]\n"
    "v_add_f32 v147, v147, v79\n"
    "v_cvt_pk_bf16_f32 v60, v108, v109\n"
    "v_mfma_f32_16x16x32_bf16 v[100:103], v[162:165], a[216:219], v[100:103]\n"
    "v_cvt_pk_bf16_f32 v61, v110, v111\n"
    "v_mfma_f32_16x16x32_bf16 v[104:107], v[162:165], a[240:243], v[136:139]\n"
    "v_exp_f32 v80, v80\n"
    "v_mfma_f32_16x16x32_bf16 v[104:107], v[158:161], a[244:247], v[104:107]\n"
    "v_add_f32 v144, v144, v80\n"
    "v_mfma_f32_16x16x32_bf16 v[104:107], v[154:157], a[248:251], v[104:107]\n"
    "v_cvt_pk_bf16_f32 v38, v112, v113\n"
    "v_mfma_f32_16x16x32_bf16 v[104:107], v[150:153], a[252:255], v[104:107]\n"
    "v_exp_f32 v81, v81\n"
    "v_mfma_f32_16x16x32_bf16 v[104:107], v[166:169], v[0:3], v[104:107]\n"
    "v_add_f32 v144, v144, v81\n"
    "v_cvt_pk_bf16_f32 v46, v116, v117\n"
    "v_mfma_f32_16x16x32_bf16 v[104:107], v[170:173], v[4:7], v[104:107]\n"
    "s_mov_b32 m0, s74\n"
    "v_cvt_pk_bf16_f32 v39, v114, v115\n"
    "v_mfma_f32_16x16x32_bf16 v[108:111], v[170:173], v[28:31], v[140:143]\n"
    "v_exp_f32 v82, v82\n"
    "v_mfma_f32_16x16x32_bf16 v[108:111], v[166:169], v[24:27], v[108:111]\n"
    "ds_read_b128 v[170:173], v254 offset:22528\n"
    "ds_read_b128 v[166:169], v251 offset:22528\n"
    "v_mfma_f32_16x16x32_bf16 v[108:111], v[150:153], v[20:23], v[108:111]\n"
    "s_add_u32 s74, s74, s73\n"
    "v_add_f32 v144, v144, v82\n"
    "v_mfma_f32_16x16x32_bf16 v[108:111], v[154:157], v[16:19], v[108:111]\n"
    "v_exp_f32 v83, v83\n"
    "v_mfma_f32_16x16x32_bf16 v[108:111], v[158:161], v[12:15], v[108:111]\n"
    "s_cmp_ge_u32 s74, s75\n"
    "s_cselect_b32 s73, 0xfffe8000, s76\n"
    "v_mfma_f32_16x16x32_bf16 v[108:111], v[162:165], v[8:11], v[108:111]\n"
    "ds_read_b128 v[150:153], v148 offset:24576\n"
    "s_waitcnt lgkmcnt(3)\n"
    "v_mfma_f32_16x16x32_bf16 v[112:115], v[186:189], a[192:195], v[128:131]\n"
    "v_exp_f32 v84, v84\n"
    "v_mfma_f32_16x16x32_bf16 v[112:115], v[182:185], a[196:199], v[112:115]\n"
    "ds_read_b128 v[154:157], v148 offset:36864\n"
    "v_add_f32 v144, v144, v83\n"
    "v_mfma_f32_16x16x32_bf16 v[112:115], v[178:181], a[200:203], v[112:115]\n"
    "buffer_load_dwordx4 v149, s[36:39], s72 offen offset:0 sc1 lds\n"
    "v_mfma_f32_16x16x32_bf16 v[112:115], v[174:177], a[204:207], v[112:115]\n"
    "v_exp_f32 v85, v85\n"
    "s_waitcnt lgkmcnt(2)\n"
    "v_mfma_f32_16x16x32_bf16 v[112:115], v[170:173], a[208:211], v[112:115]\n"
    "v_add_f32 v145, v145, v84\n"
    "v_add_f32 v145, v145, v85\n"
    "v_mfma_f32_16x16x32_bf16 v[112:115], v[166:169], a[212:215], v[112:115]\n"
    "v_cvt_pk_bf16_f32 v47, v118, v119\n"
    "v_cvt_pk_bf16_f32 v54, v120, v121\n"
    "v_mfma_f32_16x16x32_bf16 v[116:119], v[166:169], a[236:239], v[132:135]\n"
    "v_exp_f32 v86, v86\n"
    "v_mfma_f32_16x16x32_bf16 v[116:119], v[170:173], a[232:235], v[116:119]\n"
    "buffer_load_dwordx4 v149, s[36:39], s72 offen offset:1024 sc1 lds\n"
    "v_mfma_f32_16x16x32_bf16 v[116:119], v[174:177], a[228:231], v[116:119]\n"
    "ds_read_b128 v[158:161], v148 offset:25600\n"
    "ds_read_b128 v[162:165], v148 offset:37888\n"
    "v_mfma_f32_16x16x32_bf16 v[116:119], v[178:181], a[224:227], v[116:119]\n"
    "v_exp_f32 v87, v87\n"
    "v_mfma_f32_16x16x32_bf16 v[116:119], v[182:185], a[220:223], v[116:119]\n"
    "v_add_f32 v145, v145, v86\n"
    "v_add_f32 v145, v145, v87\n"
    "v_mfma_f32_16x16x32_bf16 v[116:119], v[186:189], a[216:219], v[116:119]\n"
    "v_cvt_pk_bf16_f32 v55, v122, v123\n"
    "v_cvt_pk_bf16_f32 v62, v124, v125\n"
    "v_mfma_f32_16x16x32_bf16 v[120:123], v[186:189], a[240:243], v[136:139]\n"
    "v_exp_f32 v88, v88\n"
    "v_mfma_f32_16x16x32_bf16 v[120:123], v[182:185], a[244:247], v[120:123]\n"
    "buffer_load_dwordx4 v149, s[36:39], s72 offen offset:2048 sc1 lds\n"
    "v_mfma_f32_16x16x32_bf16 v[120:123], v[178:181], a[248:251], v[120:123]\n"
    "v_add_f32 v146, v146, v88\n"
    "v_cvt_pk_bf16_f32 v63, v126, v127\n"
    "v_mfma_f32_16x16x32_bf16 v[120:123], v[174:177], a[252:255], v[120:123]\n"
    "v_exp_f32 v89, v89\n"
    "v_mfma_f32_16x16x32_bf16 v[120:123], v[170:173], v[0:3], v[120:123]\n"
    "v_add_f32 v146, v146, v89\n"
    "v_mfma_f32_16x16x32_bf16 v[120:123], v[166:169], v[4:7], v[120:123]\n"
    "v_mfma_f32_16x16x32_bf16 v[124:127], v[166:169], v[28:31], v[140:143]\n"
    "v_exp_f32 v90, v90\n"
    "v_mfma_f32_16x16x32_bf16 v[124:127], v[170:173], v[24:27], v[124:127]\n"
    "ds_read_b128 v[166:169], v148 offset:26624\n"
    "ds_read_b128 v[170:173], v148 offset:38912\n"
    "v_mfma_f32_16x16x32_bf16 v[124:127], v[174:177], v[20:23], v[124:127]\n"
    "buffer_load_dwordx4 v149, s[36:39], s72 offen offset:3072 sc1 lds\n"
    "v_mfma_f32_16x16x32_bf16 v[124:127], v[178:181], v[16:19], v[124:127]\n"
    "v_exp_f32 v91, v91\n"
    "v_mfma_f32_16x16x32_bf16 v[124:127], v[182:185], v[12:15], v[124:127]\n"
    "s_add_u32 s72, s72, 4096\n"
    "v_add_f32 v146, v146, v90\n"
    "v_mfma_f32_16x16x32_bf16 v[124:127], v[186:189], v[8:11], v[124:127]\n"
    "ds_read_b128 v[174:177], v148 offset:27648\n"
    "s_waitcnt lgkmcnt(3)\n"
    "v_mfma_f32_16x16x32_bf16 a[0:3], v[32:35], v[150:153], a[0:3]\n"
    "v_exp_f32 v92, v92\n"
    "v_mfma_f32_16x16x32_bf16 a[0:3], v[36:39], v[154:157], a[0:3]\n"
    "ds_read_b128 v[178:181], v148 offset:39936\n"
    "s_waitcnt vmcnt(13)\n"
    "v_mfma_f32_16x16x32_bf16 a[4:7], v[44:47], v[154:157], a[4:7]\n"
    "s_add_u32 m0, m0, 4096\n"
    "v_add_f32 v146, v146, v91\n"
    "v_mfma_f32_16x16x32_bf16 a[4:7], v[40:43], v[150:153], a[4:7]\n"
    "v_exp_f32 v93, v93\n"
    "v_mfma_f32_16x16x32_bf16 a[8:11], v[48:51], v[150:153], a[8:11]\n"
    "v_add_f32 v147, v147, v92\n"
    "v_add_f32 v147, v147, v93\n"
    "v_mfma_f32_16x16x32_bf16 a[8:11], v[52:55], v[154:157], a[8:11]\n"
    "v_lshlrev_b32 v230, 16, v210\n"
    "v_and_b32 v231, s78, v210\n"
    "v_mfma_f32_16x16x32_bf16 a[12:15], v[60:63], v[154:157], a[12:15]\n"
    "v_exp_f32 v94, v94\n"
    "v_mfma_f32_16x16x32_bf16 a[12:15], v[56:59], v[150:153], a[12:15]\n"
    "buffer_load_dwordx4 v149, s[36:39], s72 offen offset:0 sc1 lds\n"
    "v_mfma_f32_16x16x32_bf16 a[16:19], v[32:35], v[158:161], a[16:19]\n"
    "ds_read_b128 v[182:185], v148 offset:28672\n"
    "ds_read_b128 v[186:189], v148 offset:40960\n"
    "v_mfma_f32_16x16x32_bf16 a[16:19], v[36:39], v[162:165], a[16:19]\n"
    "v_exp_f32 v95, v95\n"
    "v_mfma_f32_16x16x32_bf16 a[20:23], v[44:47], v[162:165], a[20:23]\n"
    "v_add_f32 v147, v147, v94\n"
    "v_add_f32 v147, v147, v95\n"
    "v_mfma_f32_16x16x32_bf16 a[20:23], v[40:43], v[158:161], a[20:23]\n"
    "v_lshlrev_b32 v232, 16, v211\n"
    "v_and_b32 v233, s78, v211\n"
    "v_mfma_f32_16x16x32_bf16 a[24:27], v[48:51], v[158:161], a[24:27]\n"
    "v_exp_f32 v96, v96\n"
    "v_mfma_f32_16x16x32_bf16 a[24:27], v[52:55], v[162:165], a[24:27]\n"
    "buffer_load_dwordx4 v149, s[36:39], s72 offen offset:1024 sc1 lds\n"
    "v_mfma_f32_16x16x32_bf16 a[28:31], v[60:63], v[162:165], a[28:31]\n"
    "v_add_f32 v144, v144, v96\n"
    "v_lshlrev_b32 v234, 16, v212\n"
    "v_mfma_f32_16x16x32_bf16 a[28:31], v[56:59], v[158:161], a[28:31]\n"
    "v_exp_f32 v97, v97\n"
    "s_waitcnt lgkmcnt(0)\n"
    "v_mfma_f32_16x16x32_bf16 a[32:35], v[32:35], v[166:169], a[32:35]\n"
    "ds_read_b128 v[150:153], v148 offset:29696\n"
    "v_add_f32 v144, v144, v97\n"
    "v_mfma_f32_16x16x32_bf16 a[32:35], v[36:39], v[170:173], a[32:35]\n"
    "ds_read_b128 v[154:157], v148 offset:41984\n"
    "v_and_b32 v235, s78, v212\n"
    "v_mfma_f32_16x16x32_bf16 a[36:39], v[44:47], v[170:173], a[36:39]\n"
    "v_exp_f32 v98, v98\n"
    "v_mfma_f32_16x16x32_bf16 a[36:39], v[40:43], v[166:169], a[36:39]\n"
    "buffer_load_dwordx4 v149, s[36:39], s72 offen offset:2048 sc1 lds\n"
    "v_mfma_f32_16x16x32_bf16 a[40:43], v[48:51], v[166:169], a[40:43]\n"
    "v_add_f32 v144, v144, v98\n"
    "v_lshlrev_b32 v236, 16, v213\n"
    "v_mfma_f32_16x16x32_bf16 a[40:43], v[52:55], v[170:173], a[40:43]\n"
    "v_exp_f32 v99, v99\n"
    "v_mfma_f32_16x16x32_bf16 a[44:47], v[60:63], v[170:173], a[44:47]\n"
    "v_add_f32 v144, v144, v99\n"
    "v_and_b32 v237, s78, v213\n"
    "v_mfma_f32_16x16x32_bf16 a[44:47], v[56:59], v[166:169], a[44:47]\n"
    "ds_read_b128 v[158:161], v148 offset:30720\n"
    "v_mfma_f32_16x16x32_bf16 a[48:51], v[32:35], v[174:177], a[48:51]\n"
    "v_exp_f32 v100, v100\n"
    "v_mfma_f32_16x16x32_bf16 a[48:51], v[36:39], v[178:181], a[48:51]\n"
    "ds_read_b128 v[162:165], v148 offset:43008\n"
    "v_add_f32 v145, v145, v100\n"
    "v_mfma_f32_16x16x32_bf16 a[52:55], v[44:47], v[178:181], a[52:55]\n"
    "buffer_load_dwordx4 v149, s[36:39], s72 offen offset:3072 sc1 lds\n"
    "v_mfma_f32_16x16x32_bf16 a[52:55], v[40:43], v[174:177], a[52:55]\n"
    "v_exp_f32 v101, v101\n"
    "v_mfma_f32_16x16x32_bf16 a[56:59], v[48:51], v[174:177], a[56:59]\n"
    "s_add_u32 s72, s72, 4096\n"
    "v_add_f32 v145, v145, v101\n"
    "v_mfma_f32_16x16x32_bf16 a[56:59], v[52:55], v[178:181], a[56:59]\n"
    "s_add_u32 m0, m0, 4096\n"
    "v_and_b32 v210, s78, v206\n"
    "v_mfma_f32_16x16x32_bf16 a[60:63], v[60:63], v[178:181], a[60:63]\n"
    "v_exp_f32 v102, v102\n"
    "v_mfma_f32_16x16x32_bf16 a[60:63], v[56:59], v[174:177], a[60:63]\n"
    "v_add_f32 v145, v145, v102\n"
    "v_lshlrev_b32 v206, 16, v206\n"
    "v_mfma_f32_16x16x32_bf16 a[64:67], v[32:35], v[182:185], a[64:67]\n"
    "ds_read_b128 v[166:169], v148 offset:31744\n"
    "ds_read_b128 v[170:173], v148 offset:44032\n"
    "v_mfma_f32_16x16x32_bf16 a[64:67], v[36:39], v[186:189], a[64:67]\n"
    "v_exp_f32 v103, v103\n"
    "v_mfma_f32_16x16x32_bf16 a[68:71], v[44:47], v[186:189], a[68:71]\n"
    "v_add_f32 v145, v145, v103\n"
    "v_and_b32 v211, s78, v207\n"
    "v_mfma_f32_16x16x32_bf16 a[68:71], v[40:43], v[182:185], a[68:71]\n"
    "buffer_load_dwordx4 v149, s[36:39], s72 offen offset:0 sc1 lds\n"
    "v_mfma_f32_16x16x32_bf16 a[72:75], v[48:51], v[182:185], a[72:75]\n"
    "v_exp_f32 v104, v104\n"
    "v_mfma_f32_16x16x32_bf16 a[72:75], v[52:55], v[186:189], a[72:75]\n"
    "v_add_f32 v146, v146, v104\n"
    "v_lshlrev_b32 v207, 16, v207\n"
    "v_mfma_f32_16x16x32_bf16 a[76:79], v[60:63], v[186:189], a[76:79]\n"
    "v_and_b32 v212, s78, v208\n"
    "v_lshlrev_b32 v208, 16, v208\n"
    "v_mfma_f32_16x16x32_bf16 a[76:79], v[56:59], v[182:185], a[76:79]\n"
    "v_exp_f32 v105, v105\n"
    "s_waitcnt lgkmcnt(0)\n"
    "v_mfma_f32_16x16x32_bf16 a[80:83], v[32:35], v[150:153], a[80:83]\n"
    "v_add_f32 v146, v146, v105\n"
    "v_and_b32 v213, s78, v209\n"
    "v_mfma_f32_16x16x32_bf16 a[80:83], v[36:39], v[154:157], a[80:83]\n"
    "buffer_load_dwordx4 v149, s[36:39], s72 offen offset:1024 sc1 lds\n"
    "v_mfma_f32_16x16x32_bf16 a[84:87], v[44:47], v[154:157], a[84:87]\n"
    "v_exp_f32 v106, v106\n"
    "v_mfma_f32_16x16x32_bf16 a[84:87], v[40:43], v[150:153], a[84:87]\n"
    "v_add_f32 v146, v146, v106\n"
    "v_lshlrev_b32 v209, 16, v209\n"
    "v_mfma_f32_16x16x32_bf16 a[88:91], v[48:51], v[150:153], a[88:91]\n"
    "v_mul_f32 v206, v206, v230\n"
    "v_mfma_f32_16x16x32_bf16 a[88:91], v[52:55], v[154:157], a[88:91]\n"
    "v_exp_f32 v107, v107\n"
    "v_mfma_f32_16x16x32_bf16 a[92:95], v[60:63], v[154:157], a[92:95]\n"
    "buffer_load_dwordx4 v149, s[36:39], s72 offen offset:2048 sc1 lds\n"
    "v_mfma_f32_16x16x32_bf16 a[92:95], v[56:59], v[150:153], a[92:95]\n"
    "ds_read_b128 v[150:153], v148 offset:32768\n"
    "ds_read_b128 v[154:157], v148 offset:45056\n"
    "v_mfma_f32_16x16x32_bf16 a[96:99], v[32:35], v[158:161], a[96:99]\n"
    "v_exp_f32 v108, v108\n"
    "v_mfma_f32_16x16x32_bf16 a[96:99], v[36:39], v[162:165], a[96:99]\n"
    "v_add_f32 v146, v146, v107\n"
    "v_add_f32 v147, v147, v108\n"
    "v_mfma_f32_16x16x32_bf16 a[100:103], v[44:47], v[162:165], a[100:103]\n"
    "v_mul_f32 v210, v210, v231\n"
    "v_mul_f32 v207, v207, v232\n"
    "v_mfma_f32_16x16x32_bf16 a[100:103], v[40:43], v[158:161], a[100:103]\n"
    "v_exp_f32 v109, v109\n"
    "v_mfma_f32_16x16x32_bf16 a[104:107], v[48:51], v[158:161], a[104:107]\n"
    "buffer_load_dwordx4 v149, s[36:39], s72 offen offset:3072 sc1 lds\n"
    "v_mfma_f32_16x16x32_bf16 a[104:107], v[52:55], v[162:165], a[104:107]\n"
    "s_add_u32 s72, s72, 16384\n"
    "v_add_f32 v147, v147, v109\n"
    "v_mfma_f32_16x16x32_bf16 a[108:111], v[60:63], v[162:165], a[108:111]\n"
    "v_exp_f32 v110, v110\n"
    "v_mfma_f32_16x16x32_bf16 a[108:111], v[56:59], v[158:161], a[108:111]\n"
    "ds_read_b128 v[158:161], v148 offset:33792\n"
    "ds_read_b128 v[162:165], v148 offset:46080\n"
    "v_mfma_f32_16x16x32_bf16 a[112:115], v[32:35], v[166:169], a[112:115]\n"
    "ds_read_b128 v[174:177], v148 offset:34816\n"
    "ds_read_b128 v[178:181], v148 offset:47104\n"
    "v_mfma_f32_16x16x32_bf16 a[112:115], v[36:39], v[170:173], a[112:115]\n"
    "v_exp_f32 v111, v111\n"
    "v_mfma_f32_16x16x32_bf16 a[116:119], v[44:47], v[170:173], a[116:119]\n"
    "ds_read_b128 v[182:185], v148 offset:35840\n"
    "v_add_f32 v147, v147, v110\n"
    "v_mfma_f32_16x16x32_bf16 a[116:119], v[40:43], v[166:169], a[116:119]\n"
    "ds_read_b128 v[186:189], v148 offset:48128\n"
    "v_add_f32 v147, v147, v111\n"
    "v_mfma_f32_16x16x32_bf16 a[120:123], v[48:51], v[166:169], a[120:123]\n"
    "v_exp_f32 v112, v112\n"
    "v_mfma_f32_16x16x32_bf16 a[120:123], v[52:55], v[170:173], a[120:123]\n"
    "v_add_f32 v144, v144, v112\n"
    "v_mul_f32 v211, v211, v233\n"
    "v_mfma_f32_16x16x32_bf16 a[124:127], v[60:63], v[170:173], a[124:127]\n"
    "v_mul_f32 v208, v208, v234\n"
    "v_mul_f32 v212, v212, v235\n"
    "v_mfma_f32_16x16x32_bf16 a[124:127], v[56:59], v[166:169], a[124:127]\n"
    "v_exp_f32 v113, v113\n"
    "s_waitcnt lgkmcnt(2)\n"
    "v_mfma_f32_16x16x32_bf16 a[128:131], v[32:35], v[150:153], a[128:131]\n"
    "v_add_f32 v144, v144, v113\n"
    "v_mul_f32 v209, v209, v236\n"
    "v_mfma_f32_16x16x32_bf16 a[128:131], v[36:39], v[154:157], a[128:131]\n"
    "v_mul_f32 v213, v213, v237\n"
    "v_mfma_f32_16x16x32_bf16 a[132:135], v[44:47], v[154:157], a[132:135]\n"
    "v_exp_f32 v114, v114\n"
    "v_mfma_f32_16x16x32_bf16 a[132:135], v[40:43], v[150:153], a[132:135]\n"
    "s_waitcnt vmcnt(13) lgkmcnt(0)\n"
    "s_barrier\n"
    "v_mfma_f32_16x16x32_bf16 a[136:139], v[48:51], v[150:153], a[136:139]\n"
    "v_add_u32 v254, s73, v254\n"
    "v_add_u32 v251, s73, v251\n"
    "v_mfma_f32_16x16x32_bf16 a[136:139], v[52:55], v[154:157], a[136:139]\n"
    "v_exp_f32 v115, v115\n"
    "v_mfma_f32_16x16x32_bf16 a[140:143], v[60:63], v[154:157], a[140:143]\n"
    "buffer_load_dwordx2 v[252:253], v250, s[64:67], s87 offen sc1\n"
    "v_mfma_f32_16x16x32_bf16 a[140:143], v[56:59], v[150:153], a[140:143]\n"
    "ds_read_b128 v[150:153], v254 offset:0\n"
    "v_add_f32 v144, v144, v114\n"
    "v_mfma_f32_16x16x32_bf16 a[144:147], v[32:35], v[158:161], a[144:147]\n"
    "v_exp_f32 v116, v116\n"
    "v_mfma_f32_16x16x32_bf16 a[144:147], v[36:39], v[162:165], a[144:147]\n"
    "ds_read_b128 v[154:157], v251 offset:0\n"
    "v_add_f32 v144, v144, v115\n"
    "v_mfma_f32_16x16x32_bf16 a[148:151], v[44:47], v[162:165], a[148:151]\n"
    "buffer_load_dwordx2 v[214:215], v248, s[40:43], s82 offen offset:128 nt\n"
    "v_mfma_f32_16x16x32_bf16 a[148:151], v[40:43], v[158:161], a[148:151]\n"
    "v_exp_f32 v117, v117\n"
    "v_mfma_f32_16x16x32_bf16 a[152:155], v[48:51], v[158:161], a[152:155]\n"
    "buffer_load_dwordx2 v[216:217], v248, s[40:43], s82 offen offset:896 nt\n"
    "v_mfma_f32_16x16x32_bf16 a[152:155], v[52:55], v[162:165], a[152:155]\n"
    "buffer_load_dwordx2 v[218:219], v248, s[40:43], s82 offen offset:1664 nt\n"
    "v_mfma_f32_16x16x32_bf16 a[156:159], v[60:63], v[162:165], a[156:159]\n"
    "v_exp_f32 v118, v118\n"
    "v_mfma_f32_16x16x32_bf16 a[156:159], v[56:59], v[158:161], a[156:159]\n"
    "ds_read_b128 v[158:161], v254 offset:2048\n"
    "ds_read_b128 v[162:165], v251 offset:2048\n"
    "v_mfma_f32_16x16x32_bf16 a[160:163], v[32:35], v[174:177], a[160:163]\n"
    "v_add_u32 v148, s73, v148\n"
    "ds_read_b128 v[166:169], v254 offset:4096\n"
    "v_mfma_f32_16x16x32_bf16 a[160:163], v[36:39], v[178:181], a[160:163]\n"
    "v_exp_f32 v119, v119\n"
    "v_mfma_f32_16x16x32_bf16 a[164:167], v[44:47], v[178:181], a[164:167]\n"
    "ds_read_b128 v[170:173], v251 offset:4096\n"
    "v_add_f32 v145, v145, v116\n"
    "v_mfma_f32_16x16x32_bf16 a[164:167], v[40:43], v[174:177], a[164:167]\n"
    "buffer_load_dwordx2 v[220:221], v248, s[40:43], s82 offen offset:2432 nt\n"
    "v_mfma_f32_16x16x32_bf16 a[168:171], v[48:51], v[174:177], a[168:171]\n"
    "v_exp_f32 v120, v120\n"
    "v_mfma_f32_16x16x32_bf16 a[168:171], v[52:55], v[178:181], a[168:171]\n"
    "v_add_f32 v145, v145, v117\n"
    "v_add_f32 v145, v145, v118\n"
    "v_mfma_f32_16x16x32_bf16 a[172:175], v[60:63], v[178:181], a[172:175]\n"
    "v_add_f32 v145, v145, v119\n"
    "v_add_f32 v146, v146, v120\n"
    "v_mfma_f32_16x16x32_bf16 a[172:175], v[56:59], v[174:177], a[172:175]\n"
    "v_exp_f32 v121, v121\n"
    "v_mfma_f32_16x16x32_bf16 a[176:179], v[32:35], v[182:185], a[176:179]\n"
    "buffer_load_dwordx2 v[222:223], v248, s[40:43], s83 offen offset:128 nt\n"
    "v_mfma_f32_16x16x32_bf16 a[176:179], v[36:39], v[186:189], a[176:179]\n"
    "v_cvt_pk_bf16_f32 v32, v64, v65\n"
    "v_cvt_pk_bf16_f32 v33, v66, v67\n"
    "v_mfma_f32_16x16x32_bf16 a[180:183], v[44:47], v[186:189], a[180:183]\n"
    "v_exp_f32 v122, v122\n"
    "v_mfma_f32_16x16x32_bf16 a[180:183], v[40:43], v[182:185], a[180:183]\n"
    "v_cvt_pk_bf16_f32 v40, v68, v69\n"
    "v_add_f32 v146, v146, v121\n"
    "v_add_f32 v146, v146, v122\n"
    "v_mfma_f32_16x16x32_bf16 a[184:187], v[48:51], v[182:185], a[184:187]\n"
    "buffer_load_dwordx2 v[224:225], v248, s[40:43], s83 offen offset:896 nt\n"
    "v_mfma_f32_16x16x32_bf16 a[184:187], v[52:55], v[186:189], a[184:187]\n"
    "v_exp_f32 v123, v123\n"
    "v_mfma_f32_16x16x32_bf16 a[188:191], v[60:63], v[186:189], a[188:191]\n"
    "buffer_load_dwordx2 v[226:227], v248, s[40:43], s83 offen offset:1664 nt\n"
    "v_add_f32 v146, v146, v123\n"
    "v_mfma_f32_16x16x32_bf16 a[188:191], v[56:59], v[182:185], a[188:191]\n"
    "ds_read_b128 v[174:177], v254 offset:6144\n"
    "s_waitcnt lgkmcnt(1)\n"
    "v_mfma_f32_16x16x32_bf16 v[64:67], v[150:153], a[192:195], v[128:131]\n"
    "v_exp_f32 v124, v124\n"
    "v_mfma_f32_16x16x32_bf16 v[64:67], v[154:157], a[196:199], v[64:67]\n"
    "ds_read_b128 v[178:181], v251 offset:6144\n"
    "v_cvt_pk_bf16_f32 v41, v70, v71\n"
    "v_mfma_f32_16x16x32_bf16 v[64:67], v[158:161], a[200:203], v[64:67]\n"
    "ds_read_b128 v[182:185], v254 offset:8192\n"
    "ds_read_b128 v[186:189], v251 offset:8192\n"
    "v_mfma_f32_16x16x32_bf16 v[64:67], v[162:165], a[204:207], v[64:67]\n"
    "v_exp_f32 v125, v125\n"
    "v_mfma_f32_16x16x32_bf16 v[64:67], v[166:169], a[208:211], v[64:67]\n"
    "v_cvt_pk_bf16_f32 v48, v72, v73\n"
    "v_add_f32 v147, v147, v124\n"
    "v_mfma_f32_16x16x32_bf16 v[64:67], v[170:173], a[212:215], v[64:67]\n"
    "buffer_load_dwordx2 v[228:229], v248, s[40:43], s83 offen offset:2432 nt\n"
    "v_mfma_f32_16x16x32_bf16 v[68:71], v[170:173], a[236:239], v[132:135]\n"
    "v_exp_f32 v126, v126\n"
    "v_mfma_f32_16x16x32_bf16 v[68:71], v[166:169], a[232:235], v[68:71]\n"
    "v_cvt_pk_bf16_f32 v49, v74, v75\n"
    "v_add_f32 v147, v147, v125\n"
    "v_mfma_f32_16x16x32_bf16 v[68:71], v[162:165], a[228:231], v[68:71]\n"
    "v_cvt_pk_bf16_f32 v56, v76, v77\n"
    "v_add_f32 v147, v147, v126\n"
    "v_mfma_f32_16x16x32_bf16 v[68:71], v[158:161], a[224:227], v[68:71]\n"
    "v_exp_f32 v127, v127\n"
    "v_mfma_f32_16x16x32_bf16 v[68:71], v[154:157], a[220:223], v[68:71]\n"
    "v_add_f32 v147, v147, v127\n"
    "v_cvt_f32_i32 v190, v190\n"
    "v_mfma_f32_16x16x32_bf16 v[68:71], v[150:153], a[216:219], v[68:71]\n"
    "v_cvt_pk_bf16_f32 v57, v78, v79\n"
    "v_cvt_f32_i32 v192, v192\n"
    "v_mfma_f32_16x16x32_bf16 v[72:75], v[150:153], a[240:243], v[136:139]\n"
    "v_exp_f32 v64, v64\n"
    "v_mfma_f32_16x16x32_bf16 v[72:75], v[154:157], a[244:247], v[72:75]\n"
    "v_add_f32 v144, v144, v64\n"
    "v_cvt_f32_i32 v191, v191\n"
    "v_mfma_f32_16x16x32_bf16 v[72:75], v[158:161], a[248:251], v[72:75]\n"
    "v_cvt_pk_bf16_f32 v34, v80, v81\n"
    "v_cvt_f32_i32 v193, v193\n"
    "v_mfma_f32_16x16x32_bf16 v[72:75], v[162:165], a[252:255], v[72:75]\n"
    "v_exp_f32 v65, v65\n"
    "v_mfma_f32_16x16x32_bf16 v[72:75], v[166:169], v[0:3], v[72:75]\n"
    "v_add_f32 v144, v144, v65\n"
    "v_fma_f32 v190, v190, v230, -v206\n"
    "v_mfma_f32_16x16x32_bf16 v[72:75], v[170:173], v[4:7], v[72:75]\n"
    "v_cvt_pk_bf16_f32 v35, v82, v83\n"
    "v_fma_f32 v192, v192, v231, -v210\n"
    "v_mfma_f32_16x16x32_bf16 v[76:79], v[170:173], v[28:31], v[140:143]\n"
    "v_exp_f32 v66, v66\n"
    "v_mfma_f32_16x16x32_bf16 v[76:79], v[166:169], v[24:27], v[76:79]\n"
    "ds_read_b128 v[170:173], v254 offset:10240\n"
    "ds_read_b128 v[166:169], v251 offset:10240\n"
    "v_mfma_f32_16x16x32_bf16 v[76:79], v[162:165], v[20:23], v[76:79]\n"
    "v_cvt_pk_bf16_f32 v42, v84, v85\n"
    "v_add_f32 v144, v144, v66\n"
    "v_mfma_f32_16x16x32_bf16 v[76:79], v[158:161], v[16:19], v[76:79]\n"
    "v_exp_f32 v67, v67\n"
    "v_mfma_f32_16x16x32_bf16 v[76:79], v[154:157], v[12:15], v[76:79]\n"
    "v_add_f32 v144, v144, v67\n"
    "v_fma_f32 v191, v191, v230, -v206\n"
    "v_mfma_f32_16x16x32_bf16 v[76:79], v[150:153], v[8:11], v[76:79]\n"
    "ds_read_b128 v[162:165], v254 offset:12288\n"
    "s_waitcnt lgkmcnt(3)\n"
    "v_mfma_f32_16x16x32_bf16 v[80:83], v[174:177], a[192:195], v[128:131]\n"
    "v_exp_f32 v68, v68\n"
    "v_mfma_f32_16x16x32_bf16 v[80:83], v[178:181], a[196:199], v[80:83]\n"
    "ds_read_b128 v[158:161], v251 offset:12288\n"
    "v_cvt_pk_bf16_f32 v43, v86, v87\n"
    "v_mfma_f32_16x16x32_bf16 v[80:83], v[182:185], a[200:203], v[80:83]\n"
    "ds_read_b128 v[154:157], v254 offset:14336\n"
    "ds_read_b128 v[150:153], v251 offset:14336\n"
    "v_mfma_f32_16x16x32_bf16 v[80:83], v[186:189], a[204:207], v[80:83]\n"
    "v_exp_f32 v69, v69\n"
    "s_waitcnt lgkmcnt(4)\n"
    "v_mfma_f32_16x16x32_bf16 v[80:83], v[170:173], a[208:211], v[80:83]\n"
    "v_cvt_pk_bf16_f32 v50, v88, v89\n"
    "v_add_f32 v145, v145, v68\n"
    "v_mfma_f32_16x16x32_bf16 v[80:83], v[166:169], a[212:215], v[80:83]\n"
    "v_cvt_pk_bf16_f32 v51, v90, v91\n"
    "v_add_f32 v145, v145, v69\n"
    "v_mfma_f32_16x16x32_bf16 v[84:87], v[166:169], a[236:239], v[132:135]\n"
    "v_exp_f32 v70, v70\n"
    "v_mfma_f32_16x16x32_bf16 v[84:87], v[170:173], a[232:235], v[84:87]\n"
    "v_add_f32 v145, v145, v70\n"
    "v_fma_f32 v193, v193, v231, -v210\n"
    "v_mfma_f32_16x16x32_bf16 v[84:87], v[186:189], a[228:231], v[84:87]\n"
    "v_cvt_pk_bf16_f32 v58, v92, v93\n"
    "v_cvt_pk_bf16_f32 v238, v190, v192\n"
    "v_mfma_f32_16x16x32_bf16 v[84:87], v[182:185], a[224:227], v[84:87]\n"
    "v_exp_f32 v71, v71\n"
    "v_mfma_f32_16x16x32_bf16 v[84:87], v[178:181], a[220:223], v[84:87]\n"
    "v_add_f32 v145, v145, v71\n"
    "v_cvt_pk_bf16_f32 v242, v191, v193\n"
    "v_mfma_f32_16x16x32_bf16 v[84:87], v[174:177], a[216:219], v[84:87]\n"
    "v_cvt_pk_bf16_f32 v59, v94, v95\n"
    "v_cvt_f32_i32 v194, v194\n"
    "v_mfma_f32_16x16x32_bf16 v[88:91], v[174:177], a[240:243], v[136:139]\n"
    "v_exp_f32 v72, v72\n"
    "v_mfma_f32_16x16x32_bf16 v[88:91], v[178:181], a[244:247], v[88:91]\n"
    "v_add_f32 v146, v146, v72\n"
    "v_cvt_f32_i32 v196, v196\n"
    "v_mfma_f32_16x16x32_bf16 v[88:91], v[182:185], a[248:251], v[88:91]\n"
    "v_cvt_pk_bf16_f32 v36, v96, v97\n"
    "v_cvt_f32_i32 v195, v195\n"
    "v_mfma_f32_16x16x32_bf16 v[88:91], v[186:189], a[252:255], v[88:91]\n"
    "v_exp_f32 v73, v73\n"
    "v_mfma_f32_16x16x32_bf16 v[88:91], v[170:173], v[0:3], v[88:91]\n"
    "v_add_f32 v146, v146, v73\n"
    "v_cvt_f32_i32 v197, v197\n"
    "v_mfma_f32_16x16x32_bf16 v[88:91], v[166:169], v[4:7], v[88:91]\n"
    "v_cvt_pk_bf16_f32 v37, v98, v99\n"
    "v_mfma_f32_16x16x32_bf16 v[92:95], v[166:169], v[28:31], v[140:143]\n"
    "v_exp_f32 v74, v74\n"
    "v_mfma_f32_16x16x32_bf16 v[92:95], v[170:173], v[24:27], v[92:95]\n"
    "ds_read_b128 v[166:169], v254 offset:16384\n"
    "ds_read_b128 v[170:173], v251 offset:16384\n"
    "v_mfma_f32_16x16x32_bf16 v[92:95], v[186:189], v[20:23], v[92:95]\n"
    "v_cvt_pk_bf16_f32 v44, v100, v101\n"
    "v_add_f32 v146, v146, v74\n"
    "v_mfma_f32_16x16x32_bf16 v[92:95], v[182:185], v[16:19], v[92:95]\n"
    "v_exp_f32 v75, v75\n"
    "v_mfma_f32_16x16x32_bf16 v[92:95], v[178:181], v[12:15], v[92:95]\n"
    "v_add_f32 v146, v146, v75\n"
    "v_fma_f32 v194, v194, v232, -v207\n"
    "v_mfma_f32_16x16x32_bf16 v[92:95], v[174:177], v[8:11], v[92:95]\n"
    "ds_read_b128 v[186:189], v254 offset:18432\n"
    "s_waitcnt lgkmcnt(3)\n"
    "v_mfma_f32_16x16x32_bf16 v[96:99], v[162:165], a[192:195], v[128:131]\n"
    "v_exp_f32 v76, v76\n"
    "v_mfma_f32_16x16x32_bf16 v[96:99], v[158:161], a[196:199], v[96:99]\n"
    "ds_read_b128 v[182:185], v251 offset:18432\n"
    "v_cvt_pk_bf16_f32 v45, v102, v103\n"
    "v_mfma_f32_16x16x32_bf16 v[96:99], v[154:157], a[200:203], v[96:99]\n"
    "ds_read_b128 v[178:181], v254 offset:20480\n"
    "ds_read_b128 v[174:177], v251 offset:20480\n"
    "v_mfma_f32_16x16x32_bf16 v[96:99], v[150:153], a[204:207], v[96:99]\n"
    "v_exp_f32 v77, v77\n"
    "s_waitcnt lgkmcnt(4)\n"
    "v_mfma_f32_16x16x32_bf16 v[96:99], v[166:169], a[208:211], v[96:99]\n"
    "v_cvt_pk_bf16_f32 v52, v104, v105\n"
    "v_add_f32 v147, v147, v76\n"
    "v_mfma_f32_16x16x32_bf16 v[96:99], v[170:173], a[212:215], v[96:99]\n"
    "v_cvt_pk_bf16_f32 v53, v106, v107\n"
    "v_add_f32 v147, v147, v77\n"
    "v_mfma_f32_16x16x32_bf16 v[100:103], v[170:173], a[236:239], v[132:135]\n"
    "v_exp_f32 v78, v78\n"
    "v_mfma_f32_16x16x32_bf16 v[100:103], v[166:169], a[232:235], v[100:103]\n"
    "v_add_f32 v147, v147, v78\n"
    "v_fma_f32 v196, v196, v233, -v211\n"
    "v_mfma_f32_16x16x32_bf16 v[100:103], v[150:153], a[228:231], v[100:103]\n"
    "s_waitcnt vmcnt(8)\n"
    "v_cmp_ne_u64 vcc, s[80:81], v[252:253]\n"
    "v_mfma_f32_16x16x32_bf16 v[100:103], v[154:157], a[224:227], v[100:103]\n"
    "v_exp_f32 v79, v79\n"
    "v_mfma_f32_16x16x32_bf16 v[100:103], v[158:161], a[220:223], v[100:103]\n"
    "s_nop 0\n"
    "s_cbranch_vccnz SPV%=\n"
    "RTV%=:\n"
    "v_cvt_pk_bf16_f32 v60, v108, v109\n"
    "v_mfma_f32_16x16x32_bf16 v[100:103], v[162:165], a[216:219], v[100:103]\n"
    "s_add_u32 s87, s87, 256\n"
    "v_cvt_pk_bf16_f32 v61, v110, v111\n"
    "v_mfma_f32_16x16x32_bf16 v[104:107], v[162:165], a[240:243], v[136:139]\n"
    "v_exp_f32 v80, v80\n"
    "v_mfma_f32_16x16x32_bf16 v[104:107], v[158:161], a[244:247], v[104:107]\n"
    "v_add_f32 v147, v147, v79\n"
    "v_add_f32 v144, v144, v80\n"
    "v_mfma_f32_16x16x32_bf16 v[104:107], v[154:157], a[248:251], v[104:107]\n"
    "v_cvt_pk_bf16_f32 v38, v112, v113\n"
    "v_fma_f32 v195, v195, v232, -v207\n"
    "v_mfma_f32_16x16x32_bf16 v[104:107], v[150:153], a[252:255], v[104:107]\n"
    "v_exp_f32 v81, v81\n"
    "v_mfma_f32_16x16x32_bf16 v[104:107], v[166:169], v[0:3], v[104:107]\n"
    "v_add_f32 v144, v144, v81\n"
    "v_fma_f32 v197, v197, v233, -v211\n"
    "v_mfma_f32_16x16x32_bf16 v[104:107], v[170:173], v[4:7], v[104:107]\n"
    "s_mov_b32 m0, s74\n"
    "v_cvt_pk_bf16_f32 v39, v114, v115\n"
    "v_mfma_f32_16x16x32_bf16 v[108:111], v[170:173], v[28:31], v[140:143]\n"
    "v_exp_f32 v82, v82\n"
    "v_mfma_f32_16x16x32_bf16 v[108:111], v[166:169], v[24:27], v[108:111]\n"
    "ds_read_b128 v[170:173], v254 offset:22528\n"
    "ds_read_b128 v[166:169], v251 offset:22528\n"
    "v_mfma_f32_16x16x32_bf16 v[108:111], v[150:153], v[20:23], v[108:111]\n"
    "s_add_u32 s74, s74, s73\n"
    "v_cvt_pk_bf16_f32 v46, v116, v117\n"
    "v_mfma_f32_16x16x32_bf16 v[108:111], v[154:157], v[16:19], v[108:111]\n"
    "v_exp_f32 v83, v83\n"
    "v_mfma_f32_16x16x32_bf16 v[108:111], v[158:161], v[12:15], v[108:111]\n"
    "s_cmp_ge_u32 s74, s75\n"
    "s_cselect_b32 s73, 0xfffe8000, s76\n"
    "v_mfma_f32_16x16x32_bf16 v[108:111], v[162:165], v[8:11], v[108:111]\n"
    "ds_read_b128 v[150:153], v148 offset:24576\n"
    "s_waitcnt lgkmcnt(3)\n"
    "v_mfma_f32_16x16x32_bf16 v[112:115], v[186:189], a[192:195], v[128:131]\n"
    "v_exp_f32 v84, v84\n"
    "v_mfma_f32_16x16x32_bf16 v[112:115], v[182:185], a[196:199], v[112:115]\n"
    "ds_read_b128 v[154:157], v148 offset:36864\n"
    "v_cvt_pk_bf16_f32 v47, v118, v119\n"
    "v_mfma_f32_16x16x32_bf16 v[112:115], v[178:181], a[200:203], v[112:115]\n"
    "buffer_load_dwordx4 v149, s[36:39], s72 offen offset:0 sc1 lds\n"
    "v_mfma_f32_16x16x32_bf16 v[112:115], v[174:177], a[204:207], v[112:115]\n"
    "v_exp_f32 v85, v85\n"
    "s_waitcnt lgkmcnt(2)\n"
    "v_mfma_f32_16x16x32_bf16 v[112:115], v[170:173], a[208:211], v[112:115]\n"
    "v_cvt_pk_bf16_f32 v54, v120, v121\n"
    "v_add_f32 v144, v144, v82\n"
    "v_mfma_f32_16x16x32_bf16 v[112:115], v[166:169], a[212:215], v[112:115]\n"
    "v_cvt_pk_bf16_f32 v55, v122, v123\n"
    "v_add_f32 v144, v144, v83\n"
    "v_mfma_f32_16x16x32_bf16 v[116:119], v[166:169], a[236:239], v[132:135]\n"
    "v_exp_f32 v86, v86\n"
    "v_mfma_f32_16x16x32_bf16 v[116:119], v[170:173], a[232:235], v[116:119]\n"
    "buffer_load_dwordx4 v149, s[36:39], s72 offen offset:1024 sc1 lds\n"
    "v_mfma_f32_16x16x32_bf16 v[116:119], v[174:177], a[228:231], v[116:119]\n"
    "ds_read_b128 v[158:161], v148 offset:25600\n"
    "ds_read_b128 v[162:165], v148 offset:37888\n"
    "v_mfma_f32_16x16x32_bf16 v[116:119], v[178:181], a[224:227], v[116:119]\n"
    "v_exp_f32 v87, v87\n"
    "v_mfma_f32_16x16x32_bf16 v[116:119], v[182:185], a[220:223], v[116:119]\n"
    "v_cvt_pk_bf16_f32 v62, v124, v125\n"
    "v_add_f32 v145, v145, v84\n"
    "v_mfma_f32_16x16x32_bf16 v[116:119], v[186:189], a[216:219], v[116:119]\n"
    "v_cvt_pk_bf16_f32 v63, v126, v127\n"
    "v_add_f32 v145, v145, v85\n"
    "v_mfma_f32_16x16x32_bf16 v[120:123], v[186:189], a[240:243], v[136:139]\n"
    "v_exp_f32 v88, v88\n"
    "v_mfma_f32_16x16x32_bf16 v[120:123], v[182:185], a[244:247], v[120:123]\n"
    "buffer_load_dwordx4 v149, s[36:39], s72 offen offset:2048 sc1 lds\n"
    "v_mfma_f32_16x16x32_bf16 v[120:123], v[178:181], a[248:251], v[120:123]\n"
    "v_add_f32 v145, v145, v86\n"
    "v_add_f32 v145, v145, v87\n"
    "v_mfma_f32_16x16x32_bf16 v[120:123], v[174:177], a[252:255], v[120:123]\n"
    "v_exp_f32 v89, v89\n"
    "v_mfma_f32_16x16x32_bf16 v[120:123], v[170:173], v[0:3], v[120:123]\n"
    "v_add_f32 v146, v146, v88\n"
    "v_add_f32 v146, v146, v89\n"
    "v_mfma_f32_16x16x32_bf16 v[120:123], v[166:169], v[4:7], v[120:123]\n"
    "v_cvt_pk_bf16_f32 v239, v194, v196\n"
    "v_cvt_pk_bf16_f32 v243, v195, v197\n"
    "v_mfma_f32_16x16x32_bf16 v[124:127], v[166:169], v[28:31], v[140:143]\n"
    "v_exp_f32 v90, v90\n"
    "v_mfma_f32_16x16x32_bf16 v[124:127], v[170:173], v[24:27], v[124:127]\n"
    "ds_read_b128 v[166:169], v148 offset:26624\n"
    "ds_read_b128 v[170:173], v148 offset:38912\n"
    "v_mfma_f32_16x16x32_bf16 v[124:127], v[174:177], v[20:23], v[124:127]\n"
    "buffer_load_dwordx4 v149, s[36:39], s72 offen offset:3072 sc1 lds\n"
    "v_mfma_f32_16x16x32_bf16 v[124:127], v[178:181], v[16:19], v[124:127]\n"
    "v_exp_f32 v91, v91\n"
    "v_mfma_f32_16x16x32_bf16 v[124:127], v[182:185], v[12:15], v[124:127]\n"
    "s_add_u32 s72, s72, 4096\n"
    "v_add_f32 v146, v146, v90\n"
    "v_mfma_f32_16x16x32_bf16 v[124:127], v[186:189], v[8:11], v[124:127]\n"
    "ds_read_b128 v[174:177], v148 offset:27648\n"
    "s_waitcnt lgkmcnt(3)\n"
    "v_mfma_f32_16x16x32_bf16 a[0:3], v[32:35], v[150:153], a[0:3]\n"
    "v_exp_f32 v92, v92\n"
    "v_mfma_f32_16x16x32_bf16 a[0:3], v[36:39], v[154:157], a[0:3]\n"
    "ds_read_b128 v[178:181], v148 offset:39936\n"
    "s_add_u32 m0, m0, 4096\n"
    "v_mfma_f32_16x16x32_bf16 a[4:7], v[44:47], v[154:157], a[4:7]\n"
    "v_add_f32 v146, v146, v91\n"
    "v_add_f32 v147, v147, v92\n"
    "v_mfma_f32_16x16x32_bf16 a[4:7], v[40:43], v[150:153], a[4:7]\n"
    "v_exp_f32 v93, v93\n"
    "v_mfma_f32_16x16x32_bf16 a[8:11], v[48:51], v[150:153], a[8:11]\n"
    "v_add_f32 v147, v147, v93\n"
    "v_cvt_f32_i32 v198, v198\n"
    "v_mfma_f32_16x16x32_bf16 a[8:11], v[52:55], v[154:157], a[8:11]\n"
    "v_cvt_f32_i32 v200, v200\n"
    "v_cvt_f32_i32 v199, v199\n"
    "v_mfma_f32_16x16x32_bf16 a[12:15], v[60:63], v[154:157], a[12:15]\n"
    "v_exp_f32 v94, v94\n"
    "v_mfma_f32_16x16x32_bf16 a[12:15], v[56:59], v[150:153], a[12:15]\n"
    "buffer_load_dwordx4 v149, s[36:39], s72 offen offset:0 sc1 lds\n"
    "v_mfma_f32_16x16x32_bf16 a[16:19], v[32:35], v[158:161], a[16:19]\n"
    "ds_read_b128 v[182:185], v148 offset:28672\n"
    "ds_read_b128 v[186:189], v148 offset:40960\n"
    "v_mfma_f32_16x16x32_bf16 a[16:19], v[36:39], v[162:165], a[16:19]\n"
    "v_exp_f32 v95, v95\n"
    "v_mfma_f32_16x16x32_bf16 a[20:23], v[44:47], v[162:165], a[20:23]\n"
    "v_add_f32 v147, v147, v94\n"
    "v_add_f32 v147, v147, v95\n"
    "v_mfma_f32_16x16x32_bf16 a[20:23], v[40:43], v[158:161], a[20:23]\n"
    "v_cvt_f32_i32 v201, v201\n"
    "v_fma_f32 v198, v198, v234, -v208\n"
    "v_mfma_f32_16x16x32_bf16 a[24:27], v[48:51], v[158:161], a[24:27]\n"
    "v_exp_f32 v96, v96\n"
    "v_mfma_f32_16x16x32_bf16 a[24:27], v[52:55], v[162:165], a[24:27]\n"
    "buffer_load_dwordx4 v149, s[36:39], s72 offen offset:1024 sc1 lds\n"
    "v_mfma_f32_16x16x32_bf16 a[28:31], v[60:63], v[162:165], a[28:31]\n"
    "v_add_f32 v144, v144, v96\n"
    "v_fma_f32 v200, v200, v235, -v212\n"
    "v_mfma_f32_16x16x32_bf16 a[28:31], v[56:59], v[158:161], a[28:31]\n"
    "v_exp_f32 v97, v97\n"
    "s_waitcnt lgkmcnt(0)\n"
    "v_mfma_f32_16x16x32_bf16 a[32:35], v[32:35], v[166:169], a[32:35]\n"
    "ds_read_b128 v[150:153], v148 offset:29696\n"
    "v_add_f32 v144, v144, v97\n"
    "v_mfma_f32_16x16x32_bf16 a[32:35], v[36:39], v[170:173], a[32:35]\n"
    "ds_read_b128 v[154:157], v148 offset:41984\n"
    "v_fma_f32 v199, v199, v234, -v208\n"
    "v_mfma_f32_16x16x32_bf16 a[36:39], v[44:47], v[170:173], a[36:39]\n"
    "v_exp_f32 v98, v98\n"
    "v_mfma_f32_16x16x32_bf16 a[36:39], v[40:43], v[166:169], a[36:39]\n"
    "buffer_load_dwordx4 v149, s[36:39], s72 offen offset:2048 sc1 lds\n"
    "v_mfma_f32_16x16x32_bf16 a[40:43], v[48:51], v[166:169], a[40:43]\n"
    "v_add_f32 v144, v144, v98\n"
    "v_mfma_f32_16x16x32_bf16 a[40:43], v[52:55], v[170:173], a[40:43]\n"
    "v_exp_f32 v99, v99\n"
    "v_mfma_f32_16x16x32_bf16 a[44:47], v[60:63], v[170:173], a[44:47]\n"
    "v_add_f32 v144, v144, v99\n"
    "v_fma_f32 v201, v201, v235, -v212\n"
    "v_mfma_f32_16x16x32_bf16 a[44:47], v[56:59], v[166:169], a[44:47]\n"
    "ds_read_b128 v[158:161], v148 offset:30720\n"
    "v_cvt_pk_bf16_f32 v240, v198, v200\n"
    "v_mfma_f32_16x16x32_bf16 a[48:51], v[32:35], v[174:177], a[48:51]\n"
    "v_exp_f32 v100, v100\n"
    "v_mfma_f32_16x16x32_bf16 a[48:51], v[36:39], v[178:181], a[48:51]\n"
    "ds_read_b128 v[162:165], v148 offset:43008\n"
    "v_add_f32 v145, v145, v100\n"
    "v_mfma_f32_16x16x32_bf16 a[52:55], v[44:47], v[178:181], a[52:55]\n"
    "buffer_load_dwordx4 v149, s[36:39], s72 offen offset:3072 sc1 lds\n"
    "v_mfma_f32_16x16x32_bf16 a[52:55], v[40:43], v[174:177], a[52:55]\n"
    "v_exp_f32 v101, v101\n"
    "v_mfma_f32_16x16x32_bf16 a[56:59], v[48:51], v[174:177], a[56:59]\n"
    "s_add_u32 s72, s72, 4096\n"
    "v_add_f32 v145, v145, v101\n"
    "v_mfma_f32_16x16x32_bf16 a[56:59], v[52:55], v[178:181], a[56:59]\n"
    "s_add_u32 m0, m0, 4096\n"
    "v_cvt_pk_bf16_f32 v244, v199, v201\n"
    "v_mfma_f32_16x16x32_bf16 a[60:63], v[60:63], v[178:181], a[60:63]\n"
    "v_exp_f32 v102, v102\n"
    "v_mfma_f32_16x16x32_bf16 a[60:63], v[56:59], v[174:177], a[60:63]\n"
    "v_add_f32 v145, v145, v102\n"
    "v_cvt_f32_i32 v202, v202\n"
    "v_mfma_f32_16x16x32_bf16 a[64:67], v[32:35], v[182:185], a[64:67]\n"
    "ds_read_b128 v[166:169], v148 offset:31744\n"
    "ds_read_b128 v[170:173], v148 offset:44032\n"
    "v_mfma_f32_16x16x32_bf16 a[64:67], v[36:39], v[186:189], a[64:67]\n"
    "v_exp_f32 v103, v103\n"
    "v_mfma_f32_16x16x32_bf16 a[68:71], v[44:47], v[186:189], a[68:71]\n"
    "v_add_f32 v145, v145, v103\n"
    "v_cvt_f32_i32 v204, v204\n"
    "v_mfma_f32_16x16x32_bf16 a[68:71], v[40:43], v[182:185], a[68:71]\n"
    "buffer_load_dwordx4 v149, s[36:39], s72 offen offset:0 sc1 lds\n"
    "v_mfma_f32_16x16x32_bf16 a[72:75], v[48:51], v[182:185], a[72:75]\n"
    "v_exp_f32 v104, v104\n"
    "v_mfma_f32_16x16x32_bf16 a[72:75], v[52:55], v[186:189], a[72:75]\n"
    "v_add_f32 v146, v146, v104\n"
    "v_cvt_f32_i32 v203, v203\n"
    "v_mfma_f32_16x16x32_bf16 a[76:79], v[60:63], v[186:189], a[76:79]\n"
    "v_cvt_f32_i32 v205, v205\n"
    "v_fma_f32 v202, v202, v236, -v209\n"
    "v_mfma_f32_16x16x32_bf16 a[76:79], v[56:59], v[182:185], a[76:79]\n"
    "v_exp_f32 v105, v105\n"
    "s_waitcnt lgkmcnt(0)\n"
    "v_mfma_f32_16x16x32_bf16 a[80:83], v[32:35], v[150:153], a[80:83]\n"
    "v_add_f32 v146, v146, v105\n"
    "v_fma_f32 v204, v204, v237, -v213\n"
    "v_mfma_f32_16x16x32_bf16 a[80:83], v[36:39], v[154:157], a[80:83]\n"
    "buffer_load_dwordx4 v149, s[36:39], s72 offen offset:1024 sc1 lds\n"
    "v_mfma_f32_16x16x32_bf16 a[84:87], v[44:47], v[154:157], a[84:87]\n"
    "v_exp_f32 v106, v106\n"
    "v_mfma_f32_16x16x32_bf16 a[84:87], v[40:43], v[150:153], a[84:87]\n"
    "v_add_f32 v146, v146, v106\n"
    "v_fma_f32 v203, v203, v236, -v209\n"
    "v_mfma_f32_16x16x32_bf16 a[88:91], v[48:51], v[150:153], a[88:91]\n"
    "v_fma_f32 v205, v205, v237, -v213\n"
    "v_cvt_pk_bf16_f32 v241, v202, v204\n"
    "v_mfma_f32_16x16x32_bf16 a[88:91], v[52:55], v[154:157], a[88:91]\n"
    "v_exp_f32 v107, v107\n"
    "v_mfma_f32_16x16x32_bf16 a[92:95], v[60:63], v[154:157], a[92:95]\n"
    "buffer_load_dwordx4 v149, s[36:39], s72 offen offset:2048 sc1 lds\n"
    "v_mfma_f32_16x16x32_bf16 a[92:95], v[56:59], v[150:153], a[92:95]\n"
    "ds_read_b128 v[150:153], v148 offset:32768\n"
    "ds_read_b128 v[154:157], v148 offset:45056\n"
    "v_mfma_f32_16x16x32_bf16 a[96:99], v[32:35], v[158:161], a[96:99]\n"
    "v_exp_f32 v108, v108\n"
    "v_mfma_f32_16x16x32_bf16 a[96:99], v[36:39], v[162:165], a[96:99]\n"
    "v_add_f32 v146, v146, v107\n"
    "v_add_f32 v147, v147, v108\n"
    "v_mfma_f32_16x16x32_bf16 a[100:103], v[44:47], v[162:165], a[100:103]\n"
    "v_cvt_pk_bf16_f32 v245, v203, v205\n"
    "v_mfma_f32_16x16x32_bf16 a[100:103], v[40:43], v[158:161], a[100:103]\n"
    "v_exp_f32 v109, v109\n"
    "v_mfma_f32_16x16x32_bf16 a[104:107], v[48:51], v[158:161], a[104:107]\n"
    "buffer_load_dwordx4 v149, s[36:39], s72 offen offset:3072 sc1 lds\n"
    "v_mfma_f32_16x16x32_bf16 a[104:107], v[52:55], v[162:165], a[104:107]\n"
    "s_add_u32 s72, s72, 16384\n"
    "v_add_f32 v147, v147, v109\n"
    "v_mfma_f32_16x16x32_bf16 a[108:111], v[60:63], v[162:165], a[108:111]\n"
    "v_exp_f32 v110, v110\n"
    "v_mfma_f32_16x16x32_bf16 a[108:111], v[56:59], v[158:161], a[108:111]\n"
    "ds_read_b128 v[158:161], v148 offset:33792\n"
    "ds_read_b128 v[162:165], v148 offset:46080\n"
    "v_mfma_f32_16x16x32_bf16 a[112:115], v[32:35], v[166:169], a[112:115]\n"
    "ds_read_b128 v[174:177], v148 offset:34816\n"
    "ds_read_b128 v[178:181], v148 offset:47104\n"
    "v_mfma_f32_16x16x32_bf16 a[112:115], v[36:39], v[170:173], a[112:115]\n"
    "v_exp_f32 v111, v111\n"
    "v_mfma_f32_16x16x32_bf16 a[116:119], v[44:47], v[170:173], a[116:119]\n"
    "ds_read_b128 v[182:185], v148 offset:35840\n"
    "v_add_f32 v147, v147, v110\n"
    "v_mfma_f32_16x16x32_bf16 a[116:119], v[40:43], v[166:169], a[116:119]\n"
    "ds_read_b128 v[186:189], v148 offset:48128\n"
    "v_add_f32 v147, v147, v111\n"
    "v_mfma_f32_16x16x32_bf16 a[120:123], v[48:51], v[166:169], a[120:123]\n"
    "v_exp_f32 v112, v112\n"
    "v_mfma_f32_16x16x32_bf16 a[120:123], v[52:55], v[170:173], a[120:123]\n"
    "buffer_store_dwordx4 v[238:241], v149, s[52:55], s85 offen offset:0 sc1\n"
    "v_mfma_f32_16x16x32_bf16 a[124:127], v[60:63], v[170:173], a[124:127]\n"
    "buffer_store_dwordx4 v[242:245], v149, s[52:55], s85 offen offset:1024 sc1\n"
    "v_mfma_f32_16x16x32_bf16 a[124:127], v[56:59], v[166:169], a[124:127]\n"
    "v_exp_f32 v113, v113\n"
    "s_waitcnt lgkmcnt(2)\n"
    "v_mfma_f32_16x16x32_bf16 a[128:131], v[32:35], v[150:153], a[128:131]\n"
    "v_add_f32 v144, v144, v112\n"
    "v_add_f32 v144, v144, v113\n"
    "v_mfma_f32_16x16x32_bf16 a[128:131], v[36:39], v[154:157], a[128:131]\n"
    "v_mfma_f32_16x16x32_bf16 a[132:135], v[44:47], v[154:157], a[132:135]\n"
    "v_exp_f32 v114, v114\n"
    "v_mfma_f32_16x16x32_bf16 a[132:135], v[40:43], v[150:153], a[132:135]\n"
    "s_waitcnt vmcnt(14) lgkmcnt(0)\n"
    "s_barrier\n"
    "v_mfma_f32_16x16x32_bf16 a[136:139], v[48:51], v[150:153], a[136:139]\n"
    "v_add_u32 v254, s73, v254\n"
    "v_add_u32 v251, s73, v251\n"
    "v_mfma_f32_16x16x32_bf16 a[136:139], v[52:55], v[154:157], a[136:139]\n"
    "v_exp_f32 v115, v115\n"
    "v_mfma_f32_16x16x32_bf16 a[140:143], v[60:63], v[154:157], a[140:143]\n"
    "buffer_load_dwordx2 v[190:191], v248, s[40:43], s82 offen offset:256 nt\n"
    "v_mfma_f32_16x16x32_bf16 a[140:143], v[56:59], v[150:153], a[140:143]\n"
    "ds_read_b128 v[150:153], v254 offset:0\n"
    "v_add_f32 v144, v144, v114\n"
    "v_mfma_f32_16x16x32_bf16 a[144:147], v[32:35], v[158:161], a[144:147]\n"
    "v_exp_f32 v116, v116\n"
    "v_mfma_f32_16x16x32_bf16 a[144:147], v[36:39], v[162:165], a[144:147]\n"
    "ds_read_b128 v[154:157], v251 offset:0\n"
    "v_add_f32 v144, v144, v115\n"
    "v_mfma_f32_16x16x32_bf16 a[148:151], v[44:47], v[162:165], a[148:151]\n"
    "buffer_load_dwordx2 v[192:193], v248, s[40:43], s82 offen offset:1024 nt\n"
    "v_mfma_f32_16x16x32_bf16 a[148:151], v[40:43], v[158:161], a[148:151]\n"
    "v_exp_f32 v117, v117\n"
    "v_mfma_f32_16x16x32_bf16 a[152:155], v[48:51], v[158:161], a[152:155]\n"
    "v_add_f32 v145, v145, v116\n"
    "v_add_f32 v145, v145, v117\n"
    "v_mfma_f32_16x16x32_bf16 a[152:155], v[52:55], v[162:165], a[152:155]\n"
    "buffer_load_dwordx2 v[194:195], v248, s[40:43], s82 offen offset:1792 nt\n"
    "v_mfma_f32_16x16x32_bf16 a[156:159], v[60:63], v[162:165], a[156:159]\n"
    "v_exp_f32 v118, v118\n"
    "v_mfma_f32_16x16x32_bf16 a[156:159], v[56:59], v[158:161], a[156:159]\n"
    "ds_read_b128 v[158:161], v254 offset:2048\n"
    "ds_read_b128 v[162:165], v251 offset:2048\n"
    "v_mfma_f32_16x16x32_bf16 a[160:163], v[32:35], v[174:177], a[160:163]\n"
    "v_add_u32 v148, s73, v148\n"
    "ds_read_b128 v[166:169], v254 offset:4096\n"
    "v_mfma_f32_16x16x32_bf16 a[160:163], v[36:39], v[178:181], a[160:163]\n"
    "v_exp_f32 v119, v119\n"
    "v_mfma_f32_16x16x32_bf16 a[164:167], v[44:47], v[178:181], a[164:167]\n"
    "ds_read_b128 v[170:173], v251 offset:4096\n"
    "v_add_f32 v145, v145, v118\n"
    "v_mfma_f32_16x16x32_bf16 a[164:167], v[40:43], v[174:177], a[164:167]\n"
    "buffer_load_dwordx2 v[196:197], v248, s[40:43], s82 offen offset:2560 nt\n"
    "v_mfma_f32_16x16x32_bf16 a[168:171], v[48:51], v[174:177], a[168:171]\n"
    "v_exp_f32 v120, v120\n"
    "v_mfma_f32_16x16x32_bf16 a[168:171], v[52:55], v[178:181], a[168:171]\n"
    "v_add_f32 v145, v145, v119\n"
    "v_add_f32 v146, v146, v120\n"
    "v_mfma_f32_16x16x32_bf16 a[172:175], v[60:63], v[178:181], a[172:175]\n"
    "buffer_load_dwordx2 v[198:199], v248, s[40:43], s83 offen offset:256 nt\n"
    "v_mfma_f32_16x16x32_bf16 a[172:175], v[56:59], v[174:177], a[172:175]\n"
    "v_exp_f32 v121, v121\n"
    "v_mfma_f32_16x16x32_bf16 a[176:179], v[32:35], v[182:185], a[176:179]\n"
    "v_cvt_pk_bf16_f32 v32, v64, v65\n"
    "v_add_f32 v146, v146, v121\n"
    "v_mfma_f32_16x16x32_bf16 a[176:179], v[36:39], v[186:189], a[176:179]\n"
    "buffer_load_dwordx2 v[200:201], v248, s[40:43], s83 offen offset:1024 nt\n"
    "v_mfma_f32_16x16x32_bf16 a[180:183], v[44:47], v[186:189], a[180:183]\n"
    "v_exp_f32 v122, v122\n"
    "v_mfma_f32_16x16x32_bf16 a[180:183], v[40:43], v[182:185], a[180:183]\n"
    "v_cvt_pk_bf16_f32 v33, v66, v67\n"
    "v_add_f32 v146, v146, v122\n"
    "v_mfma_f32_16x16x32_bf16 a[184:187], v[48:51], v[182:185], a[184:187]\n"
    "v_cvt_pk_bf16_f32 v40, v68, v69\n"
    "v_cvt_f32_i32 v214, v214\n"
    "v_mfma_f32_16x16x32_bf16 a[184:187], v[52:55], v[186:189], a[184:187]\n"
    "v_exp_f32 v123, v123\n"
    "v_mfma_f32_16x16x32_bf16 a[188:191], v[60:63], v[186:189], a[188:191]\n"
    "buffer_load_dwordx2 v[202:203], v248, s[40:43], s83 offen offset:1792 nt\n"
    "v_add_f32 v146, v146, v123\n"
    "v_mfma_f32_16x16x32_bf16 a[188:191], v[56:59], v[182:185], a[188:191]\n"
    "ds_read_b128 v[174:177], v254 offset:6144\n"
    "s_waitcnt lgkmcnt(1)\n"
    "v_mfma_f32_16x16x32_bf16 v[64:67], v[150:153], a[192:195], v[128:131]\n"
    "v_exp_f32 v124, v124\n"
    "v_mfma_f32_16x16x32_bf16 v[64:67], v[154:157], a[196:199], v[64:67]\n"
    "ds_read_b128 v[178:181], v251 offset:6144\n"
    "v_cvt_pk_bf16_f32 v41, v70, v71\n"
    "v_mfma_f32_16x16x32_bf16 v[64:67], v[158:161], a[200:203], v[64:67]\n"
    "ds_read_b128 v[182:185], v254 offset:8192\n"
    "ds_read_b128 v[186:189], v251 offset:8192\n"
    "v_mfma_f32_16x16x32_bf16 v[64:67], v[162:165], a[204:207], v[64:67]\n"
    "v_exp_f32 v125, v125\n"
    "v_mfma_f32_16x16x32_bf16 v[64:67], v[166:169], a[208:211], v[64:67]\n"
    "buffer_load_dwordx2 v[204:205], v248, s[40:43], s83 offen offset:2560 nt\n"
    "v_mfma_f32_16x16x32_bf16 v[64:67], v[170:173], a[212:215], v[64:67]\n"
    "s_add_u32 s82, s82, 196608\n"
    "v_cvt_pk_bf16_f32 v48, v72, v73\n"
    "v_mfma_f32_16x16x32_bf16 v[68:71], v[170:173], a[236:239], v[132:135]\n"
    "v_exp_f32 v126, v126\n"
    "v_mfma_f32_16x16x32_bf16 v[68:71], v[166:169], a[232:235], v[68:71]\n"
    "v_cvt_pk_bf16_f32 v49, v74, v75\n"
    "v_add_f32 v147, v147, v124\n"
    "v_mfma_f32_16x16x32_bf16 v[68:71], v[162:165], a[228:231], v[68:71]\n"
    "v_cvt_pk_bf16_f32 v56, v76, v77\n"
    "v_add_f32 v147, v147, v125\n"
    "v_mfma_f32_16x16x32_bf16 v[68:71], v[158:161], a[224:227], v[68:71]\n"
    "v_exp_f32 v127, v127\n"
    "v_mfma_f32_16x16x32_bf16 v[68:71], v[154:157], a[220:223], v[68:71]\n"
    "v_add_f32 v147, v147, v126\n"
    "v_add_f32 v147, v147, v127\n"
    "v_mfma_f32_16x16x32_bf16 v[68:71], v[150:153], a[216:219], v[68:71]\n"
    "s_add_u32 s83, s83, 196608\n"
    "v_cvt_pk_bf16_f32 v57, v78, v79\n"
    "v_mfma_f32_16x16x32_bf16 v[72:75], v[150:153], a[240:243], v[136:139]\n"
    "v_exp_f32 v64, v64\n"
    "v_mfma_f32_16x16x32_bf16 v[72:75], v[154:157], a[244:247], v[72:75]\n"
    "v_add_f32 v144, v144, v64\n"
    "v_cvt_f32_i32 v216, v216\n"
    "v_mfma_f32_16x16x32_bf16 v[72:75], v[158:161], a[248:251], v[72:75]\n"
    "v_cvt_pk_bf16_f32 v34, v80, v81\n"
    "v_cvt_f32_i32 v215, v215\n"
    "v_mfma_f32_16x16x32_bf16 v[72:75], v[162:165], a[252:255], v[72:75]\n"
    "v_exp_f32 v65, v65\n"
    "v_mfma_f32_16x16x32_bf16 v[72:75], v[166:169], v[0:3], v[72:75]\n"
    "v_add_f32 v144, v144, v65\n"
    "v_cvt_f32_i32 v217, v217\n"
    "v_mfma_f32_16x16x32_bf16 v[72:75], v[170:173], v[4:7], v[72:75]\n"
    "v_cvt_pk_bf16_f32 v35, v82, v83\n"
    "v_fma_f32 v214, v214, v230, -v206\n"
    "v_mfma_f32_16x16x32_bf16 v[76:79], v[170:173], v[28:31], v[140:143]\n"
    "v_exp_f32 v66, v66\n"
    "v_mfma_f32_16x16x32_bf16 v[76:79], v[166:169], v[24:27], v[76:79]\n"
    "ds_read_b128 v[170:173], v254 offset:10240\n"
    "ds_read_b128 v[166:169], v251 offset:10240\n"
    "v_mfma_f32_16x16x32_bf16 v[76:79], v[162:165], v[20:23], v[76:79]\n"
    "s_add_u32 s84, s84, 512\n"
    "v_cvt_pk_bf16_f32 v42, v84, v85\n"
    "v_mfma_f32_16x16x32_bf16 v[76:79], v[158:161], v[16:19], v[76:79]\n"
    "v_exp_f32 v67, v67\n"
    "v_mfma_f32_16x16x32_bf16 v[76:79], v[154:157], v[12:15], v[76:79]\n"
    "v_add_f32 v144, v144, v66\n"
    "v_add_f32 v144, v144, v67\n"
    "v_mfma_f32_16x16x32_bf16 v[76:79], v[150:153], v[8:11], v[76:79]\n"
    "ds_read_b128 v[162:165], v254 offset:12288\n"
    "s_waitcnt lgkmcnt(3)\n"
    "v_mfma_f32_16x16x32_bf16 v[80:83], v[174:177], a[192:195], v[128:131]\n"
    "v_exp_f32 v68, v68\n"
    "v_mfma_f32_16x16x32_bf16 v[80:83], v[178:181], a[196:199], v[80:83]\n"
    "ds_read_b128 v[158:161], v251 offset:12288\n"
    "v_cvt_pk_bf16_f32 v43, v86, v87\n"
    "v_mfma_f32_16x16x32_bf16 v[80:83], v[182:185], a[200:203], v[80:83]\n"
    "ds_read_b128 v[154:157], v254 offset:14336\n"
    "ds_read_b128 v[150:153], v251 offset:14336\n"
    "v_mfma_f32_16x16x32_bf16 v[80:83], v[186:189], a[204:207], v[80:83]\n"
    "v_exp_f32 v69, v69\n"
    "s_waitcnt lgkmcnt(4)\n"
    "v_mfma_f32_16x16x32_bf16 v[80:83], v[170:173], a[208:211], v[80:83]\n"
    "v_cvt_pk_bf16_f32 v50, v88, v89\n"
    "v_add_f32 v145, v145, v68\n"
    "v_mfma_f32_16x16x32_bf16 v[80:83], v[166:169], a[212:215], v[80:83]\n"
    "v_cvt_pk_bf16_f32 v51, v90, v91\n"
    "v_add_f32 v145, v145, v69\n"
    "v_mfma_f32_16x16x32_bf16 v[84:87], v[166:169], a[236:239], v[132:135]\n"
    "v_exp_f32 v70, v70\n"
    "v_mfma_f32_16x16x32_bf16 v[84:87], v[170:173], a[232:235], v[84:87]\n"
    "v_add_f32 v145, v145, v70\n"
    "v_fma_f32 v216, v216, v231, -v210\n"
    "v_mfma_f32_16x16x32_bf16 v[84:87], v[186:189], a[228:231], v[84:87]\n"
    "v_cvt_pk_bf16_f32 v58, v92, v93\n"
    "v_fma_f32 v215, v215, v230, -v206\n"
    "v_mfma_f32_16x16x32_bf16 v[84:87], v[182:185], a[224:227], v[84:87]\n"
    "v_exp_f32 v71, v71\n"
    "v_mfma_f32_16x16x32_bf16 v[84:87], v[178:181], a[220:223], v[84:87]\n"
    "v_add_f32 v145, v145, v71\n"
    "v_fma_f32 v217, v217, v231, -v210\n"
    "v_mfma_f32_16x16x32_bf16 v[84:87], v[174:177], a[216:219], v[84:87]\n"
    "v_cvt_pk_bf16_f32 v59, v94, v95\n"
    "v_mfma_f32_16x16x32_bf16 v[88:91], v[174:177], a[240:243], v[136:139]\n"
    "v_exp_f32 v72, v72\n"
    "v_mfma_f32_16x16x32_bf16 v[88:91], v[178:181], a[244:247], v[88:91]\n"
    "v_add_f32 v146, v146, v72\n"
    "v_cvt_pk_bf16_f32 v238, v214, v216\n"
    "v_mfma_f32_16x16x32_bf16 v[88:91], v[182:185], a[248:251], v[88:91]\n"
    "v_cvt_pk_bf16_f32 v36, v96, v97\n"
    "v_cvt_pk_bf16_f32 v242, v215, v217\n"
    "v_mfma_f32_16x16x32_bf16 v[88:91], v[186:189], a[252:255], v[88:91]\n"
    "v_exp_f32 v73, v73\n"
    "v_mfma_f32_16x16x32_bf16 v[88:91], v[170:173], v[0:3], v[88:91]\n"
    "v_add_f32 v146, v146, v73\n"
    "v_cvt_f32_i32 v218, v218\n"
    "v_mfma_f32_16x16x32_bf16 v[88:91], v[166:169], v[4:7], v[88:91]\n"
    "v_cvt_pk_bf16_f32 v37, v98, v99\n"
    "v_cvt_f32_i32 v220, v220\n"
    "v_mfma_f32_16x16x32_bf16 v[92:95], v[166:169], v[28:31], v[140:143]\n"
    "v_exp_f32 v74, v74\n"
    "v_mfma_f32_16x16x32_bf16 v[92:95], v[170:173], v[24:27], v[92:95]\n"
    "ds_read_b128 v[166:169], v254 offset:16384\n"
    "ds_read_b128 v[170:173], v251 offset:16384\n"
    "v_mfma_f32_16x16x32_bf16 v[92:95], v[186:189], v[20:23], v[92:95]\n"
    "v_cvt_pk_bf16_f32 v44, v100, v101\n"
    "v_add_f32 v146, v146, v74\n"
    "v_mfma_f32_16x16x32_bf16 v[92:95], v[182:185], v[16:19], v[92:95]\n"
    "v_exp_f32 v75, v75\n"
    "v_mfma_f32_16x16x32_bf16 v[92:95], v[178:181], v[12:15], v[92:95]\n"
    "v_add_f32 v146, v146, v75\n"
    "v_cvt_f32_i32 v219, v219\n"
    "v_mfma_f32_16x16x32_bf16 v[92:95], v[174:177], v[8:11], v[92:95]\n"
    "ds_read_b128 v[186:189], v254 offset:18432\n"
    "s_waitcnt lgkmcnt(3)\n"
    "v_mfma_f32_16x16x32_bf16 v[96:99], v[162:165], a[192:195], v[128:131]\n"
    "v_exp_f32 v76, v76\n"
    "v_mfma_f32_16x16x32_bf16 v[96:99], v[158:161], a[196:199], v[96:99]\n"
    "ds_read_b128 v[182:185], v251 offset:18432\n"
    "v_cvt_pk_bf16_f32 v45, v102, v103\n"
    "v_mfma_f32_16x16x32_bf16 v[96:99], v[154:157], a[200:203], v[96:99]\n"
    "ds_read_b128 v[178:181], v254 offset:20480\n"
    "ds_read_b128 v[174:177], v251 offset:20480\n"
    "v_mfma_f32_16x16x32_bf16 v[96:99], v[150:153], a[204:207], v[96:99]\n"
    "v_exp_f32 v77, v77\n"
    "s_waitcnt lgkmcnt(4)\n"
    "v_mfma_f32_16x16x32_bf16 v[96:99], v[166:169], a[208:211], v[96:99]\n"
    "v_cvt_pk_bf16_f32 v52, v104, v105\n"
    "v_add_f32 v147, v147, v76\n"
    "v_mfma_f32_16x16x32_bf16 v[96:99], v[170:173], a[212:215], v[96:99]\n"
    "v_cvt_pk_bf16_f32 v53, v106, v107\n"
    "v_add_f32 v147, v147, v77\n"
    "v_mfma_f32_16x16x32_bf16 v[100:103], v[170:173], a[236:239], v[132:135]\n"
    "v_exp_f32 v78, v78\n"
    "v_mfma_f32_16x16x32_bf16 v[100:103], v[166:169], a[232:235], v[100:103]\n"
    "v_add_f32 v147, v147, v78\n"
    "v_cvt_f32_i32 v221, v221\n"
    "v_mfma_f32_16x16x32_bf16 v[100:103], v[150:153], a[228:231], v[100:103]\n"
    "v_cvt_pk_bf16_f32 v60, v108, v109\n"
    "v_fma_f32 v218, v218, v232, -v207\n"
    "v_mfma_f32_16x16x32_bf16 v[100:103], v[154:157], a[224:227], v[100:103]\n"
    "v_exp_f32 v79, v79\n"
    "v_mfma_f32_16x16x32_bf16 v[100:103], v[158:161], a[220:223], v[100:103]\n"
    "v_add_f32 v147, v147, v79\n"
    "v_fma_f32 v220, v220, v233, -v211\n"
    "v_mfma_f32_16x16x32_bf16 v[100:103], v[162:165], a[216:219], v[100:103]\n"
    "v_cvt_pk_bf16_f32 v61, v110, v111\n"
    "v_mfma_f32_16x16x32_bf16 v[104:107], v[162:165], a[240:243], v[136:139]\n"
    "v_exp_f32 v80, v80\n"
    "v_mfma_f32_16x16x32_bf16 v[104:107], v[158:161], a[244:247], v[104:107]\n"
    "v_add_f32 v144, v144, v80\n"
    "v_fma_f32 v219, v219, v232, -v207\n"
    "v_mfma_f32_16x16x32_bf16 v[104:107], v[154:157], a[248:251], v[104:107]\n"
    "v_cvt_pk_bf16_f32 v38, v112, v113\n"
    "v_fma_f32 v221, v221, v233, -v211\n"
    "v_mfma_f32_16x16x32_bf16 v[104:107], v[150:153], a[252:255], v[104:107]\n"
    "v_exp_f32 v81, v81\n"
    "v_mfma_f32_16x16x32_bf16 v[104:107], v[166:169], v[0:3], v[104:107]\n"
    "v_add_f32 v144, v144, v81\n"
    "v_cvt_pk_bf16_f32 v239, v218, v220\n"
    "v_mfma_f32_16x16x32_bf16 v[104:107], v[170:173], v[4:7], v[104:107]\n"
    "s_mov_b32 m0, s74\n"
    "v_cvt_pk_bf16_f32 v39, v114, v115\n"
    "v_mfma_f32_16x16x32_bf16 v[108:111], v[170:173], v[28:31], v[140:143]\n"
    "v_exp_f32 v82, v82\n"
    "v_mfma_f32_16x16x32_bf16 v[108:111], v[166:169], v[24:27], v[108:111]\n"
    "ds_read_b128 v[170:173], v254 offset:22528\n"
    "ds_read_b128 v[166:169], v251 offset:22528\n"
    "v_mfma_f32_16x16x32_bf16 v[108:111], v[150:153], v[20:23], v[108:111]\n"
    "s_add_u32 s74, s74, s73\n"
    "v_cvt_pk_bf16_f32 v46, v116, v117\n"
    "v_mfma_f32_16x16x32_bf16 v[108:111], v[154:157], v[16:19], v[108:111]\n"
    "v_exp_f32 v83, v83\n"
    "v_mfma_f32_16x16x32_bf16 v[108:111], v[158:161], v[12:15], v[108:111]\n"
    "s_cmp_ge_u32 s74, s75\n"
    "s_cselect_b32 s73, 0xfffe8000, s76\n"
    "v_mfma_f32_16x16x32_bf16 v[108:111], v[162:165], v[8:11], v[108:111]\n"
    "ds_read_b128 v[150:153], v148 offset:24576\n"
    "s_waitcnt lgkmcnt(3)\n"
    "v_mfma_f32_16x16x32_bf16 v[112:115], v[186:189], a[192:195], v[128:131]\n"
    "v_exp_f32 v84, v84\n"
    "v_mfma_f32_16x16x32_bf16 v[112:115], v[182:185], a[196:199], v[112:115]\n"
    "ds_read_b128 v[154:157], v148 offset:36864\n"
    "v_cvt_pk_bf16_f32 v47, v118, v119\n"
    "v_mfma_f32_16x16x32_bf16 v[112:115], v[178:181], a[200:203], v[112:115]\n"
    "buffer_load_dwordx4 v149, s[36:39], s72 offen offset:0 sc1 lds\n"
    "v_mfma_f32_16x16x32_bf16 v[112:115], v[174:177], a[204:207], v[112:115]\n"
    "v_exp_f32 v85, v85\n"
    "s_waitcnt lgkmcnt(2)\n"
    "v_mfma_f32_16x16x32_bf16 v[112:115], v[170:173], a[208:211], v[112:115]\n"
    "v_cvt_pk_bf16_f32 v54, v120, v121\n"
    "v_add_f32 v144, v144, v82\n"
    "v_mfma_f32_16x16x32_bf16 v[112:115], v[166:169], a[212:215], v[112:115]\n"
    "v_cvt_pk_bf16_f32 v55, v122, v123\n"
    "v_add_f32 v144, v144, v83\n"
    "v_mfma_f32_16x16x32_bf16 v[116:119], v[166:169], a[236:239], v[132:135]\n"
    "v_exp_f32 v86, v86\n"
    "v_mfma_f32_16x16x32_bf16 v[116:119], v[170:173], a[232:235], v[116:119]\n"
    "buffer_load_dwordx4 v149, s[36:39], s72 offen offset:1024 sc1 lds\n"
    "v_mfma_f32_16x16x32_bf16 v[116:119], v[174:177], a[228:231], v[116:119]\n"
    "ds_read_b128 v[158:161], v148 offset:25600\n"
    "ds_read_b128 v[162:165], v148 offset:37888\n"
    "v_mfma_f32_16x16x32_bf16 v[116:119], v[178:181], a[224:227], v[116:119]\n"
    "v_exp_f32 v87, v87\n"
    "v_mfma_f32_16x16x32_bf16 v[116:119], v[182:185], a[220:223], v[116:119]\n"
    "v_cvt_pk_bf16_f32 v62, v124, v125\n"
    "v_add_f32 v145, v145, v84\n"
    "v_mfma_f32_16x16x32_bf16 v[116:119], v[186:189], a[216:219], v[116:119]\n"
    "v_cvt_pk_bf16_f32 v63, v126, v127\n"
    "v_add_f32 v145, v145, v85\n"
    "v_mfma_f32_16x16x32_bf16 v[120:123], v[186:189], a[240:243], v[136:139]\n"
    "v_exp_f32 v88, v88\n"
    "v_mfma_f32_16x16x32_bf16 v[120:123], v[182:185], a[244:247], v[120:123]\n"
    "buffer_load_dwordx4 v149, s[36:39], s72 offen offset:2048 sc1 lds\n"
    "v_mfma_f32_16x16x32_bf16 v[120:123], v[178:181], a[248:251], v[120:123]\n"
    "v_add_f32 v145, v145, v86\n"
    "v_add_f32 v145, v145, v87\n"
    "v_mfma_f32_16x16x32_bf16 v[120:123], v[174:177], a[252:255], v[120:123]\n"
    "v_exp_f32 v89, v89\n"
    "v_mfma_f32_16x16x32_bf16 v[120:123], v[170:173], v[0:3], v[120:123]\n"
    "v_add_f32 v146, v146, v88\n"
    "v_add_f32 v146, v146, v89\n"
    "v_mfma_f32_16x16x32_bf16 v[120:123], v[166:169], v[4:7], v[120:123]\n"
    "v_cvt_pk_bf16_f32 v243, v219, v221\n"
    "v_cvt_f32_i32 v222, v222\n"
    "v_mfma_f32_16x16x32_bf16 v[124:127], v[166:169], v[28:31], v[140:143]\n"
    "v_exp_f32 v90, v90\n"
    "v_mfma_f32_16x16x32_bf16 v[124:127], v[170:173], v[24:27], v[124:127]\n"
    "ds_read_b128 v[166:169], v148 offset:26624\n"
    "ds_read_b128 v[170:173], v148 offset:38912\n"
    "v_mfma_f32_16x16x32_bf16 v[124:127], v[174:177], v[20:23], v[124:127]\n"
    "buffer_load_dwordx4 v149, s[36:39], s72 offen offset:3072 sc1 lds\n"
    "v_mfma_f32_16x16x32_bf16 v[124:127], v[178:181], v[16:19], v[124:127]\n"
    "v_exp_f32 v91, v91\n"
    "v_mfma_f32_16x16x32_bf16 v[124:127], v[182:185], v[12:15], v[124:127]\n"
    "s_add_u32 s72, s72, 4096\n"
    "v_add_f32 v146, v146, v90\n"
    "v_mfma_f32_16x16x32_bf16 v[124:127], v[186:189], v[8:11], v[124:127]\n"
    "ds_read_b128 v[174:177], v148 offset:27648\n"
    "s_waitcnt lgkmcnt(3)\n"
    "v_mfma_f32_16x16x32_bf16 a[0:3], v[32:35], v[150:153], a[0:3]\n"
    "v_exp_f32 v92, v92\n"
    "v_mfma_f32_16x16x32_bf16 a[0:3], v[36:39], v[154:157], a[0:3]\n"
    "ds_read_b128 v[178:181], v148 offset:39936\n"
    "s_add_u32 m0, m0, 4096\n"
    "v_mfma_f32_16x16x32_bf16 a[4:7], v[44:47], v[154:157], a[4:7]\n"
    "v_add_f32 v146, v146, v91\n"
    "v_add_f32 v147, v147, v92\n"
    "v_mfma_f32_16x16x32_bf16 a[4:7], v[40:43], v[150:153], a[4:7]\n"
    "v_exp_f32 v93, v93\n"
    "v_mfma_f32_16x16x32_bf16 a[8:11], v[48:51], v[150:153], a[8:11]\n"
    "v_add_f32 v147, v147, v93\n"
    "v_cvt_f32_i32 v224, v224\n"
    "v_mfma_f32_16x16x32_bf16 a[8:11], v[52:55], v[154:157], a[8:11]\n"
    "v_cvt_f32_i32 v223, v223\n"
    "v_cvt_f32_i32 v225, v225\n"
    "v_mfma_f32_16x16x32_bf16 a[12:15], v[60:63], v[154:157], a[12:15]\n"
    "v_exp_f32 v94, v94\n"
    "v_mfma_f32_16x16x32_bf16 a[12:15], v[56:59], v[150:153], a[12:15]\n"
    "buffer_load_dwordx4 v149, s[36:39], s72 offen offset:0 sc1 lds\n"
    "v_mfma_f32_16x16x32_bf16 a[16:19], v[32:35], v[158:161], a[16:19]\n"
    "ds_read_b128 v[182:185], v148 offset:28672\n"
    "ds_read_b128 v[186:189], v148 offset:40960\n"
    "v_mfma_f32_16x16x32_bf16 a[16:19], v[36:39], v[162:165], a[16:19]\n"
    "v_exp_f32 v95, v95\n"
    "v_mfma_f32_16x16x32_bf16 a[20:23], v[44:47], v[162:165], a[20:23]\n"
    "v_add_f32 v147, v147, v94\n"
    "v_add_f32 v147, v147, v95\n"
    "v_mfma_f32_16x16x32_bf16 a[20:23], v[40:43], v[158:161], a[20:23]\n"
    "v_fma_f32 v222, v222, v234, -v208\n"
    "v_mfma_f32_16x16x32_bf16 a[24:27], v[48:51], v[158:161], a[24:27]\n"
    "v_exp_f32 v96, v96\n"
    "v_mfma_f32_16x16x32_bf16 a[24:27], v[52:55], v[162:165], a[24:27]\n"
    "buffer_load_dwordx4 v149, s[36:39], s72 offen offset:1024 sc1 lds\n"
    "v_mfma_f32_16x16x32_bf16 a[28:31], v[60:63], v[162:165], a[28:31]\n"
    "v_add_f32 v144, v144, v96\n"
    "v_fma_f32 v224, v224, v235, -v212\n"
    "v_mfma_f32_16x16x32_bf16 a[28:31], v[56:59], v[158:161], a[28:31]\n"
    "v_exp_f32 v97, v97\n"
    "s_waitcnt lgkmcnt(0)\n"
    "v_mfma_f32_16x16x32_bf16 a[32:35], v[32:35], v[166:169], a[32:35]\n"
    "ds_read_b128 v[150:153], v148 offset:29696\n"
    "v_add_f32 v144, v144, v97\n"
    "v_mfma_f32_16x16x32_bf16 a[32:35], v[36:39], v[170:173], a[32:35]\n"
    "ds_read_b128 v[154:157], v148 offset:41984\n"
    "v_fma_f32 v223, v223, v234, -v208\n"
    "v_mfma_f32_16x16x32_bf16 a[36:39], v[44:47], v[170:173], a[36:39]\n"
    "v_exp_f32 v98, v98\n"
    "v_mfma_f32_16x16x32_bf16 a[36:39], v[40:43], v[166:169], a[36:39]\n"
    "buffer_load_dwordx4 v149, s[36:39], s72 offen offset:2048 sc1 lds\n"
    "v_mfma_f32_16x16x32_bf16 a[40:43], v[48:51], v[166:169], a[40:43]\n"
    "v_add_f32 v144, v144, v98\n"
    "v_fma_f32 v225, v225, v235, -v212\n"
    "v_mfma_f32_16x16x32_bf16 a[40:43], v[52:55], v[170:173], a[40:43]\n"
    "v_exp_f32 v99, v99\n"
    "v_mfma_f32_16x16x32_bf16 a[44:47], v[60:63], v[170:173], a[44:47]\n"
    "v_add_f32 v144, v144, v99\n"
    "v_cvt_pk_bf16_f32 v240, v222, v224\n"
    "v_mfma_f32_16x16x32_bf16 a[44:47], v[56:59], v[166:169], a[44:47]\n"
    "ds_read_b128 v[158:161], v148 offset:30720\n"
    "v_cvt_pk_bf16_f32 v244, v223, v225\n"
    "v_mfma_f32_16x16x32_bf16 a[48:51], v[32:35], v[174:177], a[48:51]\n"
    "v_exp_f32 v100, v100\n"
    "v_mfma_f32_16x16x32_bf16 a[48:51], v[36:39], v[178:181], a[48:51]\n"
    "ds_read_b128 v[162:165], v148 offset:43008\n"
    "v_add_f32 v145, v145, v100\n"
    "v_mfma_f32_16x16x32_bf16 a[52:55], v[44:47], v[178:181], a[52:55]\n"
    "buffer_load_dwordx4 v149, s[36:39], s72 offen offset:3072 sc1 lds\n"
    "v_mfma_f32_16x16x32_bf16 a[52:55], v[40:43], v[174:177], a[52:55]\n"
    "v_exp_f32 v101, v101\n"
    "v_mfma_f32_16x16x32_bf16 a[56:59], v[48:51], v[174:177], a[56:59]\n"
    "s_add_u32 s72, s72, 4096\n"
    "v_add_f32 v145, v145, v101\n"
    "v_mfma_f32_16x16x32_bf16 a[56:59], v[52:55], v[178:181], a[56:59]\n"
    "s_add_u32 m0, m0, 4096\n"
    "v_cvt_f32_i32 v226, v226\n"
    "v_mfma_f32_16x16x32_bf16 a[60:63], v[60:63], v[178:181], a[60:63]\n"
    "v_exp_f32 v102, v102\n"
    "v_mfma_f32_16x16x32_bf16 a[60:63], v[56:59], v[174:177], a[60:63]\n"
    "v_add_f32 v145, v145, v102\n"
    "v_cvt_f32_i32 v228, v228\n"
    "v_mfma_f32_16x16x32_bf16 a[64:67], v[32:35], v[182:185], a[64:67]\n"
    "ds_read_b128 v[166:169], v148 offset:31744\n"
    "ds_read_b128 v[170:173], v148 offset:44032\n"
    "v_mfma_f32_16x16x32_bf16 a[64:67], v[36:39], v[186:189], a[64:67]\n"
    "v_exp_f32 v103, v103\n"
    "v_mfma_f32_16x16x32_bf16 a[68:71], v[44:47], v[186:189], a[68:71]\n"
    "v_add_f32 v145, v145, v103\n"
    "v_mfma_f32_16x16x32_bf16 a[68:71], v[40:43], v[182:185], a[68:71]\n"
    "buffer_load_dwordx4 v149, s[36:39], s72 offen offset:0 sc1 lds\n"
    "v_mfma_f32_16x16x32_bf16 a[72:75], v[48:51], v[182:185], a[72:75]\n"
    "v_exp_f32 v104, v104\n"
    "v_mfma_f32_16x16x32_bf16 a[72:75], v[52:55], v[186:189], a[72:75]\n"
    "v_add_f32 v146, v146, v104\n"
    "v_cvt_f32_i32 v227, v227\n"
    "v_mfma_f32_16x16x32_bf16 a[76:79], v[60:63], v[186:189], a[76:79]\n"
    "v_cvt_f32_i32 v229, v229\n"
    "v_fma_f32 v226, v226, v236, -v209\n"
    "v_mfma_f32_16x16x32_bf16 a[76:79], v[56:59], v[182:185], a[76:79]\n"
    "v_exp_f32 v105, v105\n"
    "s_waitcnt lgkmcnt(0)\n"
    "v_mfma_f32_16x16x32_bf16 a[80:83], v[32:35], v[150:153], a[80:83]\n"
    "v_add_f32 v146, v146, v105\n"
    "v_fma_f32 v228, v228, v237, -v213\n"
    "v_mfma_f32_16x16x32_bf16 a[80:83], v[36:39], v[154:157], a[80:83]\n"
    "buffer_load_dwordx4 v149, s[36:39], s72 offen offset:1024 sc1 lds\n"
    "v_mfma_f32_16x16x32_bf16 a[84:87], v[44:47], v[154:157], a[84:87]\n"
    "v_exp_f32 v106, v106\n"
    "v_mfma_f32_16x16x32_bf16 a[84:87], v[40:43], v[150:153], a[84:87]\n"
    "v_add_f32 v146, v146, v106\n"
    "v_fma_f32 v227, v227, v236, -v209\n"
    "v_mfma_f32_16x16x32_bf16 a[88:91], v[48:51], v[150:153], a[88:91]\n"
    "v_fma_f32 v229, v229, v237, -v213\n"
    "v_cvt_pk_bf16_f32 v241, v226, v228\n"
    "v_mfma_f32_16x16x32_bf16 a[88:91], v[52:55], v[154:157], a[88:91]\n"
    "v_exp_f32 v107, v107\n"
    "v_mfma_f32_16x16x32_bf16 a[92:95], v[60:63], v[154:157], a[92:95]\n"
    "buffer_load_dwordx4 v149, s[36:39], s72 offen offset:2048 sc1 lds\n"
    "v_mfma_f32_16x16x32_bf16 a[92:95], v[56:59], v[150:153], a[92:95]\n"
    "ds_read_b128 v[150:153], v148 offset:32768\n"
    "ds_read_b128 v[154:157], v148 offset:45056\n"
    "v_mfma_f32_16x16x32_bf16 a[96:99], v[32:35], v[158:161], a[96:99]\n"
    "v_exp_f32 v108, v108\n"
    "v_mfma_f32_16x16x32_bf16 a[96:99], v[36:39], v[162:165], a[96:99]\n"
    "v_add_f32 v146, v146, v107\n"
    "v_add_f32 v147, v147, v108\n"
    "v_mfma_f32_16x16x32_bf16 a[100:103], v[44:47], v[162:165], a[100:103]\n"
    "v_cvt_pk_bf16_f32 v245, v227, v229\n"
    "v_mfma_f32_16x16x32_bf16 a[100:103], v[40:43], v[158:161], a[100:103]\n"
    "v_exp_f32 v109, v109\n"
    "v_mfma_f32_16x16x32_bf16 a[104:107], v[48:51], v[158:161], a[104:107]\n"
    "buffer_load_dwordx4 v149, s[36:39], s72 offen offset:3072 sc1 lds\n"
    "v_mfma_f32_16x16x32_bf16 a[104:107], v[52:55], v[162:165], a[104:107]\n"
    "s_add_u32 s72, s72, 16384\n"
    "v_add_f32 v147, v147, v109\n"
    "v_mfma_f32_16x16x32_bf16 a[108:111], v[60:63], v[162:165], a[108:111]\n"
    "v_exp_f32 v110, v110\n"
    "v_mfma_f32_16x16x32_bf16 a[108:111], v[56:59], v[158:161], a[108:111]\n"
    "ds_read_b128 v[158:161], v148 offset:33792\n"
    "ds_read_b128 v[162:165], v148 offset:46080\n"
    "v_mfma_f32_16x16x32_bf16 a[112:115], v[32:35], v[166:169], a[112:115]\n"
    "ds_read_b128 v[174:177], v148 offset:34816\n"
    "ds_read_b128 v[178:181], v148 offset:47104\n"
    "v_mfma_f32_16x16x32_bf16 a[112:115], v[36:39], v[170:173], a[112:115]\n"
    "v_exp_f32 v111, v111\n"
    "v_mfma_f32_16x16x32_bf16 a[116:119], v[44:47], v[170:173], a[116:119]\n"
    "ds_read_b128 v[182:185], v148 offset:35840\n"
    "v_add_f32 v147, v147, v110\n"
    "v_mfma_f32_16x16x32_bf16 a[116:119], v[40:43], v[166:169], a[116:119]\n"
    "ds_read_b128 v[186:189], v148 offset:48128\n"
    "v_add_f32 v147, v147, v111\n"
    "v_mfma_f32_16x16x32_bf16 a[120:123], v[48:51], v[166:169], a[120:123]\n"
    "v_exp_f32 v112, v112\n"
    "v_mfma_f32_16x16x32_bf16 a[120:123], v[52:55], v[170:173], a[120:123]\n"
    "buffer_store_dwordx4 v[238:241], v149, s[52:55], s85 offen offset:2048 sc1\n"
    "v_mfma_f32_16x16x32_bf16 a[124:127], v[60:63], v[170:173], a[124:127]\n"
    "buffer_store_dwordx4 v[242:245], v149, s[52:55], s85 offen offset:3072 sc1\n"
    "v_mfma_f32_16x16x32_bf16 a[124:127], v[56:59], v[166:169], a[124:127]\n"
    "v_exp_f32 v113, v113\n"
    "s_waitcnt lgkmcnt(2)\n"
    "v_mfma_f32_16x16x32_bf16 a[128:131], v[32:35], v[150:153], a[128:131]\n"
    "v_add_f32 v144, v144, v112\n"
    "v_add_f32 v144, v144, v113\n"
    "v_mfma_f32_16x16x32_bf16 a[128:131], v[36:39], v[154:157], a[128:131]\n"
    "v_mfma_f32_16x16x32_bf16 a[132:135], v[44:47], v[154:157], a[132:135]\n"
    "v_exp_f32 v114, v114\n"
    "v_mfma_f32_16x16x32_bf16 a[132:135], v[40:43], v[150:153], a[132:135]\n"
    "s_waitcnt vmcnt(14) lgkmcnt(0)\n"
    "s_barrier\n"
    "v_mfma_f32_16x16x32_bf16 a[136:139], v[48:51], v[150:153], a[136:139]\n"
    "v_add_u32 v254, s73, v254\n"
    "v_add_u32 v251, s73, v251\n"
    "v_mfma_f32_16x16x32_bf16 a[136:139], v[52:55], v[154:157], a[136:139]\n"
    "v_exp_f32 v115, v115\n"
    "v_mfma_f32_16x16x32_bf16 a[140:143], v[60:63], v[154:157], a[140:143]\n"
    "v_add_f32 v144, v144, v114\n"
    "v_add_f32 v144, v144, v115\n"
    "v_mfma_f32_16x16x32_bf16 a[140:143], v[56:59], v[150:153], a[140:143]\n"
    "ds_read_b128 v[150:153], v254 offset:0\n"
    "v_cvt_f32_i32 v190, v190\n"
    "v_mfma_f32_16x16x32_bf16 a[144:147], v[32:35], v[158:161], a[144:147]\n"
    "v_exp_f32 v116, v116\n"
    "v_mfma_f32_16x16x32_bf16 a[144:147], v[36:39], v[162:165], a[144:147]\n"
    "ds_read_b128 v[154:157], v251 offset:0\n"
    "v_add_f32 v145, v145, v116\n"
    "v_mfma_f32_16x16x32_bf16 a[148:151], v[44:47], v[162:165], a[148:151]\n"
    "v_cvt_f32_i32 v192, v192\n"
    "v_mfma_f32_16x16x32_bf16 a[148:151], v[40:43], v[158:161], a[148:151]\n"
    "v_exp_f32 v117, v117\n"
    "v_mfma_f32_16x16x32_bf16 a[152:155], v[48:51], v[158:161], a[152:155]\n"
    "v_add_f32 v145, v145, v117\n"
    "v_cvt_f32_i32 v191, v191\n"
    "v_mfma_f32_16x16x32_bf16 a[152:155], v[52:55], v[162:165], a[152:155]\n"
    "v_cvt_f32_i32 v193, v193\n"
    "v_mfma_f32_16x16x32_bf16 a[156:159], v[60:63], v[162:165], a[156:159]\n"
    "v_exp_f32 v118, v118\n"
    "v_mfma_f32_16x16x32_bf16 a[156:159], v[56:59], v[158:161], a[156:159]\n"
    "ds_read_b128 v[158:161], v254 offset:2048\n"
    "ds_read_b128 v[162:165], v251 offset:2048\n"
    "v_mfma_f32_16x16x32_bf16 a[160:163], v[32:35], v[174:177], a[160:163]\n"
    "v_add_u32 v148, s73, v148\n"
    "ds_read_b128 v[166:169], v254 offset:4096\n"
    "v_mfma_f32_16x16x32_bf16 a[160:163], v[36:39], v[178:181], a[160:163]\n"
    "v_exp_f32 v119, v119\n"
    "v_mfma_f32_16x16x32_bf16 a[164:167], v[44:47], v[178:181], a[164:167]\n"
    "ds_read_b128 v[170:173], v251 offset:4096\n"
    "v_add_f32 v145, v145, v118\n"
    "v_mfma_f32_16x16x32_bf16 a[164:167], v[40:43], v[174:177], a[164:167]\n"
    "v_add_f32 v145, v145, v119\n"
    "v_fma_f32 v190, v190, v230, -v206\n"
    "v_mfma_f32_16x16x32_bf16 a[168:171], v[48:51], v[174:177], a[168:171]\n"
    "v_exp_f32 v120, v120\n"
    "v_mfma_f32_16x16x32_bf16 a[168:171], v[52:55], v[178:181], a[168:171]\n"
    "v_add_f32 v146, v146, v120\n"
    "v_fma_f32 v192, v192, v231, -v210\n"
    "v_mfma_f32_16x16x32_bf16 a[172:175], v[60:63], v[178:181], a[172:175]\n"
    "v_fma_f32 v191, v191, v230, -v206\n"
    "v_mfma_f32_16x16x32_bf16 a[172:175], v[56:59], v[174:177], a[172:175]\n"
    "v_exp_f32 v121, v121\n"
    "v_mfma_f32_16x16x32_bf16 a[176:179], v[32:35], v[182:185], a[176:179]\n"
    "v_cvt_pk_bf16_f32 v32, v64, v65\n"
    "v_add_f32 v146, v146, v121\n"
    "v_mfma_f32_16x16x32_bf16 a[176:179], v[36:39], v[186:189], a[176:179]\n"
    "v_cvt_pk_bf16_f32 v33, v66, v67\n"
    "v_mfma_f32_16x16x32_bf16 a[180:183], v[44:47], v[186:189], a[180:183]\n"
    "v_exp_f32 v122, v122\n"
    "v_mfma_f32_16x16x32_bf16 a[180:183], v[40:43], v[182:185], a[180:183]\n"
    "v_add_f32 v146, v146, v122\n"
    "v_fma_f32 v193, v193, v231, -v210\n"
    "v_mfma_f32_16x16x32_bf16 a[184:187], v[48:51], v[182:185], a[184:187]\n"
    "v_cvt_pk_bf16_f32 v40, v68, v69\n"
    "v_cvt_pk_bf16_f32 v238, v190, v192\n"
    "v_mfma_f32_16x16x32_bf16 a[184:187], v[52:55], v[186:189], a[184:187]\n"
    "v_exp_f32 v123, v123\n"
    "v_mfma_f32_16x16x32_bf16 a[188:191], v[60:63], v[186:189], a[188:191]\n"
    "v_add_f32 v146, v146, v123\n"
    "v_mfma_f32_16x16x32_bf16 a[188:191], v[56:59], v[182:185], a[188:191]\n"
    "ds_read_b128 v[174:177], v254 offset:6144\n"
    "s_waitcnt lgkmcnt(1)\n"
    "v_mfma_f32_16x16x32_bf16 v[64:67], v[150:153], a[192:195], v[128:131]\n"
    "v_exp_f32 v124, v124\n"
    "v_mfma_f32_16x16x32_bf16 v[64:67], v[154:157], a[196:199], v[64:67]\n"
    "ds_read_b128 v[178:181], v251 offset:6144\n"
    "v_cvt_pk_bf16_f32 v41, v70, v71\n"
    "v_mfma_f32_16x16x32_bf16 v[64:67], v[158:161], a[200:203], v[64:67]\n"
    "ds_read_b128 v[182:185], v254 offset:8192\n"
    "ds_read_b128 v[186:189], v251 offset:8192\n"
    "v_mfma_f32_16x16x32_bf16 v[64:67], v[162:165], a[204:207], v[64:67]\n"
    "v_exp_f32 v125, v125\n"
    "v_mfma_f32_16x16x32_bf16 v[64:67], v[166:169], a[208:211], v[64:67]\n"
    "v_cvt_pk_bf16_f32 v48, v72, v73\n"
    "v_add_f32 v147, v147, v124\n"
    "v_mfma_f32_16x16x32_bf16 v[64:67], v[170:173], a[212:215], v[64:67]\n"
    "v_cvt_pk_bf16_f32 v49, v74, v75\n"
    "v_add_f32 v147, v147, v125\n"
    "v_mfma_f32_16x16x32_bf16 v[68:71], v[170:173], a[236:239], v[132:135]\n"
    "v_exp_f32 v126, v126\n"
    "v_mfma_f32_16x16x32_bf16 v[68:71], v[166:169], a[232:235], v[68:71]\n"
    "v_add_f32 v147, v147, v126\n"
    "v_cvt_pk_bf16_f32 v242, v191, v193\n"
    "v_mfma_f32_16x16x32_bf16 v[68:71], v[162:165], a[228:231], v[68:71]\n"
    "v_cvt_pk_bf16_f32 v56, v76, v77\n"
    "v_cvt_f32_i32 v194, v194\n"
    "v_mfma_f32_16x16x32_bf16 v[68:71], v[158:161], a[224:227], v[68:71]\n"
    "v_exp_f32 v127, v127\n"
    "v_mfma_f32_16x16x32_bf16 v[68:71], v[154:157], a[220:223], v[68:71]\n"
    "v_add_f32 v147, v147, v127\n"
    "v_mfma_f32_16x16x32_bf16 v[68:71], v[150:153], a[216:219], v[68:71]\n"
    "v_cvt_pk_bf16_f32 v57, v78, v79\n"
    "v_cvt_f32_i32 v196, v196\n"
    "v_mfma_f32_16x16x32_bf16 v[72:75], v[150:153], a[240:243], v[136:139]\n"
    "v_exp_f32 v64, v64\n"
    "v_mfma_f32_16x16x32_bf16 v[72:75], v[154:157], a[244:247], v[72:75]\n"
    "v_add_f32 v144, v144, v64\n"
    "v_cvt_f32_i32 v195, v195\n"
    "v_mfma_f32_16x16x32_bf16 v[72:75], v[158:161], a[248:251], v[72:75]\n"
    "v_cvt_pk_bf16_f32 v34, v80, v81\n"
    "v_mfma_f32_16x16x32_bf16 v[72:75], v[162:165], a[252:255], v[72:75]\n"
    "v_exp_f32 v65, v65\n"
    "v_mfma_f32_16x16x32_bf16 v[72:75], v[166:169], v[0:3], v[72:75]\n"
    "v_add_f32 v144, v144, v65\n"
    "v_cvt_f32_i32 v197, v197\n"
    "v_mfma_f32_16x16x32_bf16 v[72:75], v[170:173], v[4:7], v[72:75]\n"
    "v_cvt_pk_bf16_f32 v35, v82, v83\n"
    "v_mfma_f32_16x16x32_bf16 v[76:79], v[170:173], v[28:31], v[140:143]\n"
    "v_exp_f32 v66, v66\n"
    "v_mfma_f32_16x16x32_bf16 v[76:79], v[166:169], v[24:27], v[76:79]\n"
    "ds_read_b128 v[170:173], v254 offset:10240\n"
    "ds_read_b128 v[166:169], v251 offset:10240\n"
    "v_mfma_f32_16x16x32_bf16 v[76:79], v[162:165], v[20:23], v[76:79]\n"
    "v_cvt_pk_bf16_f32 v42, v84, v85\n"
    "v_add_f32 v144, v144, v66\n"
    "v_mfma_f32_16x16x32_bf16 v[76:79], v[158:161], v[16:19], v[76:79]\n"
    "v_exp_f32 v67, v67\n"
    "v_mfma_f32_16x16x32_bf16 v[76:79], v[154:157], v[12:15], v[76:79]\n"
    "v_add_f32 v144, v144, v67\n"
    "v_fma_f32 v194, v194, v232, -v207\n"
    "v_mfma_f32_16x16x32_bf16 v[76:79], v[150:153], v[8:11], v[76:79]\n"
    "ds_read_b128 v[162:165], v254 offset:12288\n"
    "s_waitcnt lgkmcnt(3)\n"
    "v_mfma_f32_16x16x32_bf16 v[80:83], v[174:177], a[192:195], v[128:131]\n"
    "v_exp_f32 v68, v68\n"
    "v_mfma_f32_16x16x32_bf16 v[80:83], v[178:181], a[196:199], v[80:83]\n"
    "ds_read_b128 v[158:161], v251 offset:12288\n"
    "v_cvt_pk_bf16_f32 v43, v86, v87\n"
    "v_mfma_f32_16x16x32_bf16 v[80:83], v[182:185], a[200:203], v[80:83]\n"
    "ds_read_b128 v[154:157], v254 offset:14336\n"
    "ds_read_b128 v[150:153], v251 offset:14336\n"
    "v_mfma_f32_16x16x32_bf16 v[80:83], v[186:189], a[204:207], v[80:83]\n"
    "v_exp_f32 v69, v69\n"
    "s_waitcnt lgkmcnt(4)\n"
    "v_mfma_f32_16x16x32_bf16 v[80:83], v[170:173], a[208:211], v[80:83]\n"
    "v_cvt_pk_bf16_f32 v50, v88, v89\n"
    "v_add_f32 v145, v145, v68\n"
    "v_mfma_f32_16x16x32_bf16 v[80:83], v[166:169], a[212:215], v[80:83]\n"
    "v_cvt_pk_bf16_f32 v51, v90, v91\n"
    "v_add_f32 v145, v145, v69\n"
    "v_mfma_f32_16x16x32_bf16 v[84:87], v[166:169], a[236:239], v[132:135]\n"
    "v_exp_f32 v70, v70\n"
    "v_mfma_f32_16x16x32_bf16 v[84:87], v[170:173], a[232:235], v[84:87]\n"
    "v_add_f32 v145, v145, v70\n"
    "v_fma_f32 v196, v196, v233, -v211\n"
    "v_mfma_f32_16x16x32_bf16 v[84:87], v[186:189], a[228:231], v[84:87]\n"
    "v_cvt_pk_bf16_f32 v58, v92, v93\n"
    "v_mfma_f32_16x16x32_bf16 v[84:87], v[182:185], a[224:227], v[84:87]\n"
    "v_exp_f32 v71, v71\n"
    "v_mfma_f32_16x16x32_bf16 v[84:87], v[178:181], a[220:223], v[84:87]\n"
    "v_add_f32 v145, v145, v71\n"
    "v_fma_f32 v195, v195, v232, -v207\n"
    "v_mfma_f32_16x16x32_bf16 v[84:87], v[174:177], a[216:219], v[84:87]\n"
    "v_cvt_pk_bf16_f32 v59, v94, v95\n"
    "v_fma_f32 v197, v197, v233, -v211\n"
    "v_mfma_f32_16x16x32_bf16 v[88:91], v[174:177], a[240:243], v[136:139]\n"
    "v_exp_f32 v72, v72\n"
    "v_mfma_f32_16x16x32_bf16 v[88:91], v[178:181], a[244:247], v[88:91]\n"
    "v_add_f32 v146, v146, v72\n"
    "v_mfma_f32_16x16x32_bf16 v[88:91], v[182:185], a[248:251], v[88:91]\n"
    "v_cvt_pk_bf16_f32 v36, v96, v97\n"
    "v_cvt_pk_bf16_f32 v239, v194, v196\n"
    "v_mfma_f32_16x16x32_bf16 v[88:91], v[186:189], a[252:255], v[88:91]\n"
    "v_exp_f32 v73, v73\n"
    "v_mfma_f32_16x16x32_bf16 v[88:91], v[170:173], v[0:3], v[88:91]\n"
    "v_add_f32 v146, v146, v73\n"
    "v_cvt_pk_bf16_f32 v243, v195, v197\n"
    "v_mfma_f32_16x16x32_bf16 v[88:91], v[166:169], v[4:7], v[88:91]\n"
    "v_cvt_pk_bf16_f32 v37, v98, v99\n"
    "v_mfma_f32_16x16x32_bf16 v[92:95], v[166:169], v[28:31], v[140:143]\n"
    "v_exp_f32 v74, v74\n"
    "v_mfma_f32_16x16x32_bf16 v[92:95], v[170:173], v[24:27], v[92:95]\n"
    "ds_read_b128 v[166:169], v254 offset:16384\n"
    "ds_read_b128 v[170:173], v251 offset:16384\n"
    "v_mfma_f32_16x16x32_bf16 v[92:95], v[186:189], v[20:23], v[92:95]\n"
    "v_cvt_pk_bf16_f32 v44, v100, v101\n"
    "v_add_f32 v146, v146, v74\n"
    "v_mfma_f32_16x16x32_bf16 v[92:95], v[182:185], v[16:19], v[92:95]\n"
    "v_exp_f32 v75, v75\n"
    "v_mfma_f32_16x16x32_bf16 v[92:95], v[178:181], v[12:15], v[92:95]\n"
    "v_add_f32 v146, v146, v75\n"
    "v_cvt_f32_i32 v198, v198\n"
    "v_mfma_f32_16x16x32_bf16 v[92:95], v[174:177], v[8:11], v[92:95]\n"
    "ds_read_b128 v[186:189], v254 offset:18432\n"
    "s_waitcnt lgkmcnt(3)\n"
    "v_mfma_f32_16x16x32_bf16 v[96:99], v[162:165], a[192:195], v[128:131]\n"
    "v_exp_f32 v76, v76\n"
    "v_mfma_f32_16x16x32_bf16 v[96:99], v[158:161], a[196:199], v[96:99]\n"
    "ds_read_b128 v[182:185], v251 offset:18432\n"
    "v_cvt_pk_bf16_f32 v45, v102, v103\n"
    "v_mfma_f32_16x16x32_bf16 v[96:99], v[154:157], a[200:203], v[96:99]\n"
    "ds_read_b128 v[178:181], v254 offset:20480\n"
    "ds_read_b128 v[174:177], v251 offset:20480\n"
    "v_mfma_f32_16x16x32_bf16 v[96:99], v[150:153], a[204:207], v[96:99]\n"
    "v_exp_f32 v77, v77\n"
    "s_waitcnt lgkmcnt(4)\n"
    "v_mfma_f32_16x16x32_bf16 v[96:99], v[166:169], a[208:211], v[96:99]\n"
    "v_cvt_pk_bf16_f32 v52, v104, v105\n"
    "v_add_f32 v147, v147, v76\n"
    "v_mfma_f32_16x16x32_bf16 v[96:99], v[170:173], a[212:215], v[96:99]\n"
    "v_cvt_pk_bf16_f32 v53, v106, v107\n"
    "v_add_f32 v147, v147, v77\n"
    "v_mfma_f32_16x16x32_bf16 v[100:103], v[170:173], a[236:239], v[132:135]\n"
    "v_exp_f32 v78, v78\n"
    "v_mfma_f32_16x16x32_bf16 v[100:103], v[166:169], a[232:235], v[100:103]\n"
    "v_add_f32 v147, v147, v78\n"
    "v_mfma_f32_16x16x32_bf16 v[100:103], v[150:153], a[228:231], v[100:103]\n"
    "v_cvt_pk_bf16_f32 v60, v108, v109\n"
    "v_cvt_f32_i32 v200, v200\n"
    "v_mfma_f32_16x16x32_bf16 v[100:103], v[154:157], a[224:227], v[100:103]\n"
    "v_exp_f32 v79, v79\n"
    "v_mfma_f32_16x16x32_bf16 v[100:103], v[158:161], a[220:223], v[100:103]\n"
    "v_add_f32 v147, v147, v79\n"
    "v_cvt_f32_i32 v199, v199\n"
    "v_mfma_f32_16x16x32_bf16 v[100:103], v[162:165], a[216:219], v[100:103]\n"
    "v_cvt_pk_bf16_f32 v61, v110, v111\n"
    "v_mfma_f32_16x16x32_bf16 v[104:107], v[162:165], a[240:243], v[136:139]\n"
    "v_exp_f32 v80, v80\n"
    "v_mfma_f32_16x16x32_bf16 v[104:107], v[158:161], a[244:247], v[104:107]\n"
    "v_add_f32 v144, v144, v80\n"
    "v_cvt_f32_i32 v201, v201\n"
    "v_mfma_f32_16x16x32_bf16 v[104:107], v[154:157], a[248:251], v[104:107]\n"
    "v_cvt_pk_bf16_f32 v38, v112, v113\n"
    "v_fma_f32 v198, v198, v234, -v208\n"
    "v_mfma_f32_16x16x32_bf16 v[104:107], v[150:153], a[252:255], v[104:107]\n"
    "v_exp_f32 v81, v81\n"
    "v_mfma_f32_16x16x32_bf16 v[104:107], v[166:169], v[0:3], v[104:107]\n"
    "v_add_f32 v144, v144, v81\n"
    "v_mfma_f32_16x16x32_bf16 v[104:107], v[170:173], v[4:7], v[104:107]\n"
    "s_mov_b32 m0, s74\n"
    "v_cvt_pk_bf16_f32 v39, v114, v115\n"
    "v_mfma_f32_16x16x32_bf16 v[108:111], v[170:173], v[28:31], v[140:143]\n"
    "v_exp_f32 v82, v82\n"
    "v_mfma_f32_16x16x32_bf16 v[108:111], v[166:169], v[24:27], v[108:111]\n"
    "ds_read_b128 v[170:173], v254 offset:22528\n"
    "ds_read_b128 v[166:169], v251 offset:22528\n"
    "v_mfma_f32_16x16x32_bf16 v[108:111], v[150:153], v[20:23], v[108:111]\n"
    "s_add_u32 s74, s74, s73\n"
    "v_cvt_pk_bf16_f32 v46, v116, v117\n"
    "v_mfma_f32_16x16x32_bf16 v[108:111], v[154:157], v[16:19], v[108:111]\n"
    "v_exp_f32 v83, v83\n"
    "v_mfma_f32_16x16x32_bf16 v[108:111], v[158:161], v[12:15], v[108:111]\n"
    "s_cmp_ge_u32 s74, s75\n"
    "s_cselect_b32 s73, 0xfffe8000, s76\n"
    "v_mfma_f32_16x16x32_bf16 v[108:111], v[162:165], v[8:11], v[108:111]\n"
    "ds_read_b128 v[150:153], v148 offset:24576\n"
    "s_waitcnt lgkmcnt(3)\n"
    "v_mfma_f32_16x16x32_bf16 v[112:115], v[186:189], a[192:195], v[128:131]\n"
    "v_exp_f32 v84, v84\n"
    "v_mfma_f32_16x16x32_bf16 v[112:115], v[182:185], a[196:199], v[112:115]\n"
    "ds_read_b128 v[154:157], v148 offset:36864\n"
    "v_cvt_pk_bf16_f32 v47, v118, v119\n"
    "v_mfma_f32_16x16x32_bf16 v[112:115], v[178:181], a[200:203], v[112:115]\n"
    "buffer_load_dwordx4 v149, s[36:39], s72 offen offset:0 sc1 lds\n"
    "v_mfma_f32_16x16x32_bf16 v[112:115], v[174:177], a[204:207], v[112:115]\n"
    "v_exp_f32 v85, v85\n"
    "s_waitcnt lgkmcnt(2)\n"
    "v_mfma_f32_16x16x32_bf16 v[112:115], v[170:173], a[208:211], v[112:115]\n"
    "v_cvt_pk_bf16_f32 v54, v120, v121\n"
    "v_add_f32 v144, v144, v82\n"
    "v_mfma_f32_16x16x32_bf16 v[112:115], v[166:169], a[212:215], v[112:115]\n"
    "v_cvt_pk_bf16_f32 v55, v122, v123\n"
    "v_add_f32 v144, v144, v83\n"
    "v_mfma_f32_16x16x32_bf16 v[116:119], v[166:169], a[236:239], v[132:135]\n"
    "v_exp_f32 v86, v86\n"
    "v_mfma_f32_16x16x32_bf16 v[116:119], v[170:173], a[232:235], v[116:119]\n"
    "buffer_load_dwordx4 v149, s[36:39], s72 offen offset:1024 sc1 lds\n"
    "v_mfma_f32_16x16x32_bf16 v[116:119], v[174:177], a[228:231], v[116:119]\n"
    "ds_read_b128 v[158:161], v148 offset:25600\n"
    "ds_read_b128 v[162:165], v148 offset:37888\n"
    "v_mfma_f32_16x16x32_bf16 v[116:119], v[178:181], a[224:227], v[116:119]\n"
    "v_exp_f32 v87, v87\n"
    "v_mfma_f32_16x16x32_bf16 v[116:119], v[182:185], a[220:223], v[116:119]\n"
    "v_cvt_pk_bf16_f32 v62, v124, v125\n"
    "v_add_f32 v145, v145, v84\n"
    "v_mfma_f32_16x16x32_bf16 v[116:119], v[186:189], a[216:219], v[116:119]\n"
    "v_cvt_pk_bf16_f32 v63, v126, v127\n"
    "v_add_f32 v145, v145, v85\n"
    "v_mfma_f32_16x16x32_bf16 v[120:123], v[186:189], a[240:243], v[136:139]\n"
    "v_exp_f32 v88, v88\n"
    "v_mfma_f32_16x16x32_bf16 v[120:123], v[182:185], a[244:247], v[120:123]\n"
    "buffer_load_dwordx4 v149, s[36:39], s72 offen offset:2048 sc1 lds\n"
    "v_mfma_f32_16x16x32_bf16 v[120:123], v[178:181], a[248:251], v[120:123]\n"
    "v_add_f32 v145, v145, v86\n"
    "v_add_f32 v145, v145, v87\n"
    "v_mfma_f32_16x16x32_bf16 v[120:123], v[174:177], a[252:255], v[120:123]\n"
    "v_exp_f32 v89, v89\n"
    "v_mfma_f32_16x16x32_bf16 v[120:123], v[170:173], v[0:3], v[120:123]\n"
    "v_add_f32 v146, v146, v88\n"
    "v_add_f32 v146, v146, v89\n"
    "v_mfma_f32_16x16x32_bf16 v[120:123], v[166:169], v[4:7], v[120:123]\n"
    "v_fma_f32 v200, v200, v235, -v212\n"
    "v_fma_f32 v199, v199, v234, -v208\n"
    "v_mfma_f32_16x16x32_bf16 v[124:127], v[166:169], v[28:31], v[140:143]\n"
    "v_exp_f32 v90, v90\n"
    "v_mfma_f32_16x16x32_bf16 v[124:127], v[170:173], v[24:27], v[124:127]\n"
    "ds_read_b128 v[166:169], v148 offset:26624\n"
    "ds_read_b128 v[170:173], v148 offset:38912\n"
    "v_mfma_f32_16x16x32_bf16 v[124:127], v[174:177], v[20:23], v[124:127]\n"
    "buffer_load_dwordx4 v149, s[36:39], s72 offen offset:3072 sc1 lds\n"
    "v_mfma_f32_16x16x32_bf16 v[124:127], v[178:181], v[16:19], v[124:127]\n"
    "v_exp_f32 v91, v91\n"
    "v_mfma_f32_16x16x32_bf16 v[124:127], v[182:185], v[12:15], v[124:127]\n"
    "s_add_u32 s72, s72, 4096\n"
    "v_add_f32 v146, v146, v90\n"
    "v_mfma_f32_16x16x32_bf16 v[124:127], v[186:189], v[8:11], v[124:127]\n"
    "ds_read_b128 v[174:177], v148 offset:27648\n"
    "s_waitcnt lgkmcnt(3)\n"
    "v_mfma_f32_16x16x32_bf16 a[0:3], v[32:35], v[150:153], a[0:3]\n"
    "v_exp_f32 v92, v92\n"
    "v_mfma_f32_16x16x32_bf16 a[0:3], v[36:39], v[154:157], a[0:3]\n"
    "ds_read_b128 v[178:181], v148 offset:39936\n"
    "s_add_u32 m0, m0, 4096\n"
    "v_mfma_f32_16x16x32_bf16 a[4:7], v[44:47], v[154:157], a[4:7]\n"
    "v_add_f32 v146, v146, v91\n"
    "v_add_f32 v147, v147, v92\n"
    "v_mfma_f32_16x16x32_bf16 a[4:7], v[40:43], v[150:153], a[4:7]\n"
    "v_exp_f32 v93, v93\n"
    "v_mfma_f32_16x16x32_bf16 a[8:11], v[48:51], v[150:153], a[8:11]\n"
    "v_add_f32 v147, v147, v93\n"
    "v_mfma_f32_16x16x32_bf16 a[8:11], v[52:55], v[154:157], a[8:11]\n"
    "v_fma_f32 v201, v201, v235, -v212\n"
    "v_mfma_f32_16x16x32_bf16 a[12:15], v[60:63], v[154:157], a[12:15]\n"
    "v_exp_f32 v94, v94\n"
    "v_mfma_f32_16x16x32_bf16 a[12:15], v[56:59], v[150:153], a[12:15]\n"
    "buffer_load_dwordx4 v149, s[36:39], s72 offen offset:0 sc1 lds\n"
    "v_mfma_f32_16x16x32_bf16 a[16:19], v[32:35], v[158:161], a[16:19]\n"
    "ds_read_b128 v[182:185], v148 offset:28672\n"
    "ds_read_b128 v[186:189], v148 offset:40960\n"
    "v_mfma_f32_16x16x32_bf16 a[16:19], v[36:39], v[162:165], a[16:19]\n"
    "v_exp_f32 v95, v95\n"
    "v_mfma_f32_16x16x32_bf16 a[20:23], v[44:47], v[162:165], a[20:23]\n"
    "v_add_f32 v147, v147, v94\n"
    "v_add_f32 v147, v147, v95\n"
    "v_mfma_f32_16x16x32_bf16 a[20:23], v[40:43], v[158:161], a[20:23]\n"
    "v_cvt_pk_bf16_f32 v240, v198, v200\n"
    "v_cvt_pk_bf16_f32 v244, v199, v201\n"
    "v_mfma_f32_16x16x32_bf16 a[24:27], v[48:51], v[158:161], a[24:27]\n"
    "v_exp_f32 v96, v96\n"
    "v_mfma_f32_16x16x32_bf16 a[24:27], v[52:55], v[162:165], a[24:27]\n"
    "buffer_load_dwordx4 v149, s[36:39], s72 offen offset:1024 sc1 lds\n"
    "v_mfma_f32_16x16x32_bf16 a[28:31], v[60:63], v[162:165], a[28:31]\n"
    "v_add_f32 v144, v144, v96\n"
    "v_mfma_f32_16x16x32_bf16 a[28:31], v[56:59], v[158:161], a[28:31]\n"
    "v_exp_f32 v97, v97\n"
    "s_waitcnt lgkmcnt(0)\n"
    "v_mfma_f32_16x16x32_bf16 a[32:35], v[32:35], v[166:169], a[32:35]\n"
    "ds_read_b128 v[150:153], v148 offset:29696\n"
    "v_add_f32 v144, v144, v97\n"
    "v_mfma_f32_16x16x32_bf16 a[32:35], v[36:39], v[170:173], a[32:35]\n"
    "ds_read_b128 v[154:157], v148 offset:41984\n"
    "v_cvt_f32_i32 v202, v202\n"
    "v_mfma_f32_16x16x32_bf16 a[36:39], v[44:47], v[170:173], a[36:39]\n"
    "v_exp_f32 v98, v98\n"
    "v_mfma_f32_16x16x32_bf16 a[36:39], v[40:43], v[166:169], a[36:39]\n"
    "buffer_load_dwordx4 v149, s[36:39], s72 offen offset:2048 sc1 lds\n"
    "v_mfma_f32_16x16x32_bf16 a[40:43], v[48:51], v[166:169], a[40:43]\n"
    "v_add_f32 v144, v144, v98\n"
    "v_cvt_f32_i32 v204, v204\n"
    "v_mfma_f32_16x16x32_bf16 a[40:43], v[52:55], v[170:173], a[40:43]\n"
    "v_exp_f32 v99, v99\n"
    "v_mfma_f32_16x16x32_bf16 a[44:47], v[60:63], v[170:173], a[44:47]\n"
    "v_add_f32 v144, v144, v99\n"
    "v_mfma_f32_16x16x32_bf16 a[44:47], v[56:59], v[166:169], a[44:47]\n"
    "ds_read_b128 v[158:161], v148 offset:30720\n"
    "v_cvt_f32_i32 v203, v203\n"
    "v_mfma_f32_16x16x32_bf16 a[48:51], v[32:35], v[174:177], a[48:51]\n"
    "v_exp_f32 v100, v100\n"
    "v_mfma_f32_16x16x32_bf16 a[48:51], v[36:39], v[178:181], a[48:51]\n"
    "ds_read_b128 v[162:165], v148 offset:43008\n"
    "v_add_f32 v145, v145, v100\n"
    "v_mfma_f32_16x16x32_bf16 a[52:55], v[44:47], v[178:181], a[52:55]\n"
    "buffer_load_dwordx4 v149, s[36:39], s72 offen offset:3072 sc1 lds\n"
    "v_mfma_f32_16x16x32_bf16 a[52:55], v[40:43], v[174:177], a[52:55]\n"
    "v_exp_f32 v101, v101\n"
    "v_mfma_f32_16x16x32_bf16 a[56:59], v[48:51], v[174:177], a[56:59]\n"
    "s_add_u32 s72, s72, 4096\n"
    "v_add_f32 v145, v145, v101\n"
    "v_mfma_f32_16x16x32_bf16 a[56:59], v[52:55], v[178:181], a[56:59]\n"
    "s_add_u32 m0, m0, 4096\n"
    "v_cvt_f32_i32 v205, v205\n"
    "v_mfma_f32_16x16x32_bf16 a[60:63], v[60:63], v[178:181], a[60:63]\n"
    "v_exp_f32 v102, v102\n"
    "v_mfma_f32_16x16x32_bf16 a[60:63], v[56:59], v[174:177], a[60:63]\n"
    "v_add_f32 v145, v145, v102\n"
    "v_mfma_f32_16x16x32_bf16 a[64:67], v[32:35], v[182:185], a[64:67]\n"
    "ds_read_b128 v[166:169], v148 offset:31744\n"
    "ds_read_b128 v[170:173], v148 offset:44032\n"
    "v_mfma_f32_16x16x32_bf16 a[64:67], v[36:39], v[186:189], a[64:67]\n"
    "v_exp_f32 v103, v103\n"
    "v_mfma_f32_16x16x32_bf16 a[68:71], v[44:47], v[186:189], a[68:71]\n"
    "v_add_f32 v145, v145, v103\n"
    "v_fma_f32 v202, v202, v236, -v209\n"
    "v_mfma_f32_16x16x32_bf16 a[68:71], v[40:43], v[182:185], a[68:71]\n"
    "buffer_load_dwordx4 v149, s[36:39], s72 offen offset:0 sc1 lds\n"
    "v_mfma_f32_16x16x32_bf16 a[72:75], v[48:51], v[182:185], a[72:75]\n"
    "v_exp_f32 v104, v104\n"
    "v_mfma_f32_16x16x32_bf16 a[72:75], v[52:55], v[186:189], a[72:75]\n"
    "v_add_f32 v146, v146, v104\n"
    "v_mfma_f32_16x16x32_bf16 a[76:79], v[60:63], v[186:189], a[76:79]\n"
    "v_fma_f32 v204, v204, v237, -v213\n"
    "v_fma_f32 v203, v203, v236, -v209\n"
    "v_mfma_f32_16x16x32_bf16 a[76:79], v[56:59], v[182:185], a[76:79]\n"
    "v_exp_f32 v105, v105\n"
    "s_waitcnt lgkmcnt(0)\n"
    "v_mfma_f32_16x16x32_bf16 a[80:83], v[32:35], v[150:153], a[80:83]\n"
    "v_add_f32 v146, v146, v105\n"
    "v_mfma_f32_16x16x32_bf16 a[80:83], v[36:39], v[154:157], a[80:83]\n"
    "buffer_load_dwordx4 v149, s[36:39], s72 offen offset:1024 sc1 lds\n"
    "v_mfma_f32_16x16x32_bf16 a[84:87], v[44:47], v[154:157], a[84:87]\n"
    "v_exp_f32 v106, v106\n"
    "v_mfma_f32_16x16x32_bf16 a[84:87], v[40:43], v[150:153], a[84:87]\n"
    "v_add_f32 v146, v146, v106\n"
    "v_fma_f32 v205, v205, v237, -v213\n"
    "v_mfma_f32_16x16x32_bf16 a[88:91], v[48:51], v[150:153], a[88:91]\n"
    "v_cvt_pk_bf16_f32 v241, v202, v204\n"
    "v_mfma_f32_16x16x32_bf16 a[88:91], v[52:55], v[154:157], a[88:91]\n"
    "v_exp_f32 v107, v107\n"
    "v_mfma_f32_16x16x32_bf16 a[92:95], v[60:63], v[154:157], a[92:95]\n"
    "buffer_load_dwordx4 v149, s[36:39], s72 offen offset:2048 sc1 lds\n"
    "v_mfma_f32_16x16x32_bf16 a[92:95], v[56:59], v[150:153], a[92:95]\n"
    "ds_read_b128 v[150:153], v148 offset:32768\n"
    "ds_read_b128 v[154:157], v148 offset:45056\n"
    "v_mfma_f32_16x16x32_bf16 a[96:99], v[32:35], v[158:161], a[96:99]\n"
    "v_exp_f32 v108, v108\n"
    "v_mfma_f32_16x16x32_bf16 a[96:99], v[36:39], v[162:165], a[96:99]\n"
    "v_add_f32 v146, v146, v107\n"
    "v_add_f32 v147, v147, v108\n"
    "v_mfma_f32_16x16x32_bf16 a[100:103], v[44:47], v[162:165], a[100:103]\n"
    "v_cvt_pk_bf16_f32 v245, v203, v205\n"
    "v_mfma_f32_16x16x32_bf16 a[100:103], v[40:43], v[158:161], a[100:103]\n"
    "v_exp_f32 v109, v109\n"
    "v_mfma_f32_16x16x32_bf16 a[104:107], v[48:51], v[158:161], a[104:107]\n"
    "buffer_load_dwordx4 v149, s[36:39], s72 offen offset:3072 sc1 lds\n"
    "v_mfma_f32_16x16x32_bf16 a[104:107], v[52:55], v[162:165], a[104:107]\n"
    "s_add_u32 s72, s72, 16384\n"
    "v_add_f32 v147, v147, v109\n"
    "v_mfma_f32_16x16x32_bf16 a[108:111], v[60:63], v[162:165], a[108:111]\n"
    "v_exp_f32 v110, v110\n"
    "v_mfma_f32_16x16x32_bf16 a[108:111], v[56:59], v[158:161], a[108:111]\n"
    "ds_read_b128 v[158:161], v148 offset:33792\n"
    "ds_read_b128 v[162:165], v148 offset:46080\n"
    "v_mfma_f32_16x16x32_bf16 a[112:115], v[32:35], v[166:169], a[112:115]\n"
    "ds_read_b128 v[174:177], v148 offset:34816\n"
    "ds_read_b128 v[178:181], v148 offset:47104\n"
    "v_mfma_f32_16x16x32_bf16 a[112:115], v[36:39], v[170:173], a[112:115]\n"
    "v_exp_f32 v111, v111\n"
    "v_mfma_f32_16x16x32_bf16 a[116:119], v[44:47], v[170:173], a[116:119]\n"
    "ds_read_b128 v[182:185], v148 offset:35840\n"
    "v_add_f32 v147, v147, v110\n"
    "v_mfma_f32_16x16x32_bf16 a[116:119], v[40:43], v[166:169], a[116:119]\n"
    "ds_read_b128 v[186:189], v148 offset:48128\n"
    "v_add_f32 v147, v147, v111\n"
    "v_mfma_f32_16x16x32_bf16 a[120:123], v[48:51], v[166:169], a[120:123]\n"
    "v_exp_f32 v112, v112\n"
    "v_mfma_f32_16x16x32_bf16 a[120:123], v[52:55], v[170:173], a[120:123]\n"
    "s_add_u32 s89, s85, 4096\n"
    "v_add_f32 v144, v144, v112\n"
    "v_mfma_f32_16x16x32_bf16 a[124:127], v[60:63], v[170:173], a[124:127]\n"
    "buffer_store_dwordx4 v[238:241], v149, s[52:55], s89 offen offset:0 sc1\n"
    "v_mfma_f32_16x16x32_bf16 a[124:127], v[56:59], v[166:169], a[124:127]\n"
    "v_exp_f32 v113, v113\n"
    "s_waitcnt lgkmcnt(2)\n"
    "v_mfma_f32_16x16x32_bf16 a[128:131], v[32:35], v[150:153], a[128:131]\n"
    "buffer_store_dwordx4 v[242:245], v149, s[52:55], s89 offen offset:1024 sc1\n"
    "v_mfma_f32_16x16x32_bf16 a[128:131], v[36:39], v[154:157], a[128:131]\n"
    "s_add_u32 s85, s85, 98304\n"
    "v_add_f32 v144, v144, v113\n"
    "v_mfma_f32_16x16x32_bf16 a[132:135], v[44:47], v[154:157], a[132:135]\n"
    "v_exp_f32 v114, v114\n"
    "v_mfma_f32_16x16x32_bf16 a[132:135], v[40:43], v[150:153], a[132:135]\n"
    "s_waitcnt vmcnt(14) lgkmcnt(0)\n"
    "s_barrier\n"
    "v_mfma_f32_16x16x32_bf16 a[136:139], v[48:51], v[150:153], a[136:139]\n"
    "v_add_u32 v254, s73, v254\n"
    "v_add_u32 v251, s73, v251\n"
    "v_mfma_f32_16x16x32_bf16 a[136:139], v[52:55], v[154:157], a[136:139]\n"
    "v_exp_f32 v115, v115\n"
    "v_mfma_f32_16x16x32_bf16 a[140:143], v[60:63], v[154:157], a[140:143]\n"
    "buffer_load_dwordx4 v[206:209], v249, s[44:47], s84 offen nt\n"
    "v_mfma_f32_16x16x32_bf16 a[140:143], v[56:59], v[150:153], a[140:143]\n"
    "ds_read_b128 v[150:153], v254 offset:0\n"
    "v_add_f32 v144, v144, v114\n"
    "v_mfma_f32_16x16x32_bf16 a[144:147], v[32:35], v[158:161], a[144:147]\n"
    "v_exp_f32 v116, v116\n"
    "v_mfma_f32_16x16x32_bf16 a[144:147], v[36:39], v[162:165], a[144:147]\n"
    "ds_read_b128 v[154:157], v251 offset:0\n"
    "v_add_f32 v144, v144, v115\n"
    "v_mfma_f32_16x16x32_bf16 a[148:151], v[44:47], v[162:165], a[148:151]\n"
    "buffer_load_dwordx4 v[210:213], v249, s[48:51], s84 offen nt\n"
    "v_mfma_f32_16x16x32_bf16 a[148:151], v[40:43], v[158:161], a[148:151]\n"
    "v_exp_f32 v117, v117\n"
    "v_mfma_f32_16x16x32_bf16 a[152:155], v[48:51], v[158:161], a[152:155]\n"
    "buffer_load_dwordx2 v[190:191], v248, s[40:43], s82 offen offset:0 nt\n"
    "v_mfma_f32_16x16x32_bf16 a[152:155], v[52:55], v[162:165], a[152:155]\n"
    "buffer_load_dwordx2 v[192:193], v248, s[40:43], s82 offen offset:768 nt\n"
    "v_mfma_f32_16x16x32_bf16 a[156:159], v[60:63], v[162:165], a[156:159]\n"
    "v_exp_f32 v118, v118\n"
    "v_mfma_f32_16x16x32_bf16 a[156:159], v[56:59], v[158:161], a[156:159]\n"
    "ds_read_b128 v[158:161], v254 offset:2048\n"
    "ds_read_b128 v[162:165], v251 offset:2048\n"
    "v_mfma_f32_16x16x32_bf16 a[160:163], v[32:35], v[174:177], a[160:163]\n"
    "v_add_u32 v148, s73, v148\n"
    "ds_read_b128 v[166:169], v254 offset:4096\n"
    "v_mfma_f32_16x16x32_bf16 a[160:163], v[36:39], v[178:181], a[160:163]\n"
    "v_exp_f32 v119, v119\n"
    "v_mfma_f32_16x16x32_bf16 a[164:167], v[44:47], v[178:181], a[164:167]\n"
    "ds_read_b128 v[170:173], v251 offset:4096\n"
    "v_add_f32 v145, v145, v116\n"
    "v_mfma_f32_16x16x32_bf16 a[164:167], v[40:43], v[174:177], a[164:167]\n"
    "buffer_load_dwordx2 v[194:195], v248, s[40:43], s82 offen offset:1536 nt\n"
    "v_mfma_f32_16x16x32_bf16 a[168:171], v[48:51], v[174:177], a[168:171]\n"
    "v_exp_f32 v120, v120\n"
    "v_mfma_f32_16x16x32_bf16 a[168:171], v[52:55], v[178:181], a[168:171]\n"
    "buffer_load_dwordx2 v[196:197], v248, s[40:43], s82 offen offset:2304 nt\n"
    "v_mfma_f32_16x16x32_bf16 a[172:175], v[60:63], v[178:181], a[172:175]\n"
    "v_add_f32 v145, v145, v117\n"
    "v_add_f32 v145, v145, v118\n"
    "v_mfma_f32_16x16x32_bf16 a[172:175], v[56:59], v[174:177], a[172:175]\n"
    "v_exp_f32 v121, v121\n"
    "v_mfma_f32_16x16x32_bf16 a[176:179], v[32:35], v[182:185], a[176:179]\n"
    "v_cvt_pk_bf16_f32 v32, v64, v65\n"
    "v_add_f32 v145, v145, v119\n"
    "v_add_f32 v146, v146, v121\n"
    "v_mfma_f32_16x16x32_bf16 a[176:179], v[36:39], v[186:189], a[176:179]\n"
    "buffer_load_dwordx2 v[198:199], v248, s[40:43], s83 offen offset:0 nt\n"
    "v_mfma_f32_16x16x32_bf16 a[180:183], v[44:47], v[186:189], a[180:183]\n"
    "v_exp_f32 v122, v122\n"
    "v_mfma_f32_16x16x32_bf16 a[180:183], v[40:43], v[182:185], a[180:183]\n"
    "v_cvt_pk_bf16_f32 v33, v66, v67\n"
    "v_add_f32 v146, v146, v120\n"
    "v_add_f32 v146, v146, v122\n"
    "v_mfma_f32_16x16x32_bf16 a[184:187], v[48:51], v[182:185], a[184:187]\n"
    "buffer_load_dwordx2 v[200:201], v248, s[40:43], s83 offen offset:768 nt\n"
    "v_mfma_f32_16x16x32_bf16 a[184:187], v[52:55], v[186:189], a[184:187]\n"
    "v_exp_f32 v123, v123\n"
    "v_mfma_f32_16x16x32_bf16 a[188:191], v[60:63], v[186:189], a[188:191]\n"
    "s_cmp_lt_u32 s72, 663552\n"
    "v_cvt_pk_bf16_f32 v40, v68, v69\n"
    "v_add_f32 v146, v146, v123\n"
    "v_mfma_f32_16x16x32_bf16 a[188:191], v[56:59], v[182:185], a[188:191]\n"
    "ds_read_b128 v[174:177], v254 offset:6144\n"
    "s_cbranch_scc1 SUPV%=\n"
    "s_branch PLAIN%=\n"
    ".p2align 6\n"
    "PLAIN%=:\n"
    "s_waitcnt lgkmcnt(1)\n"
    "v_mfma_f32_16x16x32_bf16 v[64:67], v[150:153], a[192:195], v[128:131]\n"
    "v_exp_f32 v124, v124\n"
    "v_mfma_f32_16x16x32_bf16 v[64:67], v[154:157], a[196:199], v[64:67]\n"
    "ds_read_b128 v[178:181], v251 offset:6144\n"
    "v_cvt_pk_bf16_f32 v41, v70, v71\n"
    "v_mfma_f32_16x16x32_bf16 v[64:67], v[158:161], a[200:203], v[64:67]\n"
    "ds_read_b128 v[182:185], v254 offset:8192\n"
    "ds_read_b128 v[186:189], v251 offset:8192\n"
    "v_mfma_f32_16x16x32_bf16 v[64:67], v[162:165], a[204:207], v[64:67]\n"
    "v_exp_f32 v125, v125\n"
    "v_mfma_f32_16x16x32_bf16 v[64:67], v[166:169], a[208:211], v[64:67]\n"
    "v_cvt_pk_bf16_f32 v48, v72, v73\n"
    "v_add_f32 v147, v147, v124\n"
    "v_mfma_f32_16x16x32_bf16 v[64:67], v[170:173], a[212:215], v[64:67]\n"
    "v_cvt_pk_bf16_f32 v49, v74, v75\n"
    "v_add_f32 v147, v147, v125\n"
    "v_mfma_f32_16x16x32_bf16 v[68:71], v[170:173], a[236:239], v[132:135]\n"
    "v_exp_f32 v126, v126\n"
    "v_mfma_f32_16x16x32_bf16 v[68:71], v[166:169], a[232:235], v[68:71]\n"
    "v_add_f32 v147, v147, v126\n"
    "v_mfma_f32_16x16x32_bf16 v[68:71], v[162:165], a[228:231], v[68:71]\n"
    "v_cvt_pk_bf16_f32 v56, v76, v77\n"
    "v_mfma_f32_16x16x32_bf16 v[68:71], v[158:161], a[224:227], v[68:71]\n"
    "v_exp_f32 v127, v127\n"
    "v_mfma_f32_16x16x32_bf16 v[68:71], v[154:157], a[220:223], v[68:71]\n"
    "v_add_f32 v147, v147, v127\n"
    "v_mfma_f32_16x16x32_bf16 v[68:71], v[150:153], a[216:219], v[68:71]\n"
    "v_cvt_pk_bf16_f32 v57, v78, v79\n"
    "v_mfma_f32_16x16x32_bf16 v[72:75], v[150:153], a[240:243], v[136:139]\n"
    "v_exp_f32 v64, v64\n"
    "v_mfma_f32_16x16x32_bf16 v[72:75], v[154:157], a[244:247], v[72:75]\n"
    "v_add_f32 v144, v144, v64\n"
    "v_mfma_f32_16x16x32_bf16 v[72:75], v[158:161], a[248:251], v[72:75]\n"
    "v_cvt_pk_bf16_f32 v34, v80, v81\n"
    "v_mfma_f32_16x16x32_bf16 v[72:75], v[162:165], a[252:255], v[72:75]\n"
    "v_exp_f32 v65, v65\n"
    "v_mfma_f32_16x16x32_bf16 v[72:75], v[166:169], v[0:3], v[72:75]\n"
    "v_add_f32 v144, v144, v65\n"
    "v_mfma_f32_16x16x32_bf16 v[72:75], v[170:173], v[4:7], v[72:75]\n"
    "v_cvt_pk_bf16_f32 v35, v82, v83\n"
    "v_mfma_f32_16x16x32_bf16 v[76:79], v[170:173], v[28:31], v[140:143]\n"
    "v_exp_f32 v66, v66\n"
    "v_mfma_f32_16x16x32_bf16 v[76:79], v[166:169], v[24:27], v[76:79]\n"
    "ds_read_b128 v[170:173], v254 offset:10240\n"
    "ds_read_b128 v[166:169], v251 offset:10240\n"
    "v_mfma_f32_16x16x32_bf16 v[76:79], v[162:165], v[20:23], v[76:79]\n"
    "v_cvt_pk_bf16_f32 v42, v84, v85\n"
    "v_add_f32 v144, v144, v66\n"
    "v_mfma_f32_16x16x32_bf16 v[76:79], v[158:161], v[16:19], v[76:79]\n"
    "v_exp_f32 v67, v67\n"
    "v_mfma_f32_16x16x32_bf16 v[76:79], v[154:157], v[12:15], v[76:79]\n"
    "v_add_f32 v144, v144, v67\n"
    "v_mfma_f32_16x16x32_bf16 v[76:79], v[150:153], v[8:11], v[76:79]\n"
    "ds_read_b128 v[162:165], v254 offset:12288\n"
    "s_waitcnt lgkmcnt(3)\n"
    "v_mfma_f32_16x16x32_bf16 v[80:83], v[174:177], a[192:195], v[128:131]\n"
    "v_exp_f32 v68, v68\n"
    "v_mfma_f32_16x16x32_bf16 v[80:83], v[178:181], a[196:199], v[80:83]\n"
    "ds_read_b128 v[158:161], v251 offset:12288\n"
    "v_cvt_pk_bf16_f32 v43, v86, v87\n"
    "v_mfma_f32_16x16x32_bf16 v[80:83], v[182:185], a[200:203], v[80:83]\n"
    "ds_read_b128 v[154:157], v254 offset:14336\n"
    "ds_read_b128 v[150:153], v251 offset:14336\n"
    "v_mfma_f32_16x16x32_bf16 v[80:83], v[186:189], a[204:207], v[80:83]\n"
    "v_exp_f32 v69, v69\n"
    "s_waitcnt lgkmcnt(4)\n"
    "v_mfma_f32_16x16x32_bf16 v[80:83], v[170:173], a[208:211], v[80:83]\n"
    "v_cvt_pk_bf16_f32 v50, v88, v89\n"
    "v_add_f32 v145, v145, v68\n"
    "v_mfma_f32_16x16x32_bf16 v[80:83], v[166:169], a[212:215], v[80:83]\n"
    "v_cvt_pk_bf16_f32 v51, v90, v91\n"
    "v_add_f32 v145, v145, v69\n"
    "v_mfma_f32_16x16x32_bf16 v[84:87], v[166:169], a[236:239], v[132:135]\n"
    "v_exp_f32 v70, v70\n"
    "v_mfma_f32_16x16x32_bf16 v[84:87], v[170:173], a[232:235], v[84:87]\n"
    "v_add_f32 v145, v145, v70\n"
    "v_mfma_f32_16x16x32_bf16 v[84:87], v[186:189], a[228:231], v[84:87]\n"
    "v_cvt_pk_bf16_f32 v58, v92, v93\n"
    "v_mfma_f32_16x16x32_bf16 v[84:87], v[182:185], a[224:227], v[84:87]\n"
    "v_exp_f32 v71, v71\n"
    "v_mfma_f32_16x16x32_bf16 v[84:87], v[178:181], a[220:223], v[84:87]\n"
    "v_add_f32 v145, v145, v71\n"
    "v_mfma_f32_16x16x32_bf16 v[84:87], v[174:177], a[216:219], v[84:87]\n"
    "v_cvt_pk_bf16_f32 v59, v94, v95\n"
    "v_mfma_f32_16x16x32_bf16 v[88:91], v[174:177], a[240:243], v[136:139]\n"
    "v_exp_f32 v72, v72\n"
    "v_mfma_f32_16x16x32_bf16 v[88:91], v[178:181], a[244:247], v[88:91]\n"
    "v_add_f32 v146, v146, v72\n"
    "v_mfma_f32_16x16x32_bf16 v[88:91], v[182:185], a[248:251], v[88:91]\n"
    "v_cvt_pk_bf16_f32 v36, v96, v97\n"
    "v_mfma_f32_16x16x32_bf16 v[88:91], v[186:189], a[252:255], v[88:91]\n"
    "v_exp_f32 v73, v73\n"
    "v_mfma_f32_16x16x32_bf16 v[88:91], v[170:173], v[0:3], v[88:91]\n"
    "v_add_f32 v146, v146, v73\n"
    "v_mfma_f32_16x16x32_bf16 v[88:91], v[166:169], v[4:7], v[88:91]\n"
    "v_cvt_pk_bf16_f32 v37, v98, v99\n"
    "v_mfma_f32_16x16x32_bf16 v[92:95], v[166:169], v[28:31], v[140:143]\n"
    "v_exp_f32 v74, v74\n"
    "v_mfma_f32_16x16x32_bf16 v[92:95], v[170:173], v[24:27], v[92:95]\n"
    "ds_read_b128 v[166:169], v254 offset:16384\n"
    "ds_read_b128 v[170:173], v251 offset:16384\n"
    "v_mfma_f32_16x16x32_bf16 v[92:95], v[186:189], v[20:23], v[92:95]\n"
    "s_waitcnt vmcnt(0)\n"
    "v_cmp_ne_u64 vcc, s[80:81], v[252:253]\n"
    "v_mfma_f32_16x16x32_bf16 v[92:95], v[182:185], v[16:19], v[92:95]\n"
    "v_exp_f32 v75, v75\n"
    "v_mfma_f32_16x16x32_bf16 v[92:95], v[178:181], v[12:15], v[92:95]\n"
    "s_nop 0\n"
    "s_cbranch_vccnz SPP%=\n"
    "RTP%=:\n"
    "v_cvt_pk_bf16_f32 v44, v100, v101\n"
    "v_mfma_f32_16x16x32_bf16 v[92:95], v[174:177], v[8:11], v[92:95]\n"
    "ds_read_b128 v[186:189], v254 offset:18432\n"
    "s_waitcnt lgkmcnt(3)\n"
    "v_mfma_f32_16x16x32_bf16 v[96:99], v[162:165], a[192:195], v[128:131]\n"
    "v_exp_f32 v76, v76\n"
    "v_mfma_f32_16x16x32_bf16 v[96:99], v[158:161], a[196:199], v[96:99]\n"
    "ds_read_b128 v[182:185], v251 offset:18432\n"
    "v_cvt_pk_bf16_f32 v45, v102, v103\n"
    "v_mfma_f32_16x16x32_bf16 v[96:99], v[154:157], a[200:203], v[96:99]\n"
    "ds_read_b128 v[178:181], v254 offset:20480\n"
    "ds_read_b128 v[174:177], v251 offset:20480\n"
    "v_mfma_f32_16x16x32_bf16 v[96:99], v[150:153], a[204:207], v[96:99]\n"
    "v_exp_f32 v77, v77\n"
    "s_waitcnt lgkmcnt(4)\n"
    "v_mfma_f32_16x16x32_bf16 v[96:99], v[166:169], a[208:211], v[96:99]\n"
    "s_waitcnt vmcnt(6)\n"
    "v_cvt_pk_bf16_f32 v52, v104, v105\n"
    "v_mfma_f32_16x16x32_bf16 v[96:99], v[170:173], a[212:215], v[96:99]\n"
    "buffer_store_dwordx2 v[252:253], off, s[64:67], s86 sc1\n"
    "v_mfma_f32_16x16x32_bf16 v[100:103], v[170:173], a[236:239], v[132:135]\n"
    "v_exp_f32 v78, v78\n"
    "v_mfma_f32_16x16x32_bf16 v[100:103], v[166:169], a[232:235], v[100:103]\n"
    "s_mov_b32 s86, 0x10000\n"
    "v_cvt_pk_bf16_f32 v53, v106, v107\n"
    "v_mfma_f32_16x16x32_bf16 v[100:103], v[150:153], a[228:231], v[100:103]\n"
    "v_cvt_pk_bf16_f32 v60, v108, v109\n"
    "v_add_f32 v146, v146, v74\n"
    "v_mfma_f32_16x16x32_bf16 v[100:103], v[154:157], a[224:227], v[100:103]\n"
    "v_exp_f32 v79, v79\n"
    "v_mfma_f32_16x16x32_bf16 v[100:103], v[158:161], a[220:223], v[100:103]\n"
    "v_add_f32 v146, v146, v75\n"
    "v_add_f32 v147, v147, v76\n"
    "v_mfma_f32_16x16x32_bf16 v[100:103], v[162:165], a[216:219], v[100:103]\n"
    "v_cvt_pk_bf16_f32 v61, v110, v111\n"
    "v_add_f32 v147, v147, v77\n"
    "v_mfma_f32_16x16x32_bf16 v[104:107], v[162:165], a[240:243], v[136:139]\n"
    "v_exp_f32 v80, v80\n"
    "v_mfma_f32_16x16x32_bf16 v[104:107], v[158:161], a[244:247], v[104:107]\n"
    "v_add_f32 v147, v147, v78\n"
    "v_add_f32 v147, v147, v79\n"
    "v_mfma_f32_16x16x32_bf16 v[104:107], v[154:157], a[248:251], v[104:107]\n"
    "v_cvt_pk_bf16_f32 v38, v112, v113\n"
    "v_add_f32 v144, v144, v80\n"
    "v_mfma_f32_16x16x32_bf16 v[104:107], v[150:153], a[252:255], v[104:107]\n"
    "v_exp_f32 v81, v81\n"
    "v_mfma_f32_16x16x32_bf16 v[104:107], v[166:169], v[0:3], v[104:107]\n"
    "v_add_f32 v144, v144, v81\n"
    "v_mfma_f32_16x16x32_bf16 v[104:107], v[170:173], v[4:7], v[104:107]\n"
    "s_mov_b32 m0, s74\n"
    "v_cvt_pk_bf16_f32 v39, v114, v115\n"
    "v_mfma_f32_16x16x32_bf16 v[108:111], v[170:173], v[28:31], v[140:143]\n"
    "v_exp_f32 v82, v82\n"
    "v_mfma_f32_16x16x32_bf16 v[108:111], v[166:169], v[24:27], v[108:111]\n"
    "ds_read_b128 v[170:173], v254 offset:22528\n"
    "ds_read_b128 v[166:169], v251 offset:22528\n"
    "v_mfma_f32_16x16x32_bf16 v[108:111], v[150:153], v[20:23], v[108:111]\n"
    "s_add_u32 s74, s74, s73\n"
    "v_cvt_pk_bf16_f32 v46, v116, v117\n"
    "v_mfma_f32_16x16x32_bf16 v[108:111], v[154:157], v[16:19], v[108:111]\n"
    "v_exp_f32 v83, v83\n"
    "v_mfma_f32_16x16x32_bf16 v[108:111], v[158:161], v[12:15], v[108:111]\n"
    "s_cmp_ge_u32 s74, s75\n"
    "s_cselect_b32 s73, 0xfffe8000, s76\n"
    "v_mfma_f32_16x16x32_bf16 v[108:111], v[162:165], v[8:11], v[108:111]\n"
    "ds_read_b128 v[150:153], v148 offset:24576\n"
    "s_waitcnt lgkmcnt(3)\n"
    "v_mfma_f32_16x16x32_bf16 v[112:115], v[186:189], a[192:195], v[128:131]\n"
    "v_exp_f32 v84, v84\n"
    "v_mfma_f32_16x16x32_bf16 v[112:115], v[182:185], a[196:199], v[112:115]\n"
    "ds_read_b128 v[154:157], v148 offset:36864\n"
    "v_cvt_pk_bf16_f32 v47, v118, v119\n"
    "v_mfma_f32_16x16x32_bf16 v[112:115], v[178:181], a[200:203], v[112:115]\n"
    "buffer_load_dwordx4 v149, s[36:39], s72 offen offset:0 sc1 lds\n"
    "v_mfma_f32_16x16x32_bf16 v[112:115], v[174:177], a[204:207], v[112:115]\n"
    "v_exp_f32 v85, v85\n"
    "s_waitcnt lgkmcnt(2)\n"
    "v_mfma_f32_16x16x32_bf16 v[112:115], v[170:173], a[208:211], v[112:115]\n"
    "v_cvt_pk_bf16_f32 v54, v120, v121\n"
    "v_add_f32 v144, v144, v82\n"
    "v_mfma_f32_16x16x32_bf16 v[112:115], v[166:169], a[212:215], v[112:115]\n"
    "v_cvt_pk_bf16_f32 v55, v122, v123\n"
    "v_add_f32 v144, v144, v83\n"
    "v_mfma_f32_16x16x32_bf16 v[116:119], v[166:169], a[236:239], v[132:135]\n"
    "v_exp_f32 v86, v86\n"
    "v_mfma_f32_16x16x32_bf16 v[116:119], v[170:173], a[232:235], v[116:119]\n"
    "buffer_load_dwordx4 v149, s[36:39], s72 offen offset:1024 sc1 lds\n"
    "v_mfma_f32_16x16x32_bf16 v[116:119], v[174:177], a[228:231], v[116:119]\n"
    "ds_read_b128 v[158:161], v148 offset:25600\n"
    "ds_read_b128 v[162:165], v148 offset:37888\n"
    "v_mfma_f32_16x16x32_bf16 v[116:119], v[178:181], a[224:227], v[116:119]\n"
    "v_exp_f32 v87, v87\n"
    "v_mfma_f32_16x16x32_bf16 v[116:119], v[182:185], a[220:223], v[116:119]\n"
    "v_cvt_pk_bf16_f32 v62, v124, v125\n"
    "v_add_f32 v145, v145, v84\n"
    "v_mfma_f32_16x16x32_bf16 v[116:119], v[186:189], a[216:219], v[116:119]\n"
    "v_cvt_pk_bf16_f32 v63, v126, v127\n"
    "v_add_f32 v145, v145, v85\n"
    "v_mfma_f32_16x16x32_bf16 v[120:123], v[186:189], a[240:243], v[136:139]\n"
    "v_exp_f32 v88, v88\n"
    "v_mfma_f32_16x16x32_bf16 v[120:123], v[182:185], a[244:247], v[120:123]\n"
    "buffer_load_dwordx4 v149, s[36:39], s72 offen offset:2048 sc1 lds\n"
    "v_mfma_f32_16x16x32_bf16 v[120:123], v[178:181], a[248:251], v[120:123]\n"
    "v_add_f32 v145, v145, v86\n"
    "v_add_f32 v145, v145, v87\n"
    "v_mfma_f32_16x16x32_bf16 v[120:123], v[174:177], a[252:255], v[120:123]\n"
    "v_exp_f32 v89, v89\n"
    "v_mfma_f32_16x16x32_bf16 v[120:123], v[170:173], v[0:3], v[120:123]\n"
    "v_add_f32 v146, v146, v88\n"
    "v_add_f32 v146, v146, v89\n"
    "v_mfma_f32_16x16x32_bf16 v[120:123], v[166:169], v[4:7], v[120:123]\n"
    "v_mfma_f32_16x16x32_bf16 v[124:127], v[166:169], v[28:31], v[140:143]\n"
    "v_exp_f32 v90, v90\n"
    "v_mfma_f32_16x16x32_bf16 v[124:127], v[170:173], v[24:27], v[124:127]\n"
    "ds_read_b128 v[166:169], v148 offset:26624\n"
    "ds_read_b128 v[170:173], v148 offset:38912\n"
    "v_mfma_f32_16x16x32_bf16 v[124:127], v[174:177], v[20:23], v[124:127]\n"
    "buffer_load_dwordx4 v149, s[36:39], s72 offen offset:3072 sc1 lds\n"
    "v_mfma_f32_16x16x32_bf16 v[124:127], v[178:181], v[16:19], v[124:127]\n"
    "v_exp_f32 v91, v91\n"
    "v_mfma_f32_16x16x32_bf16 v[124:127], v[182:185], v[12:15], v[124:127]\n"
    "s_add_u32 s72, s72, 4096\n"
    "v_add_f32 v146, v146, v90\n"
    "v_mfma_f32_16x16x32_bf16 v[124:127], v[186:189], v[8:11], v[124:127]\n"
    "ds_read_b128 v[174:177], v148 offset:27648\n"
    "s_waitcnt lgkmcnt(3)\n"
    "v_mfma_f32_16x16x32_bf16 a[0:3], v[32:35], v[150:153], a[0:3]\n"
    "v_exp_f32 v92, v92\n"
    "v_mfma_f32_16x16x32_bf16 a[0:3], v[36:39], v[154:157], a[0:3]\n"
    "ds_read_b128 v[178:181], v148 offset:39936\n"
    "s_add_u32 m0, m0, 4096\n"
    "v_mfma_f32_16x16x32_bf16 a[4:7], v[44:47], v[154:157], a[4:7]\n"
    "v_add_f32 v146, v146, v91\n"
    "v_add_f32 v147, v147, v92\n"
    "v_mfma_f32_16x16x32_bf16 a[4:7], v[40:43], v[150:153], a[4:7]\n"
    "v_exp_f32 v93, v93\n"
    "v_mfma_f32_16x16x32_bf16 a[8:11], v[48:51], v[150:153], a[8:11]\n"
    "v_add_f32 v147, v147, v93\n"
    "v_mfma_f32_16x16x32_bf16 a[8:11], v[52:55], v[154:157], a[8:11]\n"
    "v_mfma_f32_16x16x32_bf16 a[12:15], v[60:63], v[154:157], a[12:15]\n"
    "v_exp_f32 v94, v94\n"
    "v_mfma_f32_16x16x32_bf16 a[12:15], v[56:59], v[150:153], a[12:15]\n"
    "buffer_load_dwordx4 v149, s[36:39], s72 offen offset:0 sc1 lds\n"
    "v_mfma_f32_16x16x32_bf16 a[16:19], v[32:35], v[158:161], a[16:19]\n"
    "ds_read_b128 v[182:185], v148 offset:28672\n"
    "ds_read_b128 v[186:189], v148 offset:40960\n"
    "v_mfma_f32_16x16x32_bf16 a[16:19], v[36:39], v[162:165], a[16:19]\n"
    "v_exp_f32 v95, v95\n"
    "v_mfma_f32_16x16x32_bf16 a[20:23], v[44:47], v[162:165], a[20:23]\n"
    "v_add_f32 v147, v147, v94\n"
    "v_add_f32 v147, v147, v95\n"
    "v_mfma_f32_16x16x32_bf16 a[20:23], v[40:43], v[158:161], a[20:23]\n"
    "v_mfma_f32_16x16x32_bf16 a[24:27], v[48:51], v[158:161], a[24:27]\n"
    "v_exp_f32 v96, v96\n"
    "v_mfma_f32_16x16x32_bf16 a[24:27], v[52:55], v[162:165], a[24:27]\n"
    "buffer_load_dwordx4 v149, s[36:39], s72 offen offset:1024 sc1 lds\n"
    "v_mfma_f32_16x16x32_bf16 a[28:31], v[60:63], v[162:165], a[28:31]\n"
    "v_add_f32 v144, v144, v96\n"
    "v_mfma_f32_16x16x32_bf16 a[28:31], v[56:59], v[158:161], a[28:31]\n"
    "v_exp_f32 v97, v97\n"
    "s_waitcnt lgkmcnt(0)\n"
    "v_mfma_f32_16x16x32_bf16 a[32:35], v[32:35], v[166:169], a[32:35]\n"
    "ds_read_b128 v[150:153], v148 offset:29696\n"
    "v_add_f32 v144, v144, v97\n"
    "v_mfma_f32_16x16x32_bf16 a[32:35], v[36:39], v[170:173], a[32:35]\n"
    "ds_read_b128 v[154:157], v148 offset:41984\n"
    "v_mfma_f32_16x16x32_bf16 a[36:39], v[44:47], v[170:173], a[36:39]\n"
    "v_exp_f32 v98, v98\n"
    "v_mfma_f32_16x16x32_bf16 a[36:39], v[40:43], v[166:169], a[36:39]\n"
    "buffer_load_dwordx4 v149, s[36:39], s72 offen offset:2048 sc1 lds\n"
    "v_mfma_f32_16x16x32_bf16 a[40:43], v[48:51], v[166:169], a[40:43]\n"
    "v_add_f32 v144, v144, v98\n"
    "v_mfma_f32_16x16x32_bf16 a[40:43], v[52:55], v[170:173], a[40:43]\n"
    "v_exp_f32 v99, v99\n"
    "v_mfma_f32_16x16x32_bf16 a[44:47], v[60:63], v[170:173], a[44:47]\n"
    "v_add_f32 v144, v144, v99\n"
    "v_mfma_f32_16x16x32_bf16 a[44:47], v[56:59], v[166:169], a[44:47]\n"
    "ds_read_b128 v[158:161], v148 offset:30720\n"
    "v_mfma_f32_16x16x32_bf16 a[48:51], v[32:35], v[174:177], a[48:51]\n"
    "v_exp_f32 v100, v100\n"
    "v_mfma_f32_16x16x32_bf16 a[48:51], v[36:39], v[178:181], a[48:51]\n"
    "ds_read_b128 v[162:165], v148 offset:43008\n"
    "v_add_f32 v145, v145, v100\n"
    "v_mfma_f32_16x16x32_bf16 a[52:55], v[44:47], v[178:181], a[52:55]\n"
    "buffer_load_dwordx4 v149, s[36:39], s72 offen offset:3072 sc1 lds\n"
    "v_mfma_f32_16x16x32_bf16 a[52:55], v[40:43], v[174:177], a[52:55]\n"
    "v_exp_f32 v101, v101\n"
    "v_mfma_f32_16x16x32_bf16 a[56:59], v[48:51], v[174:177], a[56:59]\n"
    "s_add_u32 s72, s72, 4096\n"
    "v_add_f32 v145, v145, v101\n"
    "v_mfma_f32_16x16x32_bf16 a[56:59], v[52:55], v[178:181], a[56:59]\n"
    "s_add_u32 m0, m0, 4096\n"
    "v_mfma_f32_16x16x32_bf16 a[60:63], v[60:63], v[178:181], a[60:63]\n"
    "v_exp_f32 v102, v102\n"
    "v_mfma_f32_16x16x32_bf16 a[60:63], v[56:59], v[174:177], a[60:63]\n"
    "v_add_f32 v145, v145, v102\n"
    "v_mfma_f32_16x16x32_bf16 a[64:67], v[32:35], v[182:185], a[64:67]\n"
    "ds_read_b128 v[166:169], v148 offset:31744\n"
    "ds_read_b128 v[170:173], v148 offset:44032\n"
    "v_mfma_f32_16x16x32_bf16 a[64:67], v[36:39], v[186:189], a[64:67]\n"
    "v_exp_f32 v103, v103\n"
    "v_mfma_f32_16x16x32_bf16 a[68:71], v[44:47], v[186:189], a[68:71]\n"
    "v_add_f32 v145, v145, v103\n"
    "v_mfma_f32_16x16x32_bf16 a[68:71], v[40:43], v[182:185], a[68:71]\n"
    "buffer_load_dwordx4 v149, s[36:39], s72 offen offset:0 sc1 lds\n"
    "v_mfma_f32_16x16x32_bf16 a[72:75], v[48:51], v[182:185], a[72:75]\n"
    "v_exp_f32 v104, v104\n"
    "v_mfma_f32_16x16x32_bf16 a[72:75], v[52:55], v[186:189], a[72:75]\n"
    "v_add_f32 v146, v146, v104\n"
    "v_mfma_f32_16x16x32_bf16 a[76:79], v[60:63], v[186:189], a[76:79]\n"
    "v_mfma_f32_16x16x32_bf16 a[76:79], v[56:59], v[182:185], a[76:79]\n"
    "v_exp_f32 v105, v105\n"
    "s_waitcnt lgkmcnt(0)\n"
    "v_mfma_f32_16x16x32_bf16 a[80:83], v[32:35], v[150:153], a[80:83]\n"
    "v_add_f32 v146, v146, v105\n"
    "v_mfma_f32_16x16x32_bf16 a[80:83], v[36:39], v[154:157], a[80:83]\n"
    "buffer_load_dwordx4 v149, s[36:39], s72 offen offset:1024 sc1 lds\n"
    "v_mfma_f32_16x16x32_bf16 a[84:87], v[44:47], v[154:157], a[84:87]\n"
    "v_exp_f32 v106, v106\n"
    "v_mfma_f32_16x16x32_bf16 a[84:87], v[40:43], v[150:153], a[84:87]\n"
    "v_add_f32 v146, v146, v106\n"
    "v_mfma_f32_16x16x32_bf16 a[88:91], v[48:51], v[150:153], a[88:91]\n"
    "v_mfma_f32_16x16x32_bf16 a[88:91], v[52:55], v[154:157], a[88:91]\n"
    "v_exp_f32 v107, v107\n"
    "v_mfma_f32_16x16x32_bf16 a[92:95], v[60:63], v[154:157], a[92:95]\n"
    "buffer_load_dwordx4 v149, s[36:39], s72 offen offset:2048 sc1 lds\n"
    "v_mfma_f32_16x16x32_bf16 a[92:95], v[56:59], v[150:153], a[92:95]\n"
    "ds_read_b128 v[150:153], v148 offset:32768\n"
    "ds_read_b128 v[154:157], v148 offset:45056\n"
    "v_mfma_f32_16x16x32_bf16 a[96:99], v[32:35], v[158:161], a[96:99]\n"
    "v_exp_f32 v108, v108\n"
    "v_mfma_f32_16x16x32_bf16 a[96:99], v[36:39], v[162:165], a[96:99]\n"
    "v_add_f32 v146, v146, v107\n"
    "v_add_f32 v147, v147, v108\n"
    "v_mfma_f32_16x16x32_bf16 a[100:103], v[44:47], v[162:165], a[100:103]\n"
    "v_mfma_f32_16x16x32_bf16 a[100:103], v[40:43], v[158:161], a[100:103]\n"
    "v_exp_f32 v109, v109\n"
    "v_mfma_f32_16x16x32_bf16 a[104:107], v[48:51], v[158:161], a[104:107]\n"
    "buffer_load_dwordx4 v149, s[36:39], s72 offen offset:3072 sc1 lds\n"
    "v_mfma_f32_16x16x32_bf16 a[104:107], v[52:55], v[162:165], a[104:107]\n"
    "s_add_u32 s72, s72, 16384\n"
    "v_add_f32 v147, v147, v109\n"
    "v_mfma_f32_16x16x32_bf16 a[108:111], v[60:63], v[162:165], a[108:111]\n"
    "v_exp_f32 v110, v110\n"
    "v_mfma_f32_16x16x32_bf16 a[108:111], v[56:59], v[158:161], a[108:111]\n"
    "ds_read_b128 v[158:161], v148 offset:33792\n"
    "ds_read_b128 v[162:165], v148 offset:46080\n"
    "v_mfma_f32_16x16x32_bf16 a[112:115], v[32:35], v[166:169], a[112:115]\n"
    "ds_read_b128 v[174:177], v148 offset:34816\n"
    "ds_read_b128 v[178:181], v148 offset:47104\n"
    "v_mfma_f32_16x16x32_bf16 a[112:115], v[36:39], v[170:173], a[112:115]\n"
    "v_exp_f32 v111, v111\n"
    "v_mfma_f32_16x16x32_bf16 a[116:119], v[44:47], v[170:173], a[116:119]\n"
    "ds_read_b128 v[182:185], v148 offset:35840\n"
    "v_add_f32 v147, v147, v110\n"
    "v_mfma_f32_16x16x32_bf16 a[116:119], v[40:43], v[166:169], a[116:119]\n"
    "ds_read_b128 v[186:189], v148 offset:48128\n"
    "v_add_f32 v147, v147, v111\n"
    "v_mfma_f32_16x16x32_bf16 a[120:123], v[48:51], v[166:169], a[120:123]\n"
    "v_exp_f32 v112, v112\n"
    "v_mfma_f32_16x16x32_bf16 a[120:123], v[52:55], v[170:173], a[120:123]\n"
    "v_add_f32 v144, v144, v112\n"
    "v_mfma_f32_16x16x32_bf16 a[124:127], v[60:63], v[170:173], a[124:127]\n"
    "v_mfma_f32_16x16x32_bf16 a[124:127], v[56:59], v[166:169], a[124:127]\n"
    "v_exp_f32 v113, v113\n"
    "s_waitcnt lgkmcnt(2)\n"
    "v_mfma_f32_16x16x32_bf16 a[128:131], v[32:35], v[150:153], a[128:131]\n"
    "v_add_f32 v144, v144, v113\n"
    "v_mfma_f32_16x16x32_bf16 a[128:131], v[36:39], v[154:157], a[128:131]\n"
    "v_mfma_f32_16x16x32_bf16 a[132:135], v[44:47], v[154:157], a[132:135]\n"
    "v_exp_f32 v114, v114\n"
    "v_mfma_f32_16x16x32_bf16 a[132:135], v[40:43], v[150:153], a[132:135]\n"
    "s_waitcnt vmcnt(13) lgkmcnt(0)\n"
    "s_barrier\n"
    "v_mfma_f32_16x16x32_bf16 a[136:139], v[48:51], v[150:153], a[136:139]\n"
    "v_add_u32 v254, s73, v254\n"
    "v_add_u32 v251, s73, v251\n"
    "v_mfma_f32_16x16x32_bf16 a[136:139], v[52:55], v[154:157], a[136:139]\n"
    "v_exp_f32 v115, v115\n"
    "v_mfma_f32_16x16x32_bf16 a[140:143], v[60:63], v[154:157], a[140:143]\n"
    "buffer_load_dwordx2 v[252:253], v250, s[64:67], 0 offen offset:2304 sc1\n"
    "v_mfma_f32_16x16x32_bf16 a[140:143], v[56:59], v[150:153], a[140:143]\n"
    "ds_read_b128 v[150:153], v254 offset:0\n"
    "v_add_f32 v144, v144, v114\n"
    "v_mfma_f32_16x16x32_bf16 a[144:147], v[32:35], v[158:161], a[144:147]\n"
    "v_exp_f32 v116, v116\n"
    "v_mfma_f32_16x16x32_bf16 a[144:147], v[36:39], v[162:165], a[144:147]\n"
    "ds_read_b128 v[154:157], v251 offset:0\n"
    "v_add_f32 v144, v144, v115\n"
    "v_mfma_f32_16x16x32_bf16 a[148:151], v[44:47], v[162:165], a[148:151]\n"
    "v_add_f32 v145, v145, v116\n"
    "v_mfma_f32_16x16x32_bf16 a[148:151], v[40:43], v[158:161], a[148:151]\n"
    "v_exp_f32 v117, v117\n"
    "v_mfma_f32_16x16x32_bf16 a[152:155], v[48:51], v[158:161], a[152:155]\n"
    "v_add_f32 v145, v145, v117\n"
    "v_mfma_f32_16x16x32_bf16 a[152:155], v[52:55], v[162:165], a[152:155]\n"
    "v_mfma_f32_16x16x32_bf16 a[156:159], v[60:63], v[162:165], a[156:159]\n"
    "v_exp_f32 v118, v118\n"
    "v_mfma_f32_16x16x32_bf16 a[156:159], v[56:59], v[158:161], a[156:159]\n"
    "ds_read_b128 v[158:161], v254 offset:2048\n"
    "ds_read_b128 v[162:165], v251 offset:2048\n"
    "v_mfma_f32_16x16x32_bf16 a[160:163], v[32:35], v[174:177], a[160:163]\n"
    "v_add_u32 v148, s73, v148\n"
    "ds_read_b128 v[166:169], v254 offset:4096\n"
    "v_mfma_f32_16x16x32_bf16 a[160:163], v[36:39], v[178:181], a[160:163]\n"
    "v_exp_f32 v119, v119\n"
    "v_mfma_f32_16x16x32_bf16 a[164:167], v[44:47], v[178:181], a[164:167]\n"
    "ds_read_b128 v[170:173], v251 offset:4096\n"
    "v_add_f32 v145, v145, v118\n"
    "v_mfma_f32_16x16x32_bf16 a[164:167], v[40:43], v[174:177], a[164:167]\n"
    "v_add_f32 v145, v145, v119\n"
    "v_mfma_f32_16x16x32_bf16 a[168:171], v[48:51], v[174:177], a[168:171]\n"
    "v_exp_f32 v120, v120\n"
    "v_mfma_f32_16x16x32_bf16 a[168:171], v[52:55], v[178:181], a[168:171]\n"
    "v_add_f32 v146, v146, v120\n"
    "v_mfma_f32_16x16x32_bf16 a[172:175], v[60:63], v[178:181], a[172:175]\n"
    "v_mfma_f32_16x16x32_bf16 a[172:175], v[56:59], v[174:177], a[172:175]\n"
    "v_exp_f32 v121, v121\n"
    "v_mfma_f32_16x16x32_bf16 a[176:179], v[32:35], v[182:185], a[176:179]\n"
    "v_cvt_pk_bf16_f32 v32, v64, v65\n"
    "v_add_f32 v146, v146, v121\n"
    "v_mfma_f32_16x16x32_bf16 a[176:179], v[36:39], v[186:189], a[176:179]\n"
    "v_cvt_pk_bf16_f32 v33, v66, v67\n"
    "v_mfma_f32_16x16x32_bf16 a[180:183], v[44:47], v[186:189], a[180:183]\n"
    "v_exp_f32 v122, v122\n"
    "v_mfma_f32_16x16x32_bf16 a[180:183], v[40:43], v[182:185], a[180:183]\n"
    "v_add_f32 v146, v146, v122\n"
    "v_mfma_f32_16x16x32_bf16 a[184:187], v[48:51], v[182:185], a[184:187]\n"
    "v_cvt_pk_bf16_f32 v40, v68, v69\n"
    "v_mfma_f32_16x16x32_bf16 a[184:187], v[52:55], v[186:189], a[184:187]\n"
    "v_exp_f32 v123, v123\n"
    "v_mfma_f32_16x16x32_bf16 a[188:191], v[60:63], v[186:189], a[188:191]\n"
    "s_cmp_lt_u32 s72, 835584\n"
    "v_add_f32 v146, v146, v123\n"
    "v_mfma_f32_16x16x32_bf16 a[188:191], v[56:59], v[182:185], a[188:191]\n"
    "ds_read_b128 v[174:177], v254 offset:6144\n"
    "s_cbranch_scc1 PLAIN%=\n"
    "v_exp_f32 v124, v124\n"
    "v_cvt_pk_bf16_f32 v41, v70, v71\n"
    "v_exp_f32 v125, v125\n"
    "v_cvt_pk_bf16_f32 v48, v72, v73\n"
    "v_add_f32 v147, v147, v124\n"
    "v_cvt_pk_bf16_f32 v49, v74, v75\n"
    "v_add_f32 v147, v147, v125\n"
    "v_exp_f32 v126, v126\n"
    "s_nop 0\n"
    "v_add_f32 v147, v147, v126\n"
    "v_cvt_pk_bf16_f32 v56, v76, v77\n"
    "v_exp_f32 v127, v127\n"
    "s_nop 0\n"
    "v_add_f32 v147, v147, v127\n"
    "v_cvt_pk_bf16_f32 v57, v78, v79\n"
    "v_cvt_pk_bf16_f32 v34, v80, v81\n"
    "v_cvt_pk_bf16_f32 v35, v82, v83\n"
    "v_cvt_pk_bf16_f32 v42, v84, v85\n"
    "v_cvt_pk_bf16_f32 v43, v86, v87\n"
    "v_cvt_pk_bf16_f32 v50, v88, v89\n"
    "v_cvt_pk_bf16_f32 v51, v90, v91\n"
    "v_cvt_pk_bf16_f32 v58, v92, v93\n"
    "v_cvt_pk_bf16_f32 v59, v94, v95\n"
    "v_cvt_pk_bf16_f32 v36, v96, v97\n"
    "v_cvt_pk_bf16_f32 v37, v98, v99\n"
    "v_cvt_pk_bf16_f32 v44, v100, v101\n"
    "v_cvt_pk_bf16_f32 v45, v102, v103\n"
    "v_cvt_pk_bf16_f32 v52, v104, v105\n"
    "v_cvt_pk_bf16_f32 v53, v106, v107\n"
    "v_cvt_pk_bf16_f32 v60, v108, v109\n"
    "v_cvt_pk_bf16_f32 v61, v110, v111\n"
    "v_cvt_pk_bf16_f32 v38, v112, v113\n"
    "v_cvt_pk_bf16_f32 v39, v114, v115\n"
    "v_cvt_pk_bf16_f32 v46, v116, v117\n"
    "s_waitcnt lgkmcnt(6)\n"
    "ds_read_b128 v[150:153], v148 offset:24576\n"
    "s_waitcnt lgkmcnt(6)\n"
    "ds_read_b128 v[154:157], v148 offset:36864\n"
    "v_cvt_pk_bf16_f32 v47, v118, v119\n"
    "v_cvt_pk_bf16_f32 v54, v120, v121\n"
    "v_cvt_pk_bf16_f32 v55, v122, v123\n"
    "s_waitcnt lgkmcnt(6)\n"
    "ds_read_b128 v[158:161], v148 offset:25600\n"
    "s_waitcnt lgkmcnt(6)\n"
    "ds_read_b128 v[162:165], v148 offset:37888\n"
    "v_cvt_pk_bf16_f32 v62, v124, v125\n"
    "v_cvt_pk_bf16_f32 v63, v126, v127\n"
    "s_waitcnt lgkmcnt(6)\n"
    "ds_read_b128 v[166:169], v148 offset:26624\n"
    "s_waitcnt lgkmcnt(6)\n"
    "ds_read_b128 v[170:173], v148 offset:38912\n"
    "s_waitcnt lgkmcnt(6)\n"
    "ds_read_b128 v[174:177], v148 offset:27648\n"
    "v_mov_b32 v190, v144\n"
    "v_mov_b32 v191, v145\n"
    "v_mov_b32 v192, v146\n"
    "v_mov_b32 v193, v147\n"
    "v_permlane16_swap_b32 v144, v190\n"
    "v_permlane16_swap_b32 v145, v191\n"
    "v_permlane16_swap_b32 v146, v192\n"
    "v_permlane16_swap_b32 v147, v193\n"
    "v_add_f32 v144, v144, v190\n"
    "v_add_f32 v145, v145, v191\n"
    "v_add_f32 v146, v146, v192\n"
    "v_add_f32 v147, v147, v193\n"
    "v_mov_b32 v190, v144\n"
    "v_mov_b32 v191, v145\n"
    "v_mov_b32 v192, v146\n"
    "v_mov_b32 v193, v147\n"
    "v_permlane32_swap_b32 v144, v190\n"
    "v_permlane32_swap_b32 v145, v191\n"
    "v_permlane32_swap_b32 v146, v192\n"
    "v_permlane32_swap_b32 v147, v193\n"
    "v_add_f32 v144, v144, v190\n"
    "v_add_f32 v145, v145, v191\n"
    "v_add_f32 v146, v146, v192\n"
    "v_add_f32 v147, v147, v193\n"
    "s_waitcnt lgkmcnt(6)\n"
    "v_mfma_f32_16x16x32_bf16 a[0:3], v[32:35], v[150:153], a[0:3]\n"
    "v_mbcnt_lo_u32_b32 v198, -1, 0\n"
    "v_mbcnt_hi_u32_b32 v198, -1, v198\n"
    "v_and_b32 v199, 15, v198\n"
    "s_waitcnt lgkmcnt(5)\n"
    "v_mfma_f32_16x16x32_bf16 a[0:3], v[36:39], v[154:157], a[0:3]\n"
    "v_lshrrev_b32 v200, 4, v198\n"
    "v_rcp_f32 v190, v144\n"
    "v_rcp_f32 v191, v145\n"
    "ds_read_b128 v[178:181], v148 offset:39936\n"
    "v_mfma_f32_16x16x32_bf16 a[4:7], v[44:47], v[154:157], a[4:7]\n"
    "v_rcp_f32 v192, v146\n"
    "v_rcp_f32 v193, v147\n"
    "s_mov_b32 s90, 0x53800000\n"
    "v_mfma_f32_16x16x32_bf16 a[4:7], v[40:43], v[150:153], a[4:7]\n"
    "v_mov_b32 v194, s90\n"
    "v_cmp_nlt_f32 s[82:83], v144, v194\n"
    "v_cmp_nlt_f32 vcc, v145, v194\n"
    "v_mfma_f32_16x16x32_bf16 a[8:11], v[48:51], v[150:153], a[8:11]\n"
    "s_nop 2\n"
    "s_or_b64 s[82:83], s[82:83], vcc\n"
    "v_cmp_nlt_f32 vcc, v146, v194\n"
    "s_nop 3\n"
    "s_or_b64 s[82:83], s[82:83], vcc\n"
    "v_mfma_f32_16x16x32_bf16 a[8:11], v[52:55], v[154:157], a[8:11]\n"
    "v_cmp_nlt_f32 vcc, v147, v194\n"
    "s_nop 3\n"
    "s_or_b64 s[82:83], s[82:83], vcc\n"
    "s_lshl_b32 s88, %11, 8\n"
    "v_mfma_f32_16x16x32_bf16 a[12:15], v[60:63], v[154:157], a[12:15]\n"
    "s_add_u32 s88, s88, s77\n"
    "v_lshl_add_u32 v201, v199, 2, s88\n"
    "v_lshl_add_u32 v202, v200, 4, s88\n"
    "v_mfma_f32_16x16x32_bf16 a[12:15], v[56:59], v[150:153], a[12:15]\n"
    "ds_write_b32 v201, v190 offset:0\n"
    "ds_write_b32 v201, v191 offset:64\n"
    "ds_write_b32 v201, v192 offset:128\n"
    "s_waitcnt lgkmcnt(8)\n"
    "v_mfma_f32_16x16x32_bf16 a[16:19], v[32:35], v[158:161], a[16:19]\n"
    "ds_write_b32 v201, v193 offset:192\n"
    "s_waitcnt lgkmcnt(0)\n"
    "ds_read_b128 v[206:209], v202 offset:0\n"
    "ds_read_b128 v[182:185], v148 offset:28672\n"
    "ds_read_b128 v[186:189], v148 offset:40960\n"
    "v_mfma_f32_16x16x32_bf16 a[16:19], v[36:39], v[162:165], a[16:19]\n"
    "ds_read_b128 v[210:213], v202 offset:64\n"
    "ds_read_b128 v[214:217], v202 offset:128\n"
    "ds_read_b128 v[218:221], v202 offset:192\n"
    "v_mfma_f32_16x16x32_bf16 a[20:23], v[44:47], v[162:165], a[20:23]\n"
    "s_mul_i32 s88, %11, 24576\n"
    "s_add_u32 s88, s88, 0\n"
    "v_lshlrev_b32 v224, 8, v200\n"
    "v_mfma_f32_16x16x32_bf16 a[20:23], v[40:43], v[158:161], a[20:23]\n"
    "v_lshl_add_u32 v224, v199, 2, v224\n"
    "v_add_u32 v224, s88, v224\n"
    "v_add_u32 v225, 64, v224\n"
    "v_mfma_f32_16x16x32_bf16 a[24:27], v[48:51], v[158:161], a[24:27]\n"
    "v_add_u32 v226, 128, v224\n"
    "v_add_u32 v227, 192, v224\n"
    "v_lshrrev_b32 v205, 3, v198\n"
    "v_mfma_f32_16x16x32_bf16 a[24:27], v[52:55], v[162:165], a[24:27]\n"
    "v_bfe_u32 v204, v198, 2, 1\n"
    "v_lshlrev_b32 v204, 12, v204\n"
    "v_lshl_add_u32 v204, v205, 6, v204\n"
    "v_mfma_f32_16x16x32_bf16 a[28:31], v[60:63], v[162:165], a[28:31]\n"
    "v_and_b32 v223, 3, v198\n"
    "v_lshl_add_u32 v204, v223, 4, v204\n"
    "v_add_u32 v204, s88, v204\n"
    "v_mfma_f32_16x16x32_bf16 a[28:31], v[56:59], v[158:161], a[28:31]\n"
    "v_mul_u32_u24 v205, 384, v205\n"
    "v_and_b32 v223, 7, v198\n"
    "v_lshl_add_u32 v205, v223, 4, v205\n"
    "v_mfma_f32_16x16x32_bf16 a[32:35], v[32:35], v[166:169], a[32:35]\n"
    "s_mov_b64 s[68:69], %10\n"
    "s_and_b32 s69, s69, 0xffff\n"
    "s_mov_b32 s70, 24576\n"
    "ds_read_b128 v[150:153], v148 offset:29696\n"
    "v_mfma_f32_16x16x32_bf16 a[32:35], v[36:39], v[170:173], a[32:35]\n"
    "s_mov_b32 s71, 0x20000\n"
    "s_mov_b32 s40, 0\n"
    "s_mov_b32 s41, 3072\n"
    "ds_read_b128 v[154:157], v148 offset:41984\n"
    "v_mfma_f32_16x16x32_bf16 a[36:39], v[44:47], v[170:173], a[36:39]\n"
    "s_mov_b32 s42, 6144\n"
    "s_mov_b32 s43, 9216\n"
    "s_mov_b32 s44, 12288\n"
    "v_mfma_f32_16x16x32_bf16 a[36:39], v[40:43], v[166:169], a[36:39]\n"
    "s_mov_b32 s45, 15360\n"
    "s_mov_b32 s46, 18432\n"
    "s_mov_b32 s47, 21504\n"
    "v_mfma_f32_16x16x32_bf16 a[40:43], v[48:51], v[166:169], a[40:43]\n"
    "v_mov_b32 v230, 0\n"
    "v_mov_b32 v231, 0\n"
    "v_mov_b32 v232, 0\n"
    "v_mfma_f32_16x16x32_bf16 a[40:43], v[52:55], v[170:173], a[40:43]\n"
    "v_mov_b32 v233, 0\n"
    "v_mov_b32 v234, 0\n"
    "v_mov_b32 v235, 0\n"
    "v_mfma_f32_16x16x32_bf16 a[44:47], v[60:63], v[170:173], a[44:47]\n"
    "v_mov_b32 v236, 0\n"
    "v_mov_b32 v237, 0\n"
    "s_waitcnt vmcnt(0)\n"
    "v_mfma_f32_16x16x32_bf16 a[44:47], v[56:59], v[166:169], a[44:47]\n"
    "s_barrier\n"
    "ds_read_b128 v[158:161], v148 offset:30720\n"
    "v_mfma_f32_16x16x32_bf16 a[48:51], v[32:35], v[174:177], a[48:51]\n"
    "v_mfma_f32_16x16x32_bf16 a[48:51], v[36:39], v[178:181], a[48:51]\n"
    "ds_read_b128 v[162:165], v148 offset:43008\n"
    "v_mfma_f32_16x16x32_bf16 a[52:55], v[44:47], v[178:181], a[52:55]\n"
    "v_mfma_f32_16x16x32_bf16 a[52:55], v[40:43], v[174:177], a[52:55]\n"
    "v_mfma_f32_16x16x32_bf16 a[56:59], v[48:51], v[174:177], a[56:59]\n"
    "v_mfma_f32_16x16x32_bf16 a[56:59], v[52:55], v[178:181], a[56:59]\n"
    "v_mfma_f32_16x16x32_bf16 a[60:63], v[60:63], v[178:181], a[60:63]\n"
    "v_mfma_f32_16x16x32_bf16 a[60:63], v[56:59], v[174:177], a[60:63]\n"
    "s_waitcnt lgkmcnt(8)\n"
    "v_mfma_f32_16x16x32_bf16 a[64:67], v[32:35], v[182:185], a[64:67]\n"
    "ds_read_b128 v[166:169], v148 offset:31744\n"
    "ds_read_b128 v[170:173], v148 offset:44032\n"
    "s_waitcnt lgkmcnt(9)\n"
    "v_mfma_f32_16x16x32_bf16 a[64:67], v[36:39], v[186:189], a[64:67]\n"
    "v_mfma_f32_16x16x32_bf16 a[68:71], v[44:47], v[186:189], a[68:71]\n"
    "v_accvgpr_read_b32 v64, a0\n"
    "v_accvgpr_read_b32 v65, a16\n"
    "v_accvgpr_read_b32 v66, a32\n"
    "v_accvgpr_read_b32 v67, a48\n"
    "v_pk_mul_f32 v[64:65], v[64:65], v[206:207] op_sel:[0,0] op_sel_hi:[1,0]\n"
    "v_pk_mul_f32 v[66:67], v[66:67], v[206:207] op_sel:[0,0] op_sel_hi:[1,0]\n"
    "v_mfma_f32_16x16x32_bf16 a[68:71], v[40:43], v[182:185], a[68:71]\n"
    "v_cvt_pk_bf16_f32 v68, v64, v65\n"
    "v_cvt_pk_bf16_f32 v69, v66, v67\n"
    "ds_write2st64_b32 v224, v68, v69 offset0:0 offset1:16\n"
    "v_accvgpr_read_b32 v72, a1\n"
    "v_accvgpr_read_b32 v73, a17\n"
    "v_accvgpr_read_b32 v74, a33\n"
    "v_mfma_f32_16x16x32_bf16 a[72:75], v[48:51], v[182:185], a[72:75]\n"
    "v_accvgpr_read_b32 v75, a49\n"
    "v_pk_mul_f32 v[72:73], v[72:73], v[206:207] op_sel:[0,1] op_sel_hi:[1,1]\n"
    "v_pk_mul_f32 v[74:75], v[74:75], v[206:207] op_sel:[0,1] op_sel_hi:[1,1]\n"
    "v_cvt_pk_bf16_f32 v76, v72, v73\n"
    "v_cvt_pk_bf16_f32 v77, v74, v75\n"
    "ds_write2st64_b32 v225, v76, v77 offset0:0 offset1:16\n"
    "v_mfma_f32_16x16x32_bf16 a[72:75], v[52:55], v[186:189], a[72:75]\n"
    "v_accvgpr_read_b32 v80, a2\n"
    "v_accvgpr_read_b32 v81, a18\n"
    "v_accvgpr_read_b32 v82, a34\n"
    "v_accvgpr_read_b32 v83, a50\n"
    "v_pk_mul_f32 v[80:81], v[80:81], v[208:209] op_sel:[0,0] op_sel_hi:[1,0]\n"
    "v_pk_mul_f32 v[82:83], v[82:83], v[208:209] op_sel:[0,0] op_sel_hi:[1,0]\n"
    "v_mfma_f32_16x16x32_bf16 a[76:79], v[60:63], v[186:189], a[76:79]\n"
    "v_cvt_pk_bf16_f32 v84, v80, v81\n"
    "v_cvt_pk_bf16_f32 v85, v82, v83\n"
    "ds_write2st64_b32 v226, v84, v85 offset0:0 offset1:16\n"
    "v_accvgpr_read_b32 v88, a3\n"
    "v_accvgpr_read_b32 v89, a19\n"
    "v_accvgpr_read_b32 v90, a35\n"
    "v_mfma_f32_16x16x32_bf16 a[76:79], v[56:59], v[182:185], a[76:79]\n"
    "v_accvgpr_read_b32 v91, a51\n"
    "v_pk_mul_f32 v[88:89], v[88:89], v[208:209] op_sel:[0,1] op_sel_hi:[1,1]\n"
    "v_pk_mul_f32 v[90:91], v[90:91], v[208:209] op_sel:[0,1] op_sel_hi:[1,1]\n"
    "v_cvt_pk_bf16_f32 v92, v88, v89\n"
    "v_cvt_pk_bf16_f32 v93, v90, v91\n"
    "ds_write2st64_b32 v227, v92, v93 offset0:0 offset1:16\n"
    "s_waitcnt lgkmcnt(9)\n"
    "v_mfma_f32_16x16x32_bf16 a[80:83], v[32:35], v[150:153], a[80:83]\n"
    "ds_read_b128 v[96:99], v204 offset:0\n"
    "ds_read_b128 v[100:103], v204 offset:512\n"
    "v_accvgpr_read_b32 v64, a4\n"
    "v_accvgpr_read_b32 v65, a20\n"
    "v_accvgpr_read_b32 v66, a36\n"
    "v_accvgpr_read_b32 v67, a52\n"
    "s_waitcnt lgkmcnt(10)\n"
    "v_mfma_f32_16x16x32_bf16 a[80:83], v[36:39], v[154:157], a[80:83]\n"
    "v_pk_mul_f32 v[64:65], v[64:65], v[210:211] op_sel:[0,0] op_sel_hi:[1,0]\n"
    "v_pk_mul_f32 v[66:67], v[66:67], v[210:211] op_sel:[0,0] op_sel_hi:[1,0]\n"
    "v_cvt_pk_bf16_f32 v68, v64, v65\n"
    "v_cvt_pk_bf16_f32 v69, v66, v67\n"
    "ds_write2st64_b32 v224, v68, v69 offset0:4 offset1:20\n"
    "v_accvgpr_read_b32 v72, a5\n"
    "v_mfma_f32_16x16x32_bf16 a[84:87], v[44:47], v[154:157], a[84:87]\n"
    "v_accvgpr_read_b32 v73, a21\n"
    "v_accvgpr_read_b32 v74, a37\n"
    "v_accvgpr_read_b32 v75, a53\n"
    "v_pk_mul_f32 v[72:73], v[72:73], v[210:211] op_sel:[0,1] op_sel_hi:[1,1]\n"
    "v_pk_mul_f32 v[74:75], v[74:75], v[210:211] op_sel:[0,1] op_sel_hi:[1,1]\n"
    "v_mfma_f32_16x16x32_bf16 a[84:87], v[40:43], v[150:153], a[84:87]\n"
    "v_cvt_pk_bf16_f32 v76, v72, v73\n"
    "v_cvt_pk_bf16_f32 v77, v74, v75\n"
    "ds_write2st64_b32 v225, v76, v77 offset0:4 offset1:20\n"
    "v_accvgpr_read_b32 v80, a6\n"
    "v_accvgpr_read_b32 v81, a22\n"
    "v_mfma_f32_16x16x32_bf16 a[88:91], v[48:51], v[150:153], a[88:91]\n"
    "v_accvgpr_read_b32 v82, a38\n"
    "v_accvgpr_read_b32 v83, a54\n"
    "v_pk_mul_f32 v[80:81], v[80:81], v[212:213] op_sel:[0,0] op_sel_hi:[1,0]\n"
    "v_pk_mul_f32 v[82:83], v[82:83], v[212:213] op_sel:[0,0] op_sel_hi:[1,0]\n"
    "v_cvt_pk_bf16_f32 v84, v80, v81\n"
    "v_mfma_f32_16x16x32_bf16 a[88:91], v[52:55], v[154:157], a[88:91]\n"
    "v_cvt_pk_bf16_f32 v85, v82, v83\n"
    "ds_write2st64_b32 v226, v84, v85 offset0:4 offset1:20\n"
    "v_accvgpr_read_b32 v88, a7\n"
    "v_accvgpr_read_b32 v89, a23\n"
    "v_accvgpr_read_b32 v90, a39\n"
    "v_mfma_f32_16x16x32_bf16 a[92:95], v[60:63], v[154:157], a[92:95]\n"
    "v_accvgpr_read_b32 v91, a55\n"
    "v_pk_mul_f32 v[88:89], v[88:89], v[212:213] op_sel:[0,1] op_sel_hi:[1,1]\n"
    "v_pk_mul_f32 v[90:91], v[90:91], v[212:213] op_sel:[0,1] op_sel_hi:[1,1]\n"
    "v_cvt_pk_bf16_f32 v92, v88, v89\n"
    "v_cvt_pk_bf16_f32 v93, v90, v91\n"
    "v_mfma_f32_16x16x32_bf16 a[92:95], v[56:59], v[150:153], a[92:95]\n"
    "ds_write2st64_b32 v227, v92, v93 offset0:4 offset1:20\n"
    "ds_read_b128 v[104:107], v204 offset:1024\n"
    "ds_read_b128 v[108:111], v204 offset:1536\n"
    "s_waitcnt lgkmcnt(7)\n"
    "buffer_store_dwordx4 v[96:99], v205, s[68:71], s40 offen offset:0 sc1 nt\n"
    "v_mfma_f32_16x16x32_bf16 v[234:237], v[96:99], v[230:233], v[234:237]\n"
    "ds_read_b128 v[150:153], v148 offset:32768\n"
    "ds_read_b128 v[154:157], v148 offset:45056\n"
    "v_mfma_f32_16x16x32_bf16 a[96:99], v[32:35], v[158:161], a[96:99]\n"
    "s_waitcnt lgkmcnt(8)\n"
    "buffer_store_dwordx4 v[100:103], v205, s[68:71], s41 offen offset:0 sc1 nt\n"
    "v_mfma_f32_16x16x32_bf16 v[234:237], v[100:103], v[230:233], v[234:237]\n"
    "v_accvgpr_read_b32 v64, a8\n"
    "v_accvgpr_read_b32 v65, a24\n"
    "v_accvgpr_read_b32 v66, a40\n"
    "v_mfma_f32_16x16x32_bf16 a[96:99], v[36:39], v[162:165], a[96:99]\n"
    "v_accvgpr_read_b32 v67, a56\n"
    "v_pk_mul_f32 v[64:65], v[64:65], v[214:215] op_sel:[0,0] op_sel_hi:[1,0]\n"
    "v_pk_mul_f32 v[66:67], v[66:67], v[214:215] op_sel:[0,0] op_sel_hi:[1,0]\n"
    "v_cvt_pk_bf16_f32 v68, v64, v65\n"
    "v_cvt_pk_bf16_f32 v69, v66, v67\n"
    "v_mfma_f32_16x16x32_bf16 a[100:103], v[44:47], v[162:165], a[100:103]\n"
    "ds_write2st64_b32 v224, v68, v69 offset0:8 offset1:24\n"
    "v_accvgpr_read_b32 v72, a9\n"
    "v_accvgpr_read_b32 v73, a25\n"
    "v_accvgpr_read_b32 v74, a41\n"
    "v_accvgpr_read_b32 v75, a57\n"
    "v_mfma_f32_16x16x32_bf16 a[100:103], v[40:43], v[158:161], a[100:103]\n"
    "v_pk_mul_f32 v[72:73], v[72:73], v[214:215] op_sel:[0,1] op_sel_hi:[1,1]\n"
    "v_pk_mul_f32 v[74:75], v[74:75], v[214:215] op_sel:[0,1] op_sel_hi:[1,1]\n"
    "v_cvt_pk_bf16_f32 v76, v72, v73\n"
    "v_cvt_pk_bf16_f32 v77, v74, v75\n"
    "ds_write2st64_b32 v225, v76, v77 offset0:8 offset1:24\n"
    "v_mfma_f32_16x16x32_bf16 a[104:107], v[48:51], v[158:161], a[104:107]\n"
    "v_accvgpr_read_b32 v80, a10\n"
    "v_accvgpr_read_b32 v81, a26\n"
    "v_accvgpr_read_b32 v82, a42\n"
    "v_accvgpr_read_b32 v83, a58\n"
    "v_pk_mul_f32 v[80:81], v[80:81], v[216:217] op_sel:[0,0] op_sel_hi:[1,0]\n"
    "v_mfma_f32_16x16x32_bf16 a[104:107], v[52:55], v[162:165], a[104:107]\n"
    "v_pk_mul_f32 v[82:83], v[82:83], v[216:217] op_sel:[0,0] op_sel_hi:[1,0]\n"
    "v_cvt_pk_bf16_f32 v84, v80, v81\n"
    "v_cvt_pk_bf16_f32 v85, v82, v83\n"
    "ds_write2st64_b32 v226, v84, v85 offset0:8 offset1:24\n"
    "v_accvgpr_read_b32 v88, a11\n"
    "v_mfma_f32_16x16x32_bf16 a[108:111], v[60:63], v[162:165], a[108:111]\n"
    "v_accvgpr_read_b32 v89, a27\n"
    "v_accvgpr_read_b32 v90, a43\n"
    "v_accvgpr_read_b32 v91, a59\n"
    "v_pk_mul_f32 v[88:89], v[88:89], v[216:217] op_sel:[0,1] op_sel_hi:[1,1]\n"
    "v_pk_mul_f32 v[90:91], v[90:91], v[216:217] op_sel:[0,1] op_sel_hi:[1,1]\n"
    "v_mfma_f32_16x16x32_bf16 a[108:111], v[56:59], v[158:161], a[108:111]\n"
    "v_cvt_pk_bf16_f32 v92, v88, v89\n"
    "v_cvt_pk_bf16_f32 v93, v90, v91\n"
    "ds_write2st64_b32 v227, v92, v93 offset0:8 offset1:24\n"
    "ds_read_b128 v[112:115], v204 offset:2048\n"
    "ds_read_b128 v[116:119], v204 offset:2560\n"
    "ds_read_b128 v[158:161], v148 offset:33792\n"
    "ds_read_b128 v[162:165], v148 offset:46080\n"
    "v_mfma_f32_16x16x32_bf16 a[112:115], v[32:35], v[166:169], a[112:115]\n"
    "s_waitcnt lgkmcnt(11)\n"
    "buffer_store_dwordx4 v[104:107], v205, s[68:71], s42 offen offset:0 sc1 nt\n"
    "v_mfma_f32_16x16x32_bf16 v[234:237], v[104:107], v[230:233], v[234:237]\n"
    "s_waitcnt lgkmcnt(10)\n"
    "buffer_store_dwordx4 v[108:111], v205, s[68:71], s43 offen offset:0 sc1 nt\n"
    "v_mfma_f32_16x16x32_bf16 v[234:237], v[108:111], v[230:233], v[234:237]\n"
    "v_accvgpr_read_b32 v64, a12\n"
    "ds_read_b128 v[174:177], v148 offset:34816\n"
    "ds_read_b128 v[178:181], v148 offset:47104\n"
    "v_mfma_f32_16x16x32_bf16 a[112:115], v[36:39], v[170:173], a[112:115]\n"
    "v_accvgpr_read_b32 v65, a28\n"
    "v_accvgpr_read_b32 v66, a44\n"
    "v_accvgpr_read_b32 v67, a60\n"
    "v_pk_mul_f32 v[64:65], v[64:65], v[218:219] op_sel:[0,0] op_sel_hi:[1,0]\n"
    "v_pk_mul_f32 v[66:67], v[66:67], v[218:219] op_sel:[0,0] op_sel_hi:[1,0]\n"
    "v_mfma_f32_16x16x32_bf16 a[116:119], v[44:47], v[170:173], a[116:119]\n"
    "v_cvt_pk_bf16_f32 v68, v64, v65\n"
    "v_cvt_pk_bf16_f32 v69, v66, v67\n"
    "ds_write2st64_b32 v224, v68, v69 offset0:12 offset1:28\n"
    "v_accvgpr_read_b32 v72, a13\n"
    "v_accvgpr_read_b32 v73, a29\n"
    "ds_read_b128 v[182:185], v148 offset:35840\n"
    "v_mfma_f32_16x16x32_bf16 a[116:119], v[40:43], v[166:169], a[116:119]\n"
    "v_accvgpr_read_b32 v74, a45\n"
    "v_accvgpr_read_b32 v75, a61\n"
    "v_pk_mul_f32 v[72:73], v[72:73], v[218:219] op_sel:[0,1] op_sel_hi:[1,1]\n"
    "v_pk_mul_f32 v[74:75], v[74:75], v[218:219] op_sel:[0,1] op_sel_hi:[1,1]\n"
    "v_cvt_pk_bf16_f32 v76, v72, v73\n"
    "ds_read_b128 v[186:189], v148 offset:48128\n"
    "v_mfma_f32_16x16x32_bf16 a[120:123], v[48:51], v[166:169], a[120:123]\n"
    "v_cvt_pk_bf16_f32 v77, v74, v75\n"
    "ds_write2st64_b32 v225, v76, v77 offset0:12 offset1:28\n"
    "v_accvgpr_read_b32 v80, a14\n"
    "v_accvgpr_read_b32 v81, a30\n"
    "v_accvgpr_read_b32 v82, a46\n"
    "v_mfma_f32_16x16x32_bf16 a[120:123], v[52:55], v[170:173], a[120:123]\n"
    "v_accvgpr_read_b32 v83, a62\n"
    "v_pk_mul_f32 v[80:81], v[80:81], v[220:221] op_sel:[0,0] op_sel_hi:[1,0]\n"
    "v_pk_mul_f32 v[82:83], v[82:83], v[220:221] op_sel:[0,0] op_sel_hi:[1,0]\n"
    "v_cvt_pk_bf16_f32 v84, v80, v81\n"
    "v_cvt_pk_bf16_f32 v85, v82, v83\n"
    "v_mfma_f32_16x16x32_bf16 a[124:127], v[60:63], v[170:173], a[124:127]\n"
    "ds_write2st64_b32 v226, v84, v85 offset0:12 offset1:28\n"
    "v_accvgpr_read_b32 v88, a15\n"
    "v_accvgpr_read_b32 v89, a31\n"
    "v_accvgpr_read_b32 v90, a47\n"
    "v_accvgpr_read_b32 v91, a63\n"
    "v_mfma_f32_16x16x32_bf16 a[124:127], v[56:59], v[166:169], a[124:127]\n"
    "v_pk_mul_f32 v[88:89], v[88:89], v[220:221] op_sel:[0,1] op_sel_hi:[1,1]\n"
    "v_pk_mul_f32 v[90:91], v[90:91], v[220:221] op_sel:[0,1] op_sel_hi:[1,1]\n"
    "v_cvt_pk_bf16_f32 v92, v88, v89\n"
    "v_cvt_pk_bf16_f32 v93, v90, v91\n"
    "ds_write2st64_b32 v227, v92, v93 offset0:12 offset1:28\n"
    "s_waitcnt lgkmcnt(15)\n"
    "v_mfma_f32_16x16x32_bf16 a[128:131], v[32:35], v[150:153], a[128:131]\n"
    "ds_read_b128 v[120:123], v204 offset:3072\n"
    "ds_read_b128 v[124:127], v204 offset:3584\n"
    "s_waitcnt lgkmcnt(13)\n"
    "buffer_store_dwordx4 v[112:115], v205, s[68:71], s44 offen offset:0 sc1 nt\n"
    "v_mfma_f32_16x16x32_bf16 v[234:237], v[112:115], v[230:233], v[234:237]\n"
    "s_waitcnt lgkmcnt(12)\n"
    "buffer_store_dwordx4 v[116:119], v205, s[68:71], s45 offen offset:0 sc1 nt\n"
    "v_mfma_f32_16x16x32_bf16 a[128:131], v[36:39], v[154:157], a[128:131]\n"
    "v_mfma_f32_16x16x32_bf16 v[234:237], v[116:119], v[230:233], v[234:237]\n"
    "s_waitcnt lgkmcnt(1)\n"
    "buffer_store_dwordx4 v[120:123], v205, s[68:71], s46 offen offset:0 sc1 nt\n"
    "v_mfma_f32_16x16x32_bf16 v[234:237], v[120:123], v[230:233], v[234:237]\n"
    "s_waitcnt lgkmcnt(0)\n"
    "buffer_store_dwordx4 v[124:127], v205, s[68:71], s47 offen offset:0 sc1 nt\n"
    "v_mfma_f32_16x16x32_bf16 v[234:237], v[124:127], v[230:233], v[234:237]\n"
    "v_mfma_f32_16x16x32_bf16 a[132:135], v[44:47], v[154:157], a[132:135]\n"
    "v_accvgpr_read_b32 v64, a64\n"
    "v_accvgpr_read_b32 v65, a80\n"
    "v_accvgpr_read_b32 v66, a96\n"
    "v_accvgpr_read_b32 v67, a112\n"
    "v_pk_mul_f32 v[64:65], v[64:65], v[206:207] op_sel:[0,0] op_sel_hi:[1,0]\n"
    "v_pk_mul_f32 v[66:67], v[66:67], v[206:207] op_sel:[0,0] op_sel_hi:[1,0]\n"
    "v_mfma_f32_16x16x32_bf16 a[132:135], v[40:43], v[150:153], a[132:135]\n"
    "v_cvt_pk_bf16_f32 v68, v64, v65\n"
    "v_cvt_pk_bf16_f32 v69, v66, v67\n"
    "ds_write2st64_b32 v224, v68, v69 offset0:32 offset1:48\n"
    "v_accvgpr_read_b32 v72, a65\n"
    "v_accvgpr_read_b32 v73, a81\n"
    "v_accvgpr_read_b32 v74, a97\n"
    "v_mfma_f32_16x16x32_bf16 a[136:139], v[48:51], v[150:153], a[136:139]\n"
    "v_accvgpr_read_b32 v75, a113\n"
    "v_pk_mul_f32 v[72:73], v[72:73], v[206:207] op_sel:[0,1] op_sel_hi:[1,1]\n"
    "v_pk_mul_f32 v[74:75], v[74:75], v[206:207] op_sel:[0,1] op_sel_hi:[1,1]\n"
    "v_cvt_pk_bf16_f32 v76, v72, v73\n"
    "v_cvt_pk_bf16_f32 v77, v74, v75\n"
    "ds_write2st64_b32 v225, v76, v77 offset0:32 offset1:48\n"
    "v_mfma_f32_16x16x32_bf16 a[136:139], v[52:55], v[154:157], a[136:139]\n"
    "v_accvgpr_read_b32 v80, a66\n"
    "v_accvgpr_read_b32 v81, a82\n"
    "v_accvgpr_read_b32 v82, a98\n"
    "v_accvgpr_read_b32 v83, a114\n"
    "v_pk_mul_f32 v[80:81], v[80:81], v[208:209] op_sel:[0,0] op_sel_hi:[1,0]\n"
    "v_pk_mul_f32 v[82:83], v[82:83], v[208:209] op_sel:[0,0] op_sel_hi:[1,0]\n"
    "v_mfma_f32_16x16x32_bf16 a[140:143], v[60:63], v[154:157], a[140:143]\n"
    "v_cvt_pk_bf16_f32 v84, v80, v81\n"
    "v_cvt_pk_bf16_f32 v85, v82, v83\n"
    "ds_write2st64_b32 v226, v84, v85 offset0:32 offset1:48\n"
    "v_accvgpr_read_b32 v88, a67\n"
    "v_accvgpr_read_b32 v89, a83\n"
    "v_accvgpr_read_b32 v90, a99\n"
    "v_mfma_f32_16x16x32_bf16 a[140:143], v[56:59], v[150:153], a[140:143]\n"
    "v_accvgpr_read_b32 v91, a115\n"
    "v_pk_mul_f32 v[88:89], v[88:89], v[208:209] op_sel:[0,1] op_sel_hi:[1,1]\n"
    "v_pk_mul_f32 v[90:91], v[90:91], v[208:209] op_sel:[0,1] op_sel_hi:[1,1]\n"
    "v_cvt_pk_bf16_f32 v92, v88, v89\n"
    "v_cvt_pk_bf16_f32 v93, v90, v91\n"
    "ds_write2st64_b32 v227, v92, v93 offset0:32 offset1:48\n"
    "v_mfma_f32_16x16x32_bf16 a[144:147], v[32:35], v[158:161], a[144:147]\n"
    "ds_read_b128 v[96:99], v204 offset:8192\n"
    "ds_read_b128 v[100:103], v204 offset:8704\n"
    "v_accvgpr_read_b32 v64, a68\n"
    "v_accvgpr_read_b32 v65, a84\n"
    "v_accvgpr_read_b32 v66, a100\n"
    "v_accvgpr_read_b32 v67, a116\n"
    "v_mfma_f32_16x16x32_bf16 a[144:147], v[36:39], v[162:165], a[144:147]\n"
    "v_pk_mul_f32 v[64:65], v[64:65], v[210:211] op_sel:[0,0] op_sel_hi:[1,0]\n"
    "v_pk_mul_f32 v[66:67], v[66:67], v[210:211] op_sel:[0,0] op_sel_hi:[1,0]\n"
    "v_cvt_pk_bf16_f32 v68, v64, v65\n"
    "v_cvt_pk_bf16_f32 v69, v66, v67\n"
    "ds_write2st64_b32 v224, v68, v69 offset0:36 offset1:52\n"
    "v_accvgpr_read_b32 v72, a69\n"
    "v_mfma_f32_16x16x32_bf16 a[148:151], v[44:47], v[162:165], a[148:151]\n"
    "v_accvgpr_read_b32 v73, a85\n"
    "v_accvgpr_read_b32 v74, a101\n"
    "v_accvgpr_read_b32 v75, a117\n"
    "v_pk_mul_f32 v[72:73], v[72:73], v[210:211] op_sel:[0,1] op_sel_hi:[1,1]\n"
    "v_pk_mul_f32 v[74:75], v[74:75], v[210:211] op_sel:[0,1] op_sel_hi:[1,1]\n"
    "v_cvt_pk_bf16_f32 v76, v72, v73\n"
    "v_mfma_f32_16x16x32_bf16 a[148:151], v[40:43], v[158:161], a[148:151]\n"
    "v_cvt_pk_bf16_f32 v77, v74, v75\n"
    "ds_write2st64_b32 v225, v76, v77 offset0:36 offset1:52\n"
    "v_accvgpr_read_b32 v80, a70\n"
    "v_accvgpr_read_b32 v81, a86\n"
    "v_accvgpr_read_b32 v82, a102\n"
    "v_accvgpr_read_b32 v83, a118\n"
    "v_mfma_f32_16x16x32_bf16 a[152:155], v[48:51], v[158:161], a[152:155]\n"
    "v_pk_mul_f32 v[80:81], v[80:81], v[212:213] op_sel:[0,0] op_sel_hi:[1,0]\n"
    "v_pk_mul_f32 v[82:83], v[82:83], v[212:213] op_sel:[0,0] op_sel_hi:[1,0]\n"
    "v_cvt_pk_bf16_f32 v84, v80, v81\n"
    "v_cvt_pk_bf16_f32 v85, v82, v83\n"
    "ds_write2st64_b32 v226, v84, v85 offset0:36 offset1:52\n"
    "v_accvgpr_read_b32 v88, a71\n"
    "v_mfma_f32_16x16x32_bf16 a[152:155], v[52:55], v[162:165], a[152:155]\n"
    "v_accvgpr_read_b32 v89, a87\n"
    "v_accvgpr_read_b32 v90, a103\n"
    "v_accvgpr_read_b32 v91, a119\n"
    "v_pk_mul_f32 v[88:89], v[88:89], v[212:213] op_sel:[0,1] op_sel_hi:[1,1]\n"
    "v_pk_mul_f32 v[90:91], v[90:91], v[212:213] op_sel:[0,1] op_sel_hi:[1,1]\n"
    "v_cvt_pk_bf16_f32 v92, v88, v89\n"
    "v_mfma_f32_16x16x32_bf16 a[156:159], v[60:63], v[162:165], a[156:159]\n"
    "v_cvt_pk_bf16_f32 v93, v90, v91\n"
    "ds_write2st64_b32 v227, v92, v93 offset0:36 offset1:52\n"
    "ds_read_b128 v[104:107], v204 offset:9216\n"
    "ds_read_b128 v[108:111], v204 offset:9728\n"
    "s_waitcnt lgkmcnt(7)\n"
    "buffer_store_dwordx4 v[96:99], v205, s[68:71], s40 offen offset:128 sc1 nt\n"
    "v_mfma_f32_16x16x32_bf16 v[234:237], v[96:99], v[230:233], v[234:237]\n"
    "v_mfma_f32_16x16x32_bf16 a[156:159], v[56:59], v[158:161], a[156:159]\n"
    "s_waitcnt lgkmcnt(6)\n"
    "buffer_store_dwordx4 v[100:103], v205, s[68:71], s41 offen offset:128 sc1 nt\n"
    "v_mfma_f32_16x16x32_bf16 v[234:237], v[100:103], v[230:233], v[234:237]\n"
    "v_accvgpr_read_b32 v64, a72\n"
    "v_accvgpr_read_b32 v65, a88\n"
    "v_accvgpr_read_b32 v66, a104\n"
    "v_accvgpr_read_b32 v67, a120\n"
    "v_mfma_f32_16x16x32_bf16 a[160:163], v[32:35], v[174:177], a[160:163]\n"
    "v_pk_mul_f32 v[64:65], v[64:65], v[214:215] op_sel:[0,0] op_sel_hi:[1,0]\n"
    "v_pk_mul_f32 v[66:67], v[66:67], v[214:215] op_sel:[0,0] op_sel_hi:[1,0]\n"
    "v_cvt_pk_bf16_f32 v68, v64, v65\n"
    "v_cvt_pk_bf16_f32 v69, v66, v67\n"
    "ds_write2st64_b32 v224, v68, v69 offset0:40 offset1:56\n"
    "v_accvgpr_read_b32 v72, a73\n"
    "v_mfma_f32_16x16x32_bf16 a[160:163], v[36:39], v[178:181], a[160:163]\n"
    "v_accvgpr_read_b32 v73, a89\n"
    "v_accvgpr_read_b32 v74, a105\n"
    "v_accvgpr_read_b32 v75, a121\n"
    "v_pk_mul_f32 v[72:73], v[72:73], v[214:215] op_sel:[0,1] op_sel_hi:[1,1]\n"
    "v_pk_mul_f32 v[74:75], v[74:75], v[214:215] op_sel:[0,1] op_sel_hi:[1,1]\n"
    "v_cvt_pk_bf16_f32 v76, v72, v73\n"
    "v_mfma_f32_16x16x32_bf16 a[164:167], v[44:47], v[178:181], a[164:167]\n"
    "v_cvt_pk_bf16_f32 v77, v74, v75\n"
    "ds_write2st64_b32 v225, v76, v77 offset0:40 offset1:56\n"
    "v_accvgpr_read_b32 v80, a74\n"
    "v_accvgpr_read_b32 v81, a90\n"
    "v_accvgpr_read_b32 v82, a106\n"
    "v_accvgpr_read_b32 v83, a122\n"
    "v_mfma_f32_16x16x32_bf16 a[164:167], v[40:43], v[174:177], a[164:167]\n"
    "v_pk_mul_f32 v[80:81], v[80:81], v[216:217] op_sel:[0,0] op_sel_hi:[1,0]\n"
    "v_pk_mul_f32 v[82:83], v[82:83], v[216:217] op_sel:[0,0] op_sel_hi:[1,0]\n"
    "v_cvt_pk_bf16_f32 v84, v80, v81\n"
    "v_cvt_pk_bf16_f32 v85, v82, v83\n"
    "ds_write2st64_b32 v226, v84, v85 offset0:40 offset1:56\n"
    "v_accvgpr_read_b32 v88, a75\n"
    "v_mfma_f32_16x16x32_bf16 a[168:171], v[48:51], v[174:177], a[168:171]\n"
    "v_accvgpr_read_b32 v89, a91\n"
    "v_accvgpr_read_b32 v90, a107\n"
    "v_accvgpr_read_b32 v91, a123\n"
    "v_pk_mul_f32 v[88:89], v[88:89], v[216:217] op_sel:[0,1] op_sel_hi:[1,1]\n"
    "v_pk_mul_f32 v[90:91], v[90:91], v[216:217] op_sel:[0,1] op_sel_hi:[1,1]\n"
    "v_mfma_f32_16x16x32_bf16 a[168:171], v[52:55], v[178:181], a[168:171]\n"
    "v_cvt_pk_bf16_f32 v92, v88, v89\n"
    "v_cvt_pk_bf16_f32 v93, v90, v91\n"
    "ds_write2st64_b32 v227, v92, v93 offset0:40 offset1:56\n"
    "ds_read_b128 v[112:115], v204 offset:10240\n"
    "ds_read_b128 v[116:119], v204 offset:10752\n"
    "v_mfma_f32_16x16x32_bf16 a[172:175], v[60:63], v[178:181], a[172:175]\n"
    "s_waitcnt lgkmcnt(7)\n"
    "buffer_store_dwordx4 v[104:107], v205, s[68:71], s42 offen offset:128 sc1 nt\n"
    "v_mfma_f32_16x16x32_bf16 v[234:237], v[104:107], v[230:233], v[234:237]\n"
    "s_waitcnt lgkmcnt(6)\n"
    "buffer_store_dwordx4 v[108:111], v205, s[68:71], s43 offen offset:128 sc1 nt\n"
    "v_mfma_f32_16x16x32_bf16 v[234:237], v[108:111], v[230:233], v[234:237]\n"
    "v_accvgpr_read_b32 v64, a76\n"
    "v_mfma_f32_16x16x32_bf16 a[172:175], v[56:59], v[174:177], a[172:175]\n"
    "v_accvgpr_read_b32 v65, a92\n"
    "v_accvgpr_read_b32 v66, a108\n"
    "v_accvgpr_read_b32 v67, a124\n"
    "v_pk_mul_f32 v[64:65], v[64:65], v[218:219] op_sel:[0,0] op_sel_hi:[1,0]\n"
    "v_pk_mul_f32 v[66:67], v[66:67], v[218:219] op_sel:[0,0] op_sel_hi:[1,0]\n"
    "v_mfma_f32_16x16x32_bf16 a[176:179], v[32:35], v[182:185], a[176:179]\n"
    "v_cvt_pk_bf16_f32 v68, v64, v65\n"
    "v_cvt_pk_bf16_f32 v69, v66, v67\n"
    "ds_write2st64_b32 v224, v68, v69 offset0:44 offset1:60\n"
    "v_accvgpr_read_b32 v72, a77\n"
    "v_accvgpr_read_b32 v73, a93\n"
    "v_mfma_f32_16x16x32_bf16 a[176:179], v[36:39], v[186:189], a[176:179]\n"
    "v_accvgpr_read_b32 v74, a109\n"
    "v_accvgpr_read_b32 v75, a125\n"
    "v_pk_mul_f32 v[72:73], v[72:73], v[218:219] op_sel:[0,1] op_sel_hi:[1,1]\n"
    "v_pk_mul_f32 v[74:75], v[74:75], v[218:219] op_sel:[0,1] op_sel_hi:[1,1]\n"
    "v_cvt_pk_bf16_f32 v76, v72, v73\n"
    "v_mfma_f32_16x16x32_bf16 a[180:183], v[44:47], v[186:189], a[180:183]\n"
    "v_cvt_pk_bf16_f32 v77, v74, v75\n"
    "ds_write2st64_b32 v225, v76, v77 offset0:44 offset1:60\n"
    "v_accvgpr_read_b32 v80, a78\n"
    "v_accvgpr_read_b32 v81, a94\n"
    "v_accvgpr_read_b32 v82, a110\n"
    "v_mfma_f32_16x16x32_bf16 a[180:183], v[40:43], v[182:185], a[180:183]\n"
    "v_accvgpr_read_b32 v83, a126\n"
    "v_pk_mul_f32 v[80:81], v[80:81], v[220:221] op_sel:[0,0] op_sel_hi:[1,0]\n"
    "v_pk_mul_f32 v[82:83], v[82:83], v[220:221] op_sel:[0,0] op_sel_hi:[1,0]\n"
    "v_cvt_pk_bf16_f32 v84, v80, v81\n"
    "v_cvt_pk_bf16_f32 v85, v82, v83\n"
    "v_mfma_f32_16x16x32_bf16 a[184:187], v[48:51], v[182:185], a[184:187]\n"
    "ds_write2st64_b32 v226, v84, v85 offset0:44 offset1:60\n"
    "v_accvgpr_read_b32 v88, a79\n"
    "v_accvgpr_read_b32 v89, a95\n"
    "v_accvgpr_read_b32 v90, a111\n"
    "v_accvgpr_read_b32 v91, a127\n"
    "v_mfma_f32_16x16x32_bf16 a[184:187], v[52:55], v[186:189], a[184:187]\n"
    "v_pk_mul_f32 v[88:89], v[88:89], v[220:221] op_sel:[0,1] op_sel_hi:[1,1]\n"
    "v_pk_mul_f32 v[90:91], v[90:91], v[220:221] op_sel:[0,1] op_sel_hi:[1,1]\n"
    "v_cvt_pk_bf16_f32 v92, v88, v89\n"
    "v_cvt_pk_bf16_f32 v93, v90, v91\n"
    "ds_write2st64_b32 v227, v92, v93 offset0:44 offset1:60\n"
    "v_mfma_f32_16x16x32_bf16 a[188:191], v[60:63], v[186:189], a[188:191]\n"
    "ds_read_b128 v[120:123], v204 offset:11264\n"
    "ds_read_b128 v[124:127], v204 offset:11776\n"
    "s_waitcnt lgkmcnt(7)\n"
    "buffer_store_dwordx4 v[112:115], v205, s[68:71], s44 offen offset:128 sc1 nt\n"
    "v_mfma_f32_16x16x32_bf16 v[234:237], v[112:115], v[230:233], v[234:237]\n"
    "s_waitcnt lgkmcnt(6)\n"
    "buffer_store_dwordx4 v[116:119], v205, s[68:71], s45 offen offset:128 sc1 nt\n"
    "v_mfma_f32_16x16x32_bf16 a[188:191], v[56:59], v[182:185], a[188:191]\n"
    "v_mfma_f32_16x16x32_bf16 v[234:237], v[116:119], v[230:233], v[234:237]\n"
    "s_waitcnt lgkmcnt(1)\n"
    "buffer_store_dwordx4 v[120:123], v205, s[68:71], s46 offen offset:128 sc1 nt\n"
    "v_mfma_f32_16x16x32_bf16 v[234:237], v[120:123], v[230:233], v[234:237]\n"
    "s_waitcnt lgkmcnt(0)\n"
    "buffer_store_dwordx4 v[124:127], v205, s[68:71], s47 offen offset:128 sc1 nt\n"
    "v_mfma_f32_16x16x32_bf16 v[234:237], v[124:127], v[230:233], v[234:237]\n"
    "v_accvgpr_read_b32 v64, a128\n"
    "v_accvgpr_read_b32 v65, a144\n"
    "v_accvgpr_read_b32 v66, a160\n"
    "v_accvgpr_read_b32 v67, a176\n"
    "v_pk_mul_f32 v[64:65], v[64:65], v[206:207] op_sel:[0,0] op_sel_hi:[1,0]\n"
    "v_pk_mul_f32 v[66:67], v[66:67], v[206:207] op_sel:[0,0] op_sel_hi:[1,0]\n"
    "v_cvt_pk_bf16_f32 v68, v64, v65\n"
    "v_cvt_pk_bf16_f32 v69, v66, v67\n"
    "ds_write2st64_b32 v224, v68, v69 offset0:64 offset1:80\n"
    "v_accvgpr_read_b32 v72, a129\n"
    "v_accvgpr_read_b32 v73, a145\n"
    "v_accvgpr_read_b32 v74, a161\n"
    "v_accvgpr_read_b32 v75, a177\n"
    "v_pk_mul_f32 v[72:73], v[72:73], v[206:207] op_sel:[0,1] op_sel_hi:[1,1]\n"
    "v_pk_mul_f32 v[74:75], v[74:75], v[206:207] op_sel:[0,1] op_sel_hi:[1,1]\n"
    "v_cvt_pk_bf16_f32 v76, v72, v73\n"
    "v_cvt_pk_bf16_f32 v77, v74, v75\n"
    "ds_write2st64_b32 v225, v76, v77 offset0:64 offset1:80\n"
    "v_accvgpr_read_b32 v80, a130\n"
    "v_accvgpr_read_b32 v81, a146\n"
    "v_accvgpr_read_b32 v82, a162\n"
    "v_accvgpr_read_b32 v83, a178\n"
    "v_pk_mul_f32 v[80:81], v[80:81], v[208:209] op_sel:[0,0] op_sel_hi:[1,0]\n"
    "v_pk_mul_f32 v[82:83], v[82:83], v[208:209] op_sel:[0,0] op_sel_hi:[1,0]\n"
    "v_cvt_pk_bf16_f32 v84, v80, v81\n"
    "v_cvt_pk_bf16_f32 v85, v82, v83\n"
    "ds_write2st64_b32 v226, v84, v85 offset0:64 offset1:80\n"
    "v_accvgpr_read_b32 v88, a131\n"
    "v_accvgpr_read_b32 v89, a147\n"
    "v_accvgpr_read_b32 v90, a163\n"
    "v_accvgpr_read_b32 v91, a179\n"
    "v_pk_mul_f32 v[88:89], v[88:89], v[208:209] op_sel:[0,1] op_sel_hi:[1,1]\n"
    "v_pk_mul_f32 v[90:91], v[90:91], v[208:209] op_sel:[0,1] op_sel_hi:[1,1]\n"
    "v_cvt_pk_bf16_f32 v92, v88, v89\n"
    "v_cvt_pk_bf16_f32 v93, v90, v91\n"
    "ds_write2st64_b32 v227, v92, v93 offset0:64 offset1:80\n"
    "ds_read_b128 v[96:99], v204 offset:16384\n"
    "ds_read_b128 v[100:103], v204 offset:16896\n"
    "v_accvgpr_read_b32 v64, a132\n"
    "v_accvgpr_read_b32 v65, a148\n"
    "v_accvgpr_read_b32 v66, a164\n"
    "v_accvgpr_read_b32 v67, a180\n"
    "v_pk_mul_f32 v[64:65], v[64:65], v[210:211] op_sel:[0,0] op_sel_hi:[1,0]\n"
    "v_pk_mul_f32 v[66:67], v[66:67], v[210:211] op_sel:[0,0] op_sel_hi:[1,0]\n"
    "v_cvt_pk_bf16_f32 v68, v64, v65\n"
    "v_cvt_pk_bf16_f32 v69, v66, v67\n"
    "ds_write2st64_b32 v224, v68, v69 offset0:68 offset1:84\n"
    "v_accvgpr_read_b32 v72, a133\n"
    "v_accvgpr_read_b32 v73, a149\n"
    "v_accvgpr_read_b32 v74, a165\n"
    "v_accvgpr_read_b32 v75, a181\n"
    "v_pk_mul_f32 v[72:73], v[72:73], v[210:211] op_sel:[0,1] op_sel_hi:[1,1]\n"
    "v_pk_mul_f32 v[74:75], v[74:75], v[210:211] op_sel:[0,1] op_sel_hi:[1,1]\n"
    "v_cvt_pk_bf16_f32 v76, v72, v73\n"
    "v_cvt_pk_bf16_f32 v77, v74, v75\n"
    "ds_write2st64_b32 v225, v76, v77 offset0:68 offset1:84\n"
    "v_accvgpr_read_b32 v80, a134\n"
    "v_accvgpr_read_b32 v81, a150\n"
    "v_accvgpr_read_b32 v82, a166\n"
    "v_accvgpr_read_b32 v83, a182\n"
    "v_pk_mul_f32 v[80:81], v[80:81], v[212:213] op_sel:[0,0] op_sel_hi:[1,0]\n"
    "v_pk_mul_f32 v[82:83], v[82:83], v[212:213] op_sel:[0,0] op_sel_hi:[1,0]\n"
    "v_cvt_pk_bf16_f32 v84, v80, v81\n"
    "v_cvt_pk_bf16_f32 v85, v82, v83\n"
    "ds_write2st64_b32 v226, v84, v85 offset0:68 offset1:84\n"
    "v_accvgpr_read_b32 v88, a135\n"
    "v_accvgpr_read_b32 v89, a151\n"
    "v_accvgpr_read_b32 v90, a167\n"
    "v_accvgpr_read_b32 v91, a183\n"
    "v_pk_mul_f32 v[88:89], v[88:89], v[212:213] op_sel:[0,1] op_sel_hi:[1,1]\n"
    "v_pk_mul_f32 v[90:91], v[90:91], v[212:213] op_sel:[0,1] op_sel_hi:[1,1]\n"
    "v_cvt_pk_bf16_f32 v92, v88, v89\n"
    "v_cvt_pk_bf16_f32 v93, v90, v91\n"
    "ds_write2st64_b32 v227, v92, v93 offset0:68 offset1:84\n"
    "ds_read_b128 v[104:107], v204 offset:17408\n"
    "ds_read_b128 v[108:111], v204 offset:17920\n"
    "s_waitcnt lgkmcnt(7)\n"
    "buffer_store_dwordx4 v[96:99], v205, s[68:71], s40 offen offset:256 sc1 nt\n"
    "v_mfma_f32_16x16x32_bf16 v[234:237], v[96:99], v[230:233], v[234:237]\n"
    "s_waitcnt lgkmcnt(6)\n"
    "buffer_store_dwordx4 v[100:103], v205, s[68:71], s41 offen offset:256 sc1 nt\n"
    "v_mfma_f32_16x16x32_bf16 v[234:237], v[100:103], v[230:233], v[234:237]\n"
    "v_accvgpr_read_b32 v64, a136\n"
    "v_accvgpr_read_b32 v65, a152\n"
    "v_accvgpr_read_b32 v66, a168\n"
    "v_accvgpr_read_b32 v67, a184\n"
    "v_pk_mul_f32 v[64:65], v[64:65], v[214:215] op_sel:[0,0] op_sel_hi:[1,0]\n"
    "v_pk_mul_f32 v[66:67], v[66:67], v[214:215] op_sel:[0,0] op_sel_hi:[1,0]\n"
    "v_cvt_pk_bf16_f32 v68, v64, v65\n"
    "v_cvt_pk_bf16_f32 v69, v66, v67\n"
    "ds_write2st64_b32 v224, v68, v69 offset0:72 offset1:88\n"
    "v_accvgpr_read_b32 v72, a137\n"
    "v_accvgpr_read_b32 v73, a153\n"
    "v_accvgpr_read_b32 v74, a169\n"
    "v_accvgpr_read_b32 v75, a185\n"
    "v_pk_mul_f32 v[72:73], v[72:73], v[214:215] op_sel:[0,1] op_sel_hi:[1,1]\n"
    "v_pk_mul_f32 v[74:75], v[74:75], v[214:215] op_sel:[0,1] op_sel_hi:[1,1]\n"
    "v_cvt_pk_bf16_f32 v76, v72, v73\n"
    "v_cvt_pk_bf16_f32 v77, v74, v75\n"
    "ds_write2st64_b32 v225, v76, v77 offset0:72 offset1:88\n"
    "v_accvgpr_read_b32 v80, a138\n"
    "v_accvgpr_read_b32 v81, a154\n"
    "v_accvgpr_read_b32 v82, a170\n"
    "v_accvgpr_read_b32 v83, a186\n"
    "v_pk_mul_f32 v[80:81], v[80:81], v[216:217] op_sel:[0,0] op_sel_hi:[1,0]\n"
    "v_pk_mul_f32 v[82:83], v[82:83], v[216:217] op_sel:[0,0] op_sel_hi:[1,0]\n"
    "v_cvt_pk_bf16_f32 v84, v80, v81\n"
    "v_cvt_pk_bf16_f32 v85, v82, v83\n"
    "ds_write2st64_b32 v226, v84, v85 offset0:72 offset1:88\n"
    "v_accvgpr_read_b32 v88, a139\n"
    "v_accvgpr_read_b32 v89, a155\n"
    "v_accvgpr_read_b32 v90, a171\n"
    "v_accvgpr_read_b32 v91, a187\n"
    "v_pk_mul_f32 v[88:89], v[88:89], v[216:217] op_sel:[0,1] op_sel_hi:[1,1]\n"
    "v_pk_mul_f32 v[90:91], v[90:91], v[216:217] op_sel:[0,1] op_sel_hi:[1,1]\n"
    "v_cvt_pk_bf16_f32 v92, v88, v89\n"
    "v_cvt_pk_bf16_f32 v93, v90, v91\n"
    "ds_write2st64_b32 v227, v92, v93 offset0:72 offset1:88\n"
    "ds_read_b128 v[112:115], v204 offset:18432\n"
    "ds_read_b128 v[116:119], v204 offset:18944\n"
    "s_waitcnt lgkmcnt(7)\n"
    "buffer_store_dwordx4 v[104:107], v205, s[68:71], s42 offen offset:256 sc1 nt\n"
    "v_mfma_f32_16x16x32_bf16 v[234:237], v[104:107], v[230:233], v[234:237]\n"
    "s_waitcnt lgkmcnt(6)\n"
    "buffer_store_dwordx4 v[108:111], v205, s[68:71], s43 offen offset:256 sc1 nt\n"
    "v_mfma_f32_16x16x32_bf16 v[234:237], v[108:111], v[230:233], v[234:237]\n"
    "v_accvgpr_read_b32 v64, a140\n"
    "v_accvgpr_read_b32 v65, a156\n"
    "v_accvgpr_read_b32 v66, a172\n"
    "v_accvgpr_read_b32 v67, a188\n"
    "v_pk_mul_f32 v[64:65], v[64:65], v[218:219] op_sel:[0,0] op_sel_hi:[1,0]\n"
    "v_pk_mul_f32 v[66:67], v[66:67], v[218:219] op_sel:[0,0] op_sel_hi:[1,0]\n"
    "v_cvt_pk_bf16_f32 v68, v64, v65\n"
    "v_cvt_pk_bf16_f32 v69, v66, v67\n"
    "ds_write2st64_b32 v224, v68, v69 offset0:76 offset1:92\n"
    "v_accvgpr_read_b32 v72, a141\n"
    "v_accvgpr_read_b32 v73, a157\n"
    "v_accvgpr_read_b32 v74, a173\n"
    "v_accvgpr_read_b32 v75, a189\n"
    "v_pk_mul_f32 v[72:73], v[72:73], v[218:219] op_sel:[0,1] op_sel_hi:[1,1]\n"
    "v_pk_mul_f32 v[74:75], v[74:75], v[218:219] op_sel:[0,1] op_sel_hi:[1,1]\n"
    "v_cvt_pk_bf16_f32 v76, v72, v73\n"
    "v_cvt_pk_bf16_f32 v77, v74, v75\n"
    "ds_write2st64_b32 v225, v76, v77 offset0:76 offset1:92\n"
    "v_accvgpr_read_b32 v80, a142\n"
    "v_accvgpr_read_b32 v81, a158\n"
    "v_accvgpr_read_b32 v82, a174\n"
    "v_accvgpr_read_b32 v83, a190\n"
    "v_pk_mul_f32 v[80:81], v[80:81], v[220:221] op_sel:[0,0] op_sel_hi:[1,0]\n"
    "v_pk_mul_f32 v[82:83], v[82:83], v[220:221] op_sel:[0,0] op_sel_hi:[1,0]\n"
    "v_cvt_pk_bf16_f32 v84, v80, v81\n"
    "v_cvt_pk_bf16_f32 v85, v82, v83\n"
    "ds_write2st64_b32 v226, v84, v85 offset0:76 offset1:92\n"
    "v_accvgpr_read_b32 v88, a143\n"
    "v_accvgpr_read_b32 v89, a159\n"
    "v_accvgpr_read_b32 v90, a175\n"
    "v_accvgpr_read_b32 v91, a191\n"
    "v_pk_mul_f32 v[88:89], v[88:89], v[220:221] op_sel:[0,1] op_sel_hi:[1,1]\n"
    "v_pk_mul_f32 v[90:91], v[90:91], v[220:221] op_sel:[0,1] op_sel_hi:[1,1]\n"
    "v_cvt_pk_bf16_f32 v92, v88, v89\n"
    "v_cvt_pk_bf16_f32 v93, v90, v91\n"
    "ds_write2st64_b32 v227, v92, v93 offset0:76 offset1:92\n"
    "ds_read_b128 v[120:123], v204 offset:19456\n"
    "ds_read_b128 v[124:127], v204 offset:19968\n"
    "s_waitcnt lgkmcnt(7)\n"
    "buffer_store_dwordx4 v[112:115], v205, s[68:71], s44 offen offset:256 sc1 nt\n"
    "v_mfma_f32_16x16x32_bf16 v[234:237], v[112:115], v[230:233], v[234:237]\n"
    "s_waitcnt lgkmcnt(6)\n"
    "buffer_store_dwordx4 v[116:119], v205, s[68:71], s45 offen offset:256 sc1 nt\n"
    "v_mfma_f32_16x16x32_bf16 v[234:237], v[116:119], v[230:233], v[234:237]\n"
    "s_waitcnt lgkmcnt(1)\n"
    "buffer_store_dwordx4 v[120:123], v205, s[68:71], s46 offen offset:256 sc1 nt\n"
    "v_mfma_f32_16x16x32_bf16 v[234:237], v[120:123], v[230:233], v[234:237]\n"
    "s_waitcnt lgkmcnt(0)\n"
    "buffer_store_dwordx4 v[124:127], v205, s[68:71], s47 offen offset:256 sc1 nt\n"
    "v_mfma_f32_16x16x32_bf16 v[234:237], v[124:127], v[230:233], v[234:237]\n"
    "s_nop 7\n"
    "v_add_f32 v234, v234, v235\n"
    "v_add_f32 v236, v236, v237\n"
    "v_add_f32 v234, v234, v236\n"
    "v_cmp_neq_f32 vcc, 0, v234\n"
    "s_nop 3\n"
    "s_or_b64 s[82:83], s[82:83], vcc\n"
    "s_cmp_lg_u64 s[82:83], 0\n"
    "s_cselect_b32 %0, 1, 0\n"
    "s_or_b32 s88, %12, %11\n"
    "s_cmp_lg_u32 s88, 0\n"
    "s_cbranch_scc1 NOEP%=\n"
    "v_mov_b32 v252, s80\n"
    "v_mov_b32 v253, s81\n"
    "buffer_store_dwordx2 v[252:253], off, s[64:67], 0 offset:3072 sc1\n"
    "NOEP%=:\n"
    "s_waitcnt vmcnt(0)\n"
    "s_branch END%=\n"
    "SPP%=:\n"
    "s_cmp_eq_u32 s95, 0\n"
    "s_cbranch_scc1 RTP%=\n"
    "s_sub_u32 s95, s95, 1\n"
    "s_sleep 2\n"
    "buffer_load_dwordx2 v[252:253], v250, s[64:67], 0 offen offset:2304 sc1\n"
    "s_waitcnt vmcnt(0)\n"
    "v_cmp_ne_u64 vcc, s[80:81], v[252:253]\n"
    "s_nop 4\n"
    "s_nop 4\n"
    "s_cbranch_vccnz SPP%=\n"
    "s_branch RTP%=\n"
    "SPK%=:\n"
    "s_cmp_eq_u32 s95, 0\n"
    "s_cbranch_scc1 RTK%=\n"
    "s_sub_u32 s95, s95, 1\n"
    "s_sleep 2\n"
    "buffer_load_dwordx2 v[252:253], v250, s[64:67], s87 offen sc1\n"
    "s_waitcnt vmcnt(0)\n"
    "v_cmp_ne_u64 vcc, s[80:81], v[252:253]\n"
    "s_nop 4\n"
    "s_nop 4\n"
    "s_cbranch_vccnz SPK%=\n"
    "s_branch RTK%=\n"
    "SPV%=:\n"
    "s_cmp_eq_u32 s95, 0\n"
    "s_cbranch_scc1 RTV%=\n"
    "s_sub_u32 s95, s95, 1\n"
    "s_sleep 2\n"
    "buffer_load_dwordx2 v[252:253], v250, s[64:67], s87 offen sc1\n"
    "s_waitcnt vmcnt(0)\n"
    "v_cmp_ne_u64 vcc, s[80:81], v[252:253]\n"
    "s_nop 4\n"
    "s_nop 4\n"
    "s_cbranch_vccnz SPV%=\n"
    "s_branch RTV%=\n"
    "SPA%=:\n"
    "s_cmp_eq_u32 s95, 0\n"
    "s_cbranch_scc1 RTA%=\n"
    "s_sub_u32 s95, s95, 1\n"
    "s_sleep 2\n"
    "buffer_load_dwordx2 v[252:253], v250, s[64:67], s87 offen sc1\n"
    "s_waitcnt vmcnt(0)\n"
    "v_cmp_ne_u64 vcc, s[80:81], v[252:253]\n"
    "s_nop 4\n"
    "s_nop 4\n"
    "s_cbranch_vccnz SPA%=\n"
    "s_branch RTA%=\n"
    "SPBK%=:\n"
    "s_cmp_eq_u32 s95, 0\n"
    "s_cbranch_scc1 RTBK%=\n"
    "s_sub_u32 s95, s95, 1\n"
    "s_sleep 2\n"
    "buffer_load_dwordx2 v[252:253], v250, s[64:67], s87 offen sc1\n"
    "s_waitcnt vmcnt(0)\n"
    "v_cmp_ne_u64 vcc, s[80:81], v[252:253]\n"
    "s_nop 4\n"
    "s_nop 4\n"
    "s_cbranch_vccnz SPBK%=\n"
    "s_branch RTBK%=\n"
    "SPCK%=:\n"
    "s_cmp_eq_u32 s95, 0\n"
    "s_cbranch_scc1 RTCK%=\n"
    "s_sub_u32 s95, s95, 1\n"
    "s_sleep 2\n"
    "buffer_load_dwordx2 v[252:253], v250, s[64:67], s87 offen sc1\n"
    "s_waitcnt vmcnt(0)\n"
    "v_cmp_ne_u64 vcc, s[80:81], v[252:253]\n"
    "s_nop 4\n"
    "s_nop 4\n"
    "s_cbranch_vccnz SPCK%=\n"
    "s_branch RTCK%=\n"
    "SPBV%=:\n"
    "s_cmp_eq_u32 s95, 0\n"
    "s_cbranch_scc1 RTBV%=\n"
    "s_sub_u32 s95, s95, 1\n"
    "s_sleep 2\n"
    "buffer_load_dwordx2 v[252:253], v250, s[64:67], s87 offen sc1\n"
    "s_waitcnt vmcnt(0)\n"
    "v_cmp_ne_u64 vcc, s[80:81], v[252:253]\n"
    "s_nop 4\n"
    "s_nop 4\n"
    "s_cbranch_vccnz SPBV%=\n"
    "s_branch RTBV%=\n"
    "SPCV%=:\n"
    "s_cmp_eq_u32 s95, 0\n"
    "s_cbranch_scc1 RTCV%=\n"
    "s_sub_u32 s95, s95, 1\n"
    "s_sleep 2\n"
    "buffer_load_dwordx2 v[252:253], v250, s[64:67], s87 offen sc1\n"
    "s_waitcnt vmcnt(0)\n"
    "v_cmp_ne_u64 vcc, s[80:81], v[252:253]\n"
    "s_nop 4\n"
    "s_nop 4\n"
    "s_cbranch_vccnz SPCV%=\n"
    "s_branch RTCV%=\n"
    "END%=:\n"
    : "=&s"(bad)
    : "s"(qp), "s"(QK + (size_t)h * (S * D * 4)), "s"(QV + (size_t)h * (S * D * 4)), "s"(KZ + (size_t)h * (64 * D * 2)), "s"(KS + (size_t)h * (64 * D * 2)),
      "s"(VZ + (size_t)h * (S * 2)), "s"(VS + (size_t)h * (S * 2)), "s"(kw), "s"(WS + 2 * KV_BYTES + (size_t)h * 4096), "s"(op), "s"(w), "s"(j), "s"((b >> 3) * 4 + w)
    : "v0","v1","v2","v3","v4","v5","v6","v7","v8","v9","v10","v11","v12","v13","v14","v15","v16","v17","v18","v19","v20","v21","v22","v23","v24","v25","v26","v27","v28","v29","v30","v31","v32","v33","v34","v35","v36","v37","v38","v39","v40","v41","v42","v43","v44","v45","v46","v47","v48","v49","v50","v51","v52","v53","v54","v55","v56","v57","v58","v59","v60","v61","v62","v63","v64","v65","v66","v67","v68","v69","v70","v71","v72","v73","v74","v75","v76","v77","v78","v79","v80","v81","v82","v83","v84","v85","v86","v87","v88","v89","v90","v91","v92","v93","v94","v95","v96","v97","v98","v99","v100","v101","v102","v103","v104","v105","v106","v107","v108","v109","v110","v111","v112","v113","v114","v115","v116","v117","v118","v119","v120","v121","v122","v123","v124","v125","v126","v127","v128","v129","v130","v131","v132","v133","v134","v135","v136","v137","v138","v139","v140","v141","v142","v143","v144","v145","v146","v147","v148","v149","v150","v151","v152","v153","v154","v155","v156","v157","v158","v159","v160","v161","v162","v163","v164","v165","v166","v167","v168","v169","v170","v171","v172","v173","v174","v175","v176","v177","v178","v179","v180","v181","v182","v183","v184","v185","v186","v187","v188","v189","v190","v191","v192","v193","v194","v195","v196","v197","v198","v199","v200","v201","v202","v203","v204","v205","v206","v207","v208","v209","v210","v211","v212","v213","v214","v215","v216","v217","v218","v219","v220","v221","v222","v223","v224","v225","v226","v227","v228","v229","v230","v231","v232","v233","v234","v235","v236","v237","v238","v239","v240","v241","v242","v243","v244","v245","v246","v247","v248","v249","v250","v251","v252","v253","v254","v255","a0","a1","a2","a3","a4","a5","a6","a7","a8","a9","a10","a11","a12","a13","a14","a15","a16","a17","a18","a19","a20","a21","a22","a23","a24","a25","a26","a27","a28","a29","a30","a31","a32","a33","a34","a35","a36","a37","a38","a39","a40","a41","a42","a43","a44","a45","a46","a47","a48","a49","a50","a51","a52","a53","a54","a55","a56","a57","a58","a59","a60","a61","a62","a63","a64","a65","a66","a67","a68","a69","a70","a71","a72","a73","a74","a75","a76","a77","a78","a79","a80","a81","a82","a83","a84","a85","a86","a87","a88","a89","a90","a91","a92","a93","a94","a95","a96","a97","a98","a99","a100","a101","a102","a103","a104","a105","a106","a107","a108","a109","a110","a111","a112","a113","a114","a115","a116","a117","a118","a119","a120","a121","a122","a123","a124","a125","a126","a127","a128","a129","a130","a131","a132","a133","a134","a135","a136","a137","a138","a139","a140","a141","a142","a143","a144","a145","a146","a147","a148","a149","a150","a151","a152","a153","a154","a155","a156","a157","a158","a159","a160","a161","a162","a163","a164","a165","a166","a167","a168","a169","a170","a171","a172","a173","a174","a175","a176","a177","a178","a179","a180","a181","a182","a183","a184","a185","a186","a187","a188","a189","a190","a191","a192","a193","a194","a195","a196","a197","a198","a199","a200","a201","a202","a203","a204","a205","a206","a207","a208","a209","a210","a211","a212","a213","a214","a215","a216","a217","a218","a219","a220","a221","a222","a223","a224","a225","a226","a227","a228","a229","a230","a231","a232","a233","a234","a235","a236","a237","a238","a239","a240","a241","a242","a243","a244","a245","a246","a247","a248","a249","a250","a251","a252","a253","a254","a255","s28","s29","s30","s31","s36","s37","s38","s39","s40","s41","s42","s43","s44","s45","s46","s47","s48","s49","s50","s51","s52","s53","s54","s55","s56","s57","s58","s59","s60","s61","s62","s63","s64","s65","s66","s67","s68","s69","s70","s71","s72","s73","s74","s75","s76","s77","s78","s79","s80","s81","s82","s83","s84","s85","s86","s87","s88","s89","s90","s91","s92","s93","s94","s95","s96","s97","s98","s99","scc","vcc","memory");
  if (bad) {
    __builtin_amdgcn_fence(__ATOMIC_ACQUIRE, "agent");
    slow_rows(qp, kw, vw, op);
  }
}

extern "C" void kernel_launch(void* const* d_in, const int* in_sizes, int n_in, void* d_out, int out_size, void* d_ws, size_t ws_size, hipStream_t stream) {
  kvq_attn<<<256, 256, 0, stream>>>((const char*)d_in[0], (const char*)d_in[1], (const char*)d_in[2], (const char*)d_in[3], (const char*)d_in[4],
                                    (const char*)d_in[5], (const char*)d_in[6], (char*)d_ws, (char*)d_out);
}
```
